```python
import torch

def _softmax(x, axis=-1):
    return torch.softmax(x, dim=axis)

def _gelu(x):
    return torch.nn.functional.gelu(x, approximate='tanh')

def _silu(x):
    return torch.nn.functional.silu(x)

def _sigmoid(x):
    return torch.sigmoid(x)

def _layernorm(x, axis=-1, eps=1e-05):
    m = x.mean(dim=axis, keepdim=True)
    v = x.var(dim=axis, keepdim=True, unbiased=False)
    return (x - m) / torch.sqrt(v + eps)

def _scatter_add(tgt, idx, src, axis=0):
    return tgt.clone().index_add_(axis, idx, src)

def _segment_sum(src, seg, n):
    out = torch.zeros((n,) + src.shape[1:], dtype=src.dtype)
    return out.index_add_(0, seg, src)

def _pad(x, pw):
    flat = tuple((v for pair in reversed(pw) for v in pair))
    return torch.nn.functional.pad(x, flat)
kernel_name = 'adaLN_cond__sliding_block_cached__dense_activation_relu__decode'

def setup_inputs():

    _w = lambda a: (lambda t: t.to(torch.bfloat16) if t.is_floating_point() else t)(torch.as_tensor(a))

    def _A_setup():
        B, Sq, D = (1, 1, 640)
        Hq, Hkv, Sk, Dh = (32, 32, 16384, 128)
        assert Hq % Hkv == 0, (Hq, Hkv)
        return {'x': _w((lambda *s: torch.randn(*s, dtype=torch.bfloat16) * 0.05)(B, Sq, D)), 'k_cache': _w((lambda *s: torch.randn(*s, dtype=torch.bfloat16) * 3.2)(B, Hkv, Sk, Dh)), 'v_cache': _w((lambda *s: torch.randn(*s, dtype=torch.bfloat16) * 0.4)(B, Hkv, Sk, Dh)), 'wq': _w((lambda *s: torch.randn(*s, dtype=torch.bfloat16) * 3.2)(D, Hq * Dh) * D ** (-0.5)), 'wo': _w((lambda *s: torch.randn(*s, dtype=torch.bfloat16) * 0.4)(Hq * Dh, D) * (Hq * Dh) ** (-0.5))}
    _A_in = _A_setup()

    def _B_setup():
        B, S, D, Dff = (1, 1, 640, 1728)
        return {'x': _w((lambda *s: torch.randn(*s, dtype=torch.bfloat16) * 0.05)(B, S, D)), 'w1': _w((lambda *s: torch.randn(*s, dtype=torch.bfloat16) * 0.05)(D, Dff)), 'w2': _w((lambda *s: torch.randn(*s, dtype=torch.bfloat16) * 0.05)(Dff, D))}
    _B_in = _B_setup()
    return {'A_x': _A_in['x'], 'A_k_cache': _A_in['k_cache'], 'A_v_cache': _A_in['v_cache'], 'A_wq': _A_in['wq'], 'A_wo': _A_in['wo'], 'B_x': _B_in['x'], 'B_w1': _B_in['w1'], 'B_w2': _B_in['w2'], 'c': _w((lambda *s: torch.randn(*s, dtype=torch.bfloat16) * 0.1)(1, 640) * 8.0), 'w_mod': _w((lambda *s: torch.randn(*s, dtype=torch.bfloat16) * 0.1)(640, 6 * 640) * 640 ** (-0.5) * 8.0)}

def reference(A_x, A_k_cache, A_v_cache, A_wq, A_wo, B_x, B_w1, B_w2, c, w_mod):

    def _A_ref(x, k_cache, v_cache, wq, wo):
        B, Sq, D = x.shape
        Hq, Dh = (32, 128)
        Hkv, Sk = (k_cache.shape[1], k_cache.shape[2])
        W = min(4096, max(256, Sk // 4))
        rep = Hq // Hkv
        q = (lambda x, *d: x.permute(*d))((x @ wq).reshape(B, Sq, Hq, Dh), 0, 2, 1, 3)
        k = (lambda x, n, axis: torch.repeat_interleave(x, n, dim=axis))(k_cache[:, :, Sk - W:, :], rep, axis=1)
        v = (lambda x, n, axis: torch.repeat_interleave(x, n, dim=axis))(v_cache[:, :, Sk - W:, :], rep, axis=1)
        scores = torch.einsum('bhqd,bhkd->bhqk', q, k) * Dh ** (-0.5)
        p = _softmax(scores, axis=-1)
        o = torch.einsum('bhqk,bhkd->bhqd', p, v)
        return (lambda x, *d: x.permute(*d))(o, 0, 2, 1, 3).reshape(B, Sq, Hq * Dh) @ wo

    def _B_ref(x, w1, w2):
        h = torch.einsum('bsd,df->bsf', x, w1)
        zero = torch.zeros_like(h)
        h = torch.maximum(h, zero)
        return torch.einsum('bsf,fd->bsd', h, w2)
    x = A_x
    mod = (_silu(c) @ w_mod).reshape(c.shape[0], 6, 1, -1)
    sa, ca, ga, sb, cb, gb = (mod[:, 0].clone(), mod[:, 1].clone(), mod[:, 2].clone(), mod[:, 3].clone(), mod[:, 4].clone(), mod[:, 5].clone())
    h = _layernorm(x, axis=-1) * (1 + ca) + sa
    x = x + ga * _A_ref(h, A_k_cache, A_v_cache, A_wq, A_wo)
    h = _layernorm(x, axis=-1) * (1 + cb) + sb
    x = x + gb * _B_ref(h, B_w1, B_w2)
    return x
```

```cpp
#include <hip/hip_runtime.h>
#include <hip/hip_bf16.h>
#include <stdint.h>

#define MIMIC 1

typedef unsigned short u16;
typedef unsigned u32x4 __attribute__((ext_vector_type(4)));

__device__ __forceinline__ float bf2f(u16 b) { return __builtin_bit_cast(float, (unsigned)b << 16); }
__device__ __forceinline__ float bflo(unsigned w) { return __builtin_bit_cast(float, w << 16); }
__device__ __forceinline__ float bfhi(unsigned w) { return __builtin_bit_cast(float, w & 0xffff0000u); }
__device__ __forceinline__ u16 f2bf(float f) { return __builtin_bit_cast(u16, (__bf16)f); }
__device__ __forceinline__ float rbf(float f) {
  return bf2f(f2bf(f));
}

constexpr int D = 640, HQ = 32, SK = 16384, DH = 128, W = 4096, DFF = 1728;

template <int K, int N>
__device__ __forceinline__ void gemv64(const float* __restrict__ sin, const u16* __restrict__ Wt, int col0, float* sred, float* out64) {
  const int t = threadIdx.x;
  const int cg = t & 7, rg = t >> 3;
  float acc[8] = {0, 0, 0, 0, 0, 0, 0, 0};
  for (int k = rg; k < K; k += 32) {
    u32x4 w = *reinterpret_cast<const u32x4*>(Wt + (size_t)k * N + col0 + cg * 8);
    float x = sin[k];
#pragma unroll
    for (int j = 0; j < 4; ++j) { acc[2 * j] += x * bflo(w[j]); acc[2 * j + 1] += x * bfhi(w[j]); }
  }
#pragma unroll
  for (int j = 0; j < 8; ++j) sred[rg * 64 + cg * 8 + j] = acc[j];
  __syncthreads();
  if (t < 64) {
    float s = 0.f;
    for (int r = 0; r < 32; ++r) s += sred[r * 64 + t];
    out64[t] = s;
  }
  __syncthreads();
}

__device__ __forceinline__ float block_sum(float v, float* sred) {
  for (int o = 32; o > 0; o >>= 1) v += __shfl_xor(v, o);
  __syncthreads();
  if ((threadIdx.x & 63) == 0) sred[threadIdx.x >> 6] = v;
  __syncthreads();
  float s = 0.f;
  for (int i = 0; i < (int)(blockDim.x >> 6); ++i) s += sred[i];
  __syncthreads();
  return s;
}

__global__ void __launch_bounds__(256) k_mod(const u16* c, const u16* wmod, float* mod) {
  __shared__ float sin[D];
  __shared__ float sred[32 * 64];
  __shared__ float o64[64];
  for (int i = threadIdx.x; i < D; i += 256) { float v = bf2f(c[i]); sin[i] = rbf(v / (1.f + __expf(-v))); }
  __syncthreads();
  gemv64<D, 6 * D>(sin, wmod, blockIdx.x * 64, sred, o64);
  if (threadIdx.x < 64) mod[blockIdx.x * 64 + threadIdx.x] = rbf(o64[threadIdx.x]);
}

__device__ __forceinline__ void ln_mod(const float* sx, const float* shift, const float* scale, float* sout, float* sred) {
  float s = 0.f;
  for (int i = threadIdx.x; i < D; i += blockDim.x) s += sx[i];
  float mean = block_sum(s, sred) / D;
  float v = 0.f;
  for (int i = threadIdx.x; i < D; i += blockDim.x) { float d = sx[i] - mean; v += d * d; }
  float var = block_sum(v, sred) / D;
  float m_r = rbf(mean), var_r = rbf(var);
  float den = rbf(sqrtf(rbf(var_r + 1e-5f)));
  for (int i = threadIdx.x; i < D; i += blockDim.x) {
    float n = rbf(rbf(sx[i] - m_r) / den);
    sout[i] = rbf(rbf(n * rbf(1.f + scale[i])) + shift[i]);
  }
  __syncthreads();
}

__global__ void __launch_bounds__(256) k_q(const u16* x, const float* mod, const u16* wq, float* q) {
  __shared__ float sx[D], sh[D];
  __shared__ float sred[32 * 64];
  __shared__ float o64[64];
  for (int i = threadIdx.x; i < D; i += 256) sx[i] = bf2f(x[i]);
  __syncthreads();
  ln_mod(sx, mod, mod + D, sh, sred);
  gemv64<D, HQ * DH>(sh, wq, blockIdx.x * 64, sred, o64);
  if (threadIdx.x < 64) q[blockIdx.x * 64 + threadIdx.x] = rbf(o64[threadIdx.x]);
}

constexpr int NSPLIT = 16, KPS = W / NSPLIT;
__global__ void __launch_bounds__(256) k_attn(const float* q, const u16* kc, const u16* vc, float* pml, float* po) {
  __shared__ float ss[KPS];
  __shared__ float sred[16 * 128];
  __shared__ float sw[8];
  const int s = blockIdx.x, h = blockIdx.y, t = threadIdx.x;
  const int c = t & 15, r = t >> 4;
  const u16* kb = kc + ((size_t)h * SK + (SK - W) + s * KPS) * DH;
  const u16* vb = vc + ((size_t)h * SK + (SK - W) + s * KPS) * DH;
  float qv[8];
#pragma unroll
  for (int j = 0; j < 8; ++j) qv[j] = q[h * DH + c * 8 + j];
  u32x4 kr[16], vr[16];
#pragma unroll
  for (int i = 0; i < 16; ++i) kr[i] = __builtin_nontemporal_load(reinterpret_cast<const u32x4*>(kb + (size_t)(i * 16 + r) * DH + c * 8));
#pragma unroll
  for (int i = 0; i < 16; ++i) vr[i] = __builtin_nontemporal_load(reinterpret_cast<const u32x4*>(vb + (size_t)(i * 16 + r) * DH + c * 8));
#pragma unroll
  for (int i = 0; i < 16; ++i) {
    float d = 0.f;
#pragma unroll
    for (int j = 0; j < 4; ++j) { d += qv[2 * j] * bflo(kr[i][j]) + qv[2 * j + 1] * bfhi(kr[i][j]); }
    d += __shfl_xor(d, 1); d += __shfl_xor(d, 2); d += __shfl_xor(d, 4); d += __shfl_xor(d, 8);
    if (c == 0) ss[i * 16 + r] = rbf(rbf(d) * 0.08838834764831845f);
  }
  __syncthreads();
  float sc = ss[t];
  float m = sc;
  for (int o = 32; o > 0; o >>= 1) m = fmaxf(m, __shfl_xor(m, o));
  if ((t & 63) == 0) sw[t >> 6] = m;
  __syncthreads();
  m = fmaxf(fmaxf(sw[0], sw[1]), fmaxf(sw[2], sw[3]));
  float p = __expf(sc - m);
  __syncthreads();
  ss[t] = p;
  float l = p;
  for (int o = 32; o > 0; o >>= 1) l += __shfl_xor(l, o);
  if ((t & 63) == 0) sw[4 + (t >> 6)] = l;
  __syncthreads();
  l = sw[4] + sw[5] + sw[6] + sw[7];
  float acc[8] = {0, 0, 0, 0, 0, 0, 0, 0};
#pragma unroll
  for (int i = 0; i < 16; ++i) {
    float pp = ss[i * 16 + r];
#pragma unroll
    for (int j = 0; j < 4; ++j) { acc[2 * j] += pp * bflo(vr[i][j]); acc[2 * j + 1] += pp * bfhi(vr[i][j]); }
  }
#pragma unroll
  for (int j = 0; j < 8; ++j) sred[r * 128 + c * 8 + j] = acc[j];
  __syncthreads();
  if (t < 128) {
    float o = 0.f;
    for (int rr = 0; rr < 16; ++rr) o += sred[rr * 128 + t];
    po[((size_t)h * NSPLIT + s) * DH + t] = o;
  }
  if (t == 0) { pml[(h * NSPLIT + s) * 2] = m; pml[(h * NSPLIT + s) * 2 + 1] = l; }
}

__global__ void __launch_bounds__(256) k_wo(const float* pml, const float* po, const u16* wo, float* part) {
  __shared__ float so[DH];
  __shared__ float sred[32 * 64];
  __shared__ float o64[64];
  const int h = blockIdx.y, t = threadIdx.x;
  if (t < DH) {
    float M = -INFINITY;
    for (int s = 0; s < NSPLIT; ++s) M = fmaxf(M, pml[(h * NSPLIT + s) * 2]);
    float L = 0.f, o = 0.f;
    for (int s = 0; s < NSPLIT; ++s) {
      float w = __expf(pml[(h * NSPLIT + s) * 2] - M);
      L += w * pml[(h * NSPLIT + s) * 2 + 1];
      o += w * po[((size_t)h * NSPLIT + s) * DH + t];
    }
    so[t] = rbf(o / L);
  }
  __syncthreads();
  gemv64<DH, D>(so, wo + (size_t)h * DH * D, blockIdx.x * 64, sred, o64);
  if (t < 64) part[h * D + blockIdx.x * 64 + t] = o64[t];
}

__global__ void __launch_bounds__(256) k_w1(const u16* x, const float* mod, const float* part, const u16* w1, float* hid, float* x1out) {
  __shared__ float sx[D], sh[D];
  __shared__ float sred[32 * 64];
  __shared__ float o64[64];
  for (int i = threadIdx.x; i < D; i += 256) {
    float a = 0.f;
    for (int h = 0; h < HQ; ++h) a += part[h * D + i];
    float v = rbf(bf2f(x[i]) + rbf(mod[2 * D + i] * rbf(a)));
    sx[i] = v;
    if (blockIdx.x == 0) x1out[i] = v;
  }
  __syncthreads();
  ln_mod(sx, mod + 3 * D, mod + 4 * D, sh, sred);
  gemv64<D, DFF>(sh, w1, blockIdx.x * 64, sred, o64);
  if (threadIdx.x < 64) hid[blockIdx.x * 64 + threadIdx.x] = fmaxf(rbf(o64[threadIdx.x]), 0.f);
}

__global__ void __launch_bounds__(256) k_w2(const float* hid, const float* x1, const float* mod, const u16* w2, u16* out) {
  __shared__ float sh[DFF];
  __shared__ float sred[32 * 64];
  __shared__ float o64[64];
  for (int i = threadIdx.x; i < DFF; i += 256) sh[i] = hid[i];
  __syncthreads();
  gemv64<DFF, D>(sh, w2, blockIdx.x * 64, sred, o64);
  if (threadIdx.x < 64) {
    int i = blockIdx.x * 64 + threadIdx.x;
    out[i] = f2bf(x1[i] + rbf(mod[5 * D + i] * rbf(o64[threadIdx.x])));
  }
}

extern "C" void kernel_launch(void* const* d_in, const int* in_sizes, int n_in,
                              void* d_out, int out_size, void* d_ws, size_t ws_size,
                              hipStream_t stream) {
  const u16* x = (const u16*)d_in[0];
  const u16* kc = (const u16*)d_in[1];
  const u16* vc = (const u16*)d_in[2];
  const u16* wq = (const u16*)d_in[3];
  const u16* wo = (const u16*)d_in[4];
  const u16* w1 = (const u16*)d_in[6];
  const u16* w2 = (const u16*)d_in[7];
  const u16* c = (const u16*)d_in[8];
  const u16* wmod = (const u16*)d_in[9];
  float* ws = (float*)d_ws;
  float* mod = ws;
  float* q = ws + 4096;
  float* pml = ws + 8192;
  float* po = ws + 16384;
  float* part = ws + 16384 + 65536;
  float* hid = part + 32 * 640;
  float* x1 = hid + 2048;
  k_mod<<<60, 256, 0, stream>>>(c, wmod, mod);
  k_q<<<64, 256, 0, stream>>>(x, mod, wq, q);
  k_attn<<<dim3(NSPLIT, HQ), 256, 0, stream>>>(q, kc, vc, pml, po);
  k_wo<<<dim3(10, HQ), 256, 0, stream>>>(pml, po, wo, part);
  k_w1<<<27, 256, 0, stream>>>(x, mod, part, w1, hid, x1);
  k_w2<<<10, 256, 0, stream>>>(hid, x1, mod, w2, (u16*)d_out);
}
```

```cpp
#include <hip/hip_runtime.h>
#include <hip/hip_bf16.h>
#include <stdint.h>

#define STAMP 0
#define PSLEEP 16
#define I1 12
#define I2 21
#define I3 17
#define IT 31
#define IS 31
#define ITK 24
#define PREB 1
#define IB 25
#define TA_M2 0
#define TA_WO 2
#define TA_FFN 2
#define TDEPF 40
#define TSTART(m) do { if ((m) == 2) lds_wait(L, C_PV, 9u); else if ((m) == 1) lds_wait8(L, IT); } while (0)
#define MP10 1
#define T1 400
#define T3 560
#define T2 740
#define TM2 150
#define DEPA 1
#define ISW 14
#define WQE 1
#define DEP 2
#define TDEP 40

#define GAS __attribute__((address_space(1)))
#define LAS __attribute__((address_space(3)))
#define RLX_AGENT __ATOMIC_RELAXED, __HIP_MEMORY_SCOPE_AGENT
typedef unsigned short u16;
typedef unsigned long long u64;
typedef unsigned u32x4 __attribute__((ext_vector_type(4)));
typedef unsigned u32x2 __attribute__((ext_vector_type(2)));
typedef float f32x4 __attribute__((ext_vector_type(4)));
typedef float f32x2 __attribute__((ext_vector_type(2)));
typedef __bf16 bf16x2 __attribute__((ext_vector_type(2)));
typedef GAS u64 gu64;
typedef __amdgpu_buffer_rsrc_t rsrc_t;

constexpr int D = 640, HQ = 32, SK = 16384, DH = 128, WIN = 4096, DFF = 1728;
constexpr int NF = 54;

constexpr int WS_EPOCH = 0;
constexpr int WS_FLAGS = 4096;
constexpr int WS_TICKET = 2048;
constexpr int NMP = 4;
constexpr int WS_MP = 8192;
constexpr int WS_QP = 131072;
constexpr int WS_OP = WS_QP + 131072;
constexpr int WS_ST = WS_OP + 131072;
constexpr int WS_YP = WS_ST + 32768;
constexpr int WS_M2 = WS_YP + 20480;
constexpr int WS_ZP = WS_M2 + 10240;
constexpr int WS_END = WS_ZP + NF * 2560;
constexpr int N_M2 = 0, N_WO = 80;
constexpr int WS_STAMP = 1 << 20;

constexpr unsigned SENT = 0xFFFFFFFFu;
constexpr unsigned SPIN_G = 1u << 18;
constexpr unsigned SPIN_L = 1u << 22;

constexpr int C_PROG = 0;
constexpr int C_MOD1 = 8, C_QP = 9, C_QREADY = 10, C_M = 11, C_ROLE = 12, C_PV = 13, C_FLAGOK = 14;
constexpr int L_P = 128;
constexpr int L_MR = L_P + 1024;
constexpr int L_QR = L_MR + 1024;
constexpr int L_OR = L_QR + 2048;
constexpr int L_OB = L_OR + 4096;
constexpr int L_YR = L_OB + 1024;
constexpr int L_STAT = L_YR + 1024;
constexpr int L_Q = L_STAT + 64;
constexpr int L_X1 = L_Q + 256;
constexpr int L_ZR = L_X1 + 2560;
constexpr int L_END = L_ZR + 10240;
constexpr int LDS_BYTES = 96 * 1024;
static_assert(L_END <= LDS_BYTES, "lds");

__device__ __forceinline__ float bflo(unsigned w) { return __builtin_bit_cast(float, w << 16); }
__device__ __forceinline__ float bfhi(unsigned w) { return __builtin_bit_cast(float, w & 0xffff0000u); }
__device__ __forceinline__ unsigned f2bf(float f) { return (unsigned)__builtin_bit_cast(u16, (__bf16)f); }
__device__ __forceinline__ float rbf(float f) { return bflo(f2bf(f)); }
__device__ __forceinline__ unsigned pack2(float lo, float hi) {
  bf16x2 v = {(__bf16)lo, (__bf16)hi};
  return __builtin_bit_cast(unsigned, v);
}
__device__ __forceinline__ float dot2(unsigned a, unsigned b, float acc) {
  return __builtin_amdgcn_fdot2_f32_bf16(__builtin_bit_cast(bf16x2, a), __builtin_bit_cast(bf16x2, b), acc, false);
}
__device__ __forceinline__ unsigned fb(float f) { return __builtin_bit_cast(unsigned, f); }
__device__ __forceinline__ float bf(unsigned u) { return __builtin_bit_cast(float, u); }
__device__ __forceinline__ unsigned nosent(float f) { return min(fb(f), 0xFFFFFFFEu); }
__device__ __forceinline__ u32x4 nosent4(f32x4 s) { return u32x4{nosent(s[0]), nosent(s[1]), nosent(s[2]), nosent(s[3])}; }
template <int CTRL> __device__ __forceinline__ float dpp(float x) {
  return __builtin_bit_cast(float, __builtin_amdgcn_mov_dpp(__builtin_bit_cast(int, x), CTRL, 0xf, 0xf, true));
}
constexpr int XOR1 = 0xB1, XOR2 = 0x4E, XOR7 = 0x141, XOR8 = 0x128;
__device__ __forceinline__ float xor4(float x) {
  int v = __builtin_bit_cast(int, x);
  int r = __builtin_amdgcn_update_dpp(0, v, 0x114, 0xf, 0xa, false);
  r = __builtin_amdgcn_update_dpp(r, v, 0x104, 0xf, 0x5, false);
  return __builtin_bit_cast(float, r);
}
__device__ __forceinline__ float tr16(float a, float b) {
  auto s = __builtin_amdgcn_permlane16_swap(fb(a), fb(b), false, false);
  return bf(s[0]) + bf(s[1]);
}
__device__ __forceinline__ float tr32(float a, float b) {
  auto s = __builtin_amdgcn_permlane32_swap(fb(a), fb(b), false, false);
  return bf(s[0]) + bf(s[1]);
}
__device__ __forceinline__ float mx16(float a) {
  auto s = __builtin_amdgcn_permlane16_swap(fb(a), fb(a), false, false);
  return fmaxf(bf(s[0]), bf(s[1]));
}
__device__ __forceinline__ float mx32(float a) {
  auto s = __builtin_amdgcn_permlane32_swap(fb(a), fb(a), false, false);
  return fmaxf(bf(s[0]), bf(s[1]));
}
__device__ __forceinline__ float row_sum(float x) {
  x += dpp<XOR1>(x); x += dpp<XOR2>(x); x += dpp<XOR7>(x); x += dpp<XOR8>(x); return x;
}
__device__ __forceinline__ float row_max(float x) {
  x = fmaxf(x, dpp<XOR1>(x)); x = fmaxf(x, dpp<XOR2>(x)); x = fmaxf(x, dpp<XOR7>(x)); x = fmaxf(x, dpp<XOR8>(x)); return x;
}
__device__ __forceinline__ float wave_sum(float x) { x = row_sum(x); x = tr16(x, x); return tr32(x, x); }
__device__ __forceinline__ float wave_max(float x) { x = row_max(x); x = mx16(x); return mx32(x); }

__device__ __forceinline__ unsigned bij32(unsigned x) {
  x += 0x9E3779B9u; x ^= x >> 16; x *= 0x85EBCA6Bu; x ^= x >> 13; x *= 0xC2B2AE35u; x ^= x >> 16; return x;
}
__device__ __forceinline__ rsrc_t mkrs(const void* p, int bytes) { return __builtin_amdgcn_make_buffer_rsrc((void*)p, 0, bytes, 0x00020000); }
__device__ __forceinline__ u32x4 ldnt(const void* p) { return __builtin_nontemporal_load(reinterpret_cast<const u32x4*>(p)); }
__device__ __forceinline__ u32x4 ld_sc1(rsrc_t rs, int off) { return __builtin_amdgcn_raw_buffer_load_b128(rs, off, 0, 16); }
__device__ __forceinline__ u32x2 ld_sc1_64(rsrc_t rs, int off) { return __builtin_amdgcn_raw_buffer_load_b64(rs, off, 0, 16); }
__device__ __forceinline__ void st_sc1(rsrc_t rs, int off, u32x4 v) { __builtin_amdgcn_raw_buffer_store_b128(v, rs, off, 0, 16); }
__device__ __forceinline__ void st_sc1_64(rsrc_t rs, int off, u32x2 v) { __builtin_amdgcn_raw_buffer_store_b64(v, rs, off, 0, 16); }
__device__ __forceinline__ unsigned mx4(u32x4 v) { return max(max(v[0], v[1]), max(v[2], v[3])); }
#define MEMBAR() asm volatile("" ::: "memory")
#define LGKM0() asm volatile("s_waitcnt lgkmcnt(0)" ::: "memory")
#define SB() __builtin_amdgcn_sched_barrier(0)
#define NOUNROLL _Pragma("nounroll")

__device__ __forceinline__ void lds_put(LAS unsigned char* L, int word, unsigned v) { ((volatile LAS unsigned*)L)[word] = v; }
__device__ __forceinline__ void lds_wait(LAS unsigned char* L, int word, unsigned v) {
  NOUNROLL for (unsigned s = 0; s < SPIN_L; ++s) {
    if (((volatile LAS unsigned*)L)[word] >= v) break;
    __builtin_amdgcn_s_sleep(1);
  }
  MEMBAR();
}
__device__ __forceinline__ void lds_wait4(LAS unsigned char* L, int word, unsigned v) {
  NOUNROLL for (unsigned s = 0; s < SPIN_L; ++s) {
    const u32x4 q = *(volatile LAS u32x4*)(L + word * 4);
    if (min(min(q[0], q[1]), min(q[2], q[3])) >= v) break;
    __builtin_amdgcn_s_sleep(1);
  }
  MEMBAR();
}
__device__ __forceinline__ void lds_spin(LAS unsigned char* L, int word, unsigned v) {
  NOUNROLL for (unsigned s = 0; s < SPIN_L; ++s) {
    if (((volatile LAS unsigned*)L)[word] >= v) break;
  }
  MEMBAR();
}
__device__ __forceinline__ unsigned lds_arrive(int word) {
  unsigned old;
  asm volatile("s_mov_b64 exec, 1\n\tds_add_rtn_u32 %0, %1, %2\n\ts_mov_b64 exec, -1\n\ts_waitcnt lgkmcnt(0)"
               : "=&v"(old) : "v"(word * 4), "v"(1u) : "memory");
  return __builtin_amdgcn_readfirstlane(old);
}
__device__ __forceinline__ void lds_arrive_nr(int word) {
  asm volatile("s_mov_b64 exec, 1\n\tds_add_u32 %0, %1\n\ts_mov_b64 exec, -1" :: "v"(word * 4), "v"(1u) : "memory");
}
__device__ __forceinline__ void lds_wait8(LAS unsigned char* L, unsigned v) {
  NOUNROLL for (unsigned s = 0; s < SPIN_L; ++s) {
    const u32x4 q = *(volatile LAS u32x4*)L, r = *(volatile LAS u32x4*)(L + 16);
    if (min(min(min(q[0], q[1]), min(q[2], q[3])), min(min(r[0], r[1]), min(r[2], r[3]))) >= v) break;
    __builtin_amdgcn_s_sleep(2);
  }
  MEMBAR();
}
#define HBAR() do { LGKM0(); __builtin_amdgcn_s_barrier(); MEMBAR(); } while (0)
__device__ __forceinline__ void wait_until(u64 t) {
  NOUNROLL for (unsigned s = 0; s < 4096u; ++s) {
    if (__builtin_amdgcn_s_memrealtime() >= t) break;
    __builtin_amdgcn_s_sleep(2);
  }
  MEMBAR();
}
template <int N> __device__ __forceinline__ void vmwait() { asm volatile("s_waitcnt vmcnt(%0)" :: "i"(N) : "memory"); }

#define ST_(k) do {} while (0)
#define CNT_(sh) do {} while (0)

__device__ __forceinline__ float red8_b345(const float (&a)[8], int lane) {
  float v[4];
  const bool hi = (lane & 8) != 0;
#pragma unroll
  for (int k = 0; k < 4; ++k) {
    const float t1 = a[k] + dpp<XOR8>(a[k]), t2 = a[k + 4] + dpp<XOR8>(a[k + 4]);
    v[k] = hi ? t2 : t1;
  }
  const float u0 = tr16(v[0], v[2]), u1 = tr16(v[1], v[3]);
  return tr32(u0, u1);
}
__device__ __forceinline__ void prepack(u32x4& wa, u32x4& wb) {
#pragma unroll
  for (int d = 0; d < 4; ++d) {
    const unsigned lo = __builtin_amdgcn_perm(wb[d], wa[d], 0x05040100u);
    const unsigned hi = __builtin_amdgcn_perm(wb[d], wa[d], 0x07060302u);
    wa[d] = lo; wb[d] = hi;
  }
}
__device__ __forceinline__ void pin(u32x4& a) { asm volatile("" : "+v"(a)); }
__device__ __forceinline__ void mac_packed(float (&acc)[8], u32x4 wa, u32x4 wb, unsigned xp) {
#pragma unroll
  for (int d = 0; d < 4; ++d) { acc[2 * d] = dot2(wa[d], xp, acc[2 * d]); acc[2 * d + 1] = dot2(wb[d], xp, acc[2 * d + 1]); }
}
__device__ __forceinline__ void mac_pair(float (&acc)[8], u32x4 wa, u32x4 wb, unsigned xp) { prepack(wa, wb); mac_packed(acc, wa, wb, xp); }
__device__ __forceinline__ float silu_f(float v) { return v * __builtin_amdgcn_rcpf(1.f + __expf(-v)); }
__device__ __forceinline__ f32x4 sum4(const LAS float* p, int stride) {
  return (*(const LAS f32x4*)p + *(const LAS f32x4*)(p + stride)) + (*(const LAS f32x4*)(p + 2 * stride) + *(const LAS f32x4*)(p + 3 * stride));
}

__global__ void __launch_bounds__(768, 3)
mega(const u16* __restrict__ x, const u16* __restrict__ kc, const u16* __restrict__ vc, const u16* __restrict__ wq,
     const u16* __restrict__ wo, const u16* __restrict__ w1, const u16* __restrict__ w2, const u16* __restrict__ cnd,
     const u16* __restrict__ wmod, u16* __restrict__ out, unsigned char* ws) {
  extern __shared__ __attribute__((aligned(16))) unsigned char smem[];
  LAS unsigned char* L = (LAS unsigned char*)smem;
  const int tid = threadIdx.x, lane = tid & 63, wave = __builtin_amdgcn_readfirstlane(tid >> 6);
  const int b = blockIdx.x;
  const int h = (b & 7) * 4 + (b >> 6), j = (b >> 3) & 7, u = h * 8 + j;
  const int r4 = lane >> 4, c16 = lane & 15;
  const rsrc_t rs = mkrs(ws, WS_END);
  ST_(0);

  if (wave < 8) {
    const size_t kvoff = ((size_t)h * SK + (SK - WIN) + j * 512) * DH;
    const rsrc_t rk = mkrs(kc + kvoff, 131072), rv = mkrs(vc + kvoff, 131072);
    const int rot = (b * 8 + (b >> 4) + wave) * 1024;
    u32x4 big[32];
    u32x4 pp0, pp1;
    float mw;
    LAS float* STAT = (LAS float*)(L + L_STAT);
#define LOAD_STEP(i) do { \
      big[i] = __builtin_amdgcn_raw_buffer_load_b128((i) < 16 ? rk : rv, lane * 16, (((i) & 15) * 8192 + rot) & 131071, 2); \
      if ((i) == PREB - 1) __builtin_amdgcn_s_barrier();     \
      if ((i) == ITK || (i) == IT) lds_put(L, C_PROG + wave, (unsigned)(i)); \
      if ((i) >= 16 + DEP + 2 && (((i) - DEP) & 1) == 0 && (i) <= IB) { prepack(big[(i) - DEP - 2], big[(i) - DEP - 1]); pin(big[(i) - DEP - 2]); pin(big[(i) - DEP - 1]); } \
      if ((i) < ISW) { if ((i) >= DEPA) vmwait<DEPA>(); } else if ((i) < IB && (i) != IS) vmwait<DEP>(); \
      SB(); } while (0)
#pragma unroll
    for (int i = 0; i <= IS; ++i) LOAD_STEP(i);
    {
      ST_(2); lds_wait(L, C_QREADY, 1u); ST_(3);
      const u32x4 qp = *(LAS u32x4*)(L + L_Q + c16 * 16);
      float xs[16];
#pragma unroll
      for (int ii = 0; ii < 16; ++ii) {
        float d = 0.f;
#pragma unroll
        for (int k = 0; k < 4; ++k) d = dot2(big[ii][k], qp[k], d);
        xs[ii] = d;
      }
#define TR_STEP(HALF, CTRL, BIT) _Pragma("unroll") for (int q = 0; q < (HALF); ++q) { \
      const float lo_ = xs[q], hi_ = xs[q + (HALF)]; const float t1_ = lo_ + dpp<CTRL>(lo_), t2_ = hi_ + dpp<CTRL>(hi_); \
      xs[q] = (c16 & (BIT)) ? t2_ : t1_; }
      TR_STEP(8, XOR8, 8) TR_STEP(4, XOR7, 4) TR_STEP(2, XOR2, 2) TR_STEP(1, XOR1, 1)
#undef TR_STEP
      const float sc = rbf(rbf(xs[0]) * 0.08838834764831845f);
      mw = wave_max(sc);
      const float e = __expf(sc - mw);
      LAS u16* P = (LAS u16*)(L + L_P) + wave * 64;
      P[lane] = (u16)f2bf(e);
      MEMBAR();
      pp0 = *(volatile LAS u32x4*)(P + r4 * 16); pp1 = *(volatile LAS u32x4*)(P + r4 * 16 + 8);
      STAT[wave] = mw;
      STAT[8 + wave] = wave_sum(e);
      if (lds_arrive(C_M) == 7u) {
        const float m8 = STAT[lane & 7], l8 = STAT[8 + (lane & 7)];
        float mc = fmaxf(m8, dpp<XOR1>(m8)); mc = fmaxf(mc, dpp<XOR2>(mc)); mc = fmaxf(mc, dpp<XOR7>(mc));
        float lc = l8 * __expf(m8 - mc);
        lc += dpp<XOR1>(lc); lc += dpp<XOR2>(lc); lc += dpp<XOR7>(lc);
        if (lane == 0) st_sc1_64(rs, WS_ST + u * 128, u32x2{nosent(mc), nosent(lc)});
      }
          ST_(4);
      }
#pragma unroll
    for (int i = IS + 1; i < 32; ++i) LOAD_STEP(i);
    ST_(1);
    __builtin_amdgcn_s_setprio(2);
    {
      float acc[8] = {0.f, 0.f, 0.f, 0.f, 0.f, 0.f, 0.f, 0.f};
      constexpr int NPRE = ((IB < 30 ? IB : 30) - 16 - DEP) / 2;
      float scl;
#pragma unroll
      for (int m = 0; m < 8; ++m) {
        if (m == NPRE) {
          lds_spin(L, C_M, 8u);
          const f32x4 ma = *(LAS f32x4*)STAT, mb = *(LAS f32x4*)(STAT + 4);
          const float mc = fmaxf(fmaxf(fmaxf(ma[0], ma[1]), fmaxf(ma[2], ma[3])), fmaxf(fmaxf(mb[0], mb[1]), fmaxf(mb[2], mb[3])));
          scl = __expf(mw - mc);
          asm volatile("" : "+v"(scl));
        }
        if (m >= NPRE) prepack(big[16 + 2 * m], big[16 + 2 * m + 1]);
        mac_packed(acc, big[16 + 2 * m], big[16 + 2 * m + 1], m < 4 ? pp0[m & 3] : pp1[m & 3]);
        SB();
      }
      ST_(5);
      float v[4];
#pragma unroll
      for (int k = 0; k < 4; ++k) v[k] = tr16(acc[k], acc[k + 4]);
      const float y0 = tr32(v[0], v[2]) * scl, y1 = tr32(v[1], v[3]) * scl;
      LAS float* OR = (LAS float*)(L + L_OR);
      *(LAS f32x2*)(OR + wave * 128 + c16 * 8 + (r4 & 1) * 4 + (r4 >> 1) * 2) = f32x2{y0, y1};
      if (lds_arrive(C_PV) == 7u) {
        const f32x4 s = sum4(OR + (lane >> 5) * 512 + 4 * (lane & 31), 128);
        f32x4 t;
#pragma unroll
        for (int k = 0; k < 4; ++k) t[k] = tr32(s[k], s[k]);
        if (lane < 32) st_sc1(rs, WS_OP + u * 512 + lane * 16, nosent4(t));
        MEMBAR();
        lds_put(L, C_PV, 9u);
      }
      ST_(6);
    }
  } else {
    const int w = wave - 8;
    int zero_v;
    asm volatile("v_mov_b32 %0, 0" : "=v"(zero_v));
    const int r8 = lane >> 3, c8 = lane & 7;
    __builtin_amdgcn_s_setprio(3);
    const u64 tstart = __builtin_amdgcn_s_memrealtime();
    const bool m1 = b < 240;
    const int cg = b % 60, rg = b / 60;
    u32x4 wm[5]; unsigned cw0, cw1, cw2;
#define MOD_TILE_LOAD() do {  \
      const int row0 = 160 * rg + 40 * w; \
      const u16* wb = wmod + (size_t)row0 * (6 * D) + cg * 64 + c8 * 8; \
      wm[0] = ldnt(wb + (size_t)(2 * r8) * (6 * D)); wm[1] = ldnt(wb + (size_t)(2 * r8 + 1) * (6 * D)); \
      wm[2] = ldnt(wb + (size_t)(16 + 2 * r8) * (6 * D)); wm[3] = ldnt(wb + (size_t)(17 + 2 * r8) * (6 * D)); \
      wm[4] = ldnt(wb + (size_t)(32 + r8) * (6 * D)); \
      cw0 = __builtin_nontemporal_load((const unsigned*)(cnd + row0 + 2 * r8)); \
      cw1 = __builtin_nontemporal_load((const unsigned*)(cnd + row0 + 16 + 2 * r8)); \
      cw2 = __builtin_nontemporal_load(cnd + row0 + 32 + r8); \
     } while (0)
    if (m1 && cg < 20) MOD_TILE_LOAD();
    const u32x2 Ev = ld_sc1_64(rs, WS_EPOCH + zero_v);
    const int l20 = (lane & 31) < 20 ? (lane & 31) : 19;
    const int hrow = 80 * j + 20 * w + l20;
    const u32x4 xa = ldnt(x + lane * 8);
    const unsigned xb = __builtin_nontemporal_load((const unsigned*)x + 256 + lane);
    const float xr = bflo((unsigned)__builtin_nontemporal_load(x + hrow));
    if (w == 3) {
      if (lane < 32) lds_put(L, lane, 0u);
    }
    SB();
    LGKM0();
    __builtin_amdgcn_s_barrier();
    SB();
    u32x4 wqv[5];
    if (WQE) {
#pragma unroll
      for (int i = 0; i < 5; ++i) wqv[i] = ldnt(wq + (size_t)(80 * j + 20 * w + 5 * r4 + i) * (HQ * DH) + h * DH + c16 * 8);
      SB();
    }
    if (m1 && cg >= 20) { wait_until(tstart + TM2); MOD_TILE_LOAD(); }
    const unsigned tag = bij32(Ev[0]), flo = bij32(Ev[0] ^ Ev[1] ^ 0x5bd1e995u);
    if (m1) {
      float acc[8] = {0.f, 0.f, 0.f, 0.f, 0.f, 0.f, 0.f, 0.f};
      mac_pair(acc, wm[0], wm[1], pack2(silu_f(bflo(cw0)), silu_f(bfhi(cw0))));
      mac_pair(acc, wm[2], wm[3], pack2(silu_f(bflo(cw1)), silu_f(bfhi(cw1))));
      {
        const unsigned sl = f2bf(silu_f(bflo(cw2))), sh = sl << 16;
#pragma unroll
        for (int d = 0; d < 4; ++d) { acc[2 * d] = dot2(wm[4][d], sl, acc[2 * d]); acc[2 * d + 1] = dot2(wm[4][d], sh, acc[2 * d + 1]); }
      }
      const float y = red8_b345(acc, lane);
      const int col = c8 * 8 + ((lane >> 3) & 1) * 4 + ((lane >> 4) & 1) * 2 + (lane >> 5);
      LAS float* MR = (LAS float*)(L + L_MR);
      MR[w * 64 + col] = y;
      if (lds_arrive(C_MOD1) == 3u) {
        const float s = (MR[lane] + MR[64 + lane]) + (MR[128 + lane] + MR[192 + lane]);
        __hip_atomic_store((gu64*)(ws + WS_MP) + rg * 3840 + cg * 64 + lane, ((u64)tag << 32) | (u64)fb(s), RLX_AGENT);
      }
    }
    ST_(1);
    if (w == 3) {
      u32x4 sv = {SENT, SENT, SENT, SENT};
      st_sc1(rs, (lane < 32 ? WS_QP : WS_OP) + u * 512 + (lane & 31) * 16, sv);
      if (b >= 128 && b < 128 + NF) {
        st_sc1(rs, WS_ZP + (b - 128) * 2560 + lane * 16, sv);
        st_sc1(rs, WS_ZP + (b - 128) * 2560 + 1024 + lane * 16, sv);
      }
      int o2 = 0x7FFFFF00;
      if (lane == 63) o2 = WS_ST + u * 128;
      else if (b < 120) { if (lane < 16) o2 = WS_YP + b * 256 + lane * 16; }
      else if (b >= 128 && b < 128 + NF) { if (lane < 32) o2 = WS_ZP + (b - 128) * 2560 + 2048 + lane * 16; }
      else if (b == 255 && lane < 8) { o2 = WS_TICKET + lane * 128; sv = u32x4{0u, 0u, 0u, 0u}; }
      st_sc1(rs, o2, sv);
          vmwait<0>();
      if (lane == 0) __hip_atomic_store((gu64*)(ws + WS_FLAGS) + b, ((u64)tag << 32) | (u64)flo, RLX_AGENT);
    }
    {
      if (!WQE) {
#pragma unroll
        for (int i = 0; i < 5; ++i) wqv[i] = ldnt(wq + (size_t)(80 * j + 20 * w + 5 * r4 + i) * (HQ * DH) + h * DH + c16 * 8);
      }
      SB();
      float nrm;
      {
        float s = bflo(xb) + bfhi(xb);
#pragma unroll
        for (int i = 0; i < 4; ++i) s += bflo(xa[i]) + bfhi(xa[i]);
        const float mean = wave_sum(s) * (1.f / 640.f);
        float q2 = (bflo(xb) - mean) * (bflo(xb) - mean) + (bfhi(xb) - mean) * (bfhi(xb) - mean);
#pragma unroll
        for (int i = 0; i < 4; ++i) { const float d0 = bflo(xa[i]) - mean, d1 = bfhi(xa[i]) - mean; q2 += d0 * d0 + d1 * d1; }
        const float var = wave_sum(q2) * (1.f / 640.f);
        const float den = rbf(sqrtf(rbf(rbf(var) + 1e-5f)));
        nrm = rbf(rbf(xr - rbf(mean)) / den);
      }
      gu64* mp = (gu64*)(ws + WS_MP) + (lane >> 5) * 640 + hrow;
      u64 g[NMP];
      wait_until(tstart + T1);
      NOUNROLL for (unsigned s = 0; s < SPIN_G; ++s) {
        bool ok = true;
#pragma unroll
        for (int i = 0; i < NMP; ++i) g[i] = __hip_atomic_load(mp + i * 3840, RLX_AGENT);
#pragma unroll
        for (int i = 0; i < NMP; ++i) ok = ok & ((unsigned)(g[i] >> 32) == tag);
        CNT_(0);
        if (__all(ok)) break;
        __builtin_amdgcn_s_sleep(PSLEEP);
      }
      const float mv = rbf((bf((unsigned)g[0]) + bf((unsigned)g[1])) + (bf((unsigned)g[2]) + bf((unsigned)g[3])));
      auto sw = __builtin_amdgcn_permlane32_swap(fb(mv), fb(mv), false, false);
      const float hval = rbf(rbf(nrm * rbf(1.f + bf(sw[1]))) + bf(sw[0]));
      ST_(2);
      float acc[8] = {0.f, 0.f, 0.f, 0.f, 0.f, 0.f, 0.f, 0.f};
#pragma unroll
      for (int i = 0; i < 5; ++i) {
        const float hk = __shfl(hval, 5 * r4 + i);
#pragma unroll
        for (int d = 0; d < 4; ++d) { acc[2 * d] = fmaf(hk, bflo(wqv[i][d]), acc[2 * d]); acc[2 * d + 1] = fmaf(hk, bfhi(wqv[i][d]), acc[2 * d + 1]); }
      }
      float v[4];
#pragma unroll
      for (int k = 0; k < 4; ++k) v[k] = tr16(acc[k], acc[k + 4]);
      const float y0 = tr32(v[0], v[2]), y1 = tr32(v[1], v[3]);
      LAS float* QR = (LAS float*)(L + L_QR);
      *(LAS f32x2*)(QR + w * 128 + c16 * 8 + (r4 & 1) * 4 + (r4 >> 1) * 2) = f32x2{y0, y1};
      if (lds_arrive(C_QP) == 3u) {
        if (lane < 32) st_sc1(rs, WS_QP + u * 512 + lane * 16, nosent4(sum4(QR + 4 * lane, 128)));
      }
    }
    ST_(3);
    if (w == 1) {
      wait_until(tstart + T3);
      NOUNROLL for (unsigned s = 0; s < SPIN_G; ++s) {
        MEMBAR();
        const u32x4 g0 = ld_sc1(rs, WS_FLAGS + lane * 16), g1 = ld_sc1(rs, WS_FLAGS + 1024 + lane * 16);
        const unsigned bad = ((g0[0] ^ flo) | (g0[1] ^ tag)) | ((g0[2] ^ flo) | (g0[3] ^ tag)) | ((g1[0] ^ flo) | (g1[1] ^ tag)) | ((g1[2] ^ flo) | (g1[3] ^ tag));
        if (__all(bad == 0u)) break;
        __builtin_amdgcn_s_sleep(PSLEEP);
      }
      lds_put(L, C_FLAGOK, 1u);
      lds_wait(L, C_PROG, ITK);
      if (lane == 0) {
        const unsigned tk = __hip_atomic_fetch_add((GAS unsigned*)(ws + WS_TICKET + j * 128), 1u, RLX_AGENT);
        lds_put(L, C_ROLE, tk * 8u + (unsigned)j + 1u);
      }
    } else if (w == 0) {
      u32x4 a0, a1, b0, b1;
      const int off = WS_QP + (h * 8 + 2 * r4) * 512 + c16 * 32;
      lds_wait(L, C_FLAGOK, 1u);
      wait_until(tstart + T2);
      NOUNROLL for (unsigned s = 0; s < SPIN_G; ++s) {
        MEMBAR();
        a0 = ld_sc1(rs, off); a1 = ld_sc1(rs, off + 16); b0 = ld_sc1(rs, off + 512); b1 = ld_sc1(rs, off + 528);
        CNT_(16);
        if (__all(max(max(mx4(a0), mx4(a1)), max(mx4(b0), mx4(b1))) != SENT)) break;
        __builtin_amdgcn_s_sleep(PSLEEP);
      }
      float v[4];
#pragma unroll
      for (int k = 0; k < 4; ++k) v[k] = tr16(bf(a0[k]) + bf(b0[k]), bf(a1[k]) + bf(b1[k]));
      const float y0 = tr32(v[0], v[2]), y1 = tr32(v[1], v[3]);
      ((LAS unsigned*)(L + L_Q))[c16 * 4 + (r4 & 1) * 2 + (r4 >> 1)] = pack2(y0, y1);
      MEMBAR();
      lds_put(L, C_QREADY, 1u);
      ST_(4);
    } else if (b >= 240) {
      const int mc = ((b - 240) * 2 + (w - 2)) * 80 + 2 * (lane < 40 ? lane : 39);
      u32x4 g[4];
      lds_wait(L, C_FLAGOK, 1u);
      NOUNROLL for (unsigned s = 0; s < SPIN_G; ++s) {
        MEMBAR();
        unsigned bad = 0u;
#pragma unroll
        for (int i = 0; i < 4; ++i) g[i] = ld_sc1(rs, WS_MP + (i * 3840 + 1280 + mc) * 8);
#pragma unroll
        for (int i = 0; i < 4; ++i) bad |= (g[i][1] ^ tag) | (g[i][3] ^ tag);
        if (__all(bad == 0u)) break;
        __builtin_amdgcn_s_sleep(PSLEEP);
      }
      const float m0 = rbf((bf(g[0][0]) + bf(g[1][0])) + (bf(g[2][0]) + bf(g[3][0]))), m1v = rbf((bf(g[0][2]) + bf(g[1][2])) + (bf(g[2][2]) + bf(g[3][2])));
      st_sc1_64(rs, WS_M2 + mc * 4, u32x2{nosent(m0), nosent(m1v)});
    }
    lds_wait(L, C_ROLE, 1u);
    const int role = __builtin_amdgcn_readfirstlane(((volatile LAS unsigned*)L)[C_ROLE]) - 1;
    __builtin_amdgcn_s_setprio(0);
    const int r16 = lane >> 2, c4 = lane & 3;
    const int col32 = c4 * 8 + ((lane >> 3) & 1) * 4 + ((lane >> 4) & 1) * 2 + (lane >> 5);
    const int col64 = c8 * 8 + ((lane >> 3) & 1) * 4 + ((lane >> 4) & 1) * 2 + (lane >> 5);
    LAS float* YR = (LAS float*)(L + L_YR);
    if (role < N_M2 + N_WO) {
      const int g = (role - N_M2) & 7, cgp = (role - N_M2) >> 3, hw = 4 * g + w;
      u32x4 wt[16];
      TSTART(TA_WO);
      const u16* wb = wo + (size_t)(hw * 128 + 16 * r8) * D + cgp * 64 + c8 * 8;
#pragma unroll
      for (int i = 0; i < 16; ++i) { wt[i] = ldnt(wb + i * D); if (i >= TDEP && i < 15) vmwait<TDEP>(); SB(); }
      SB();
      ST_(5);
      u32x4 a[4]; u32x2 ml;
      const int off = WS_OP + (hw * 8 + 4 * (lane >> 5)) * 512 + (lane & 31) * 16;
      const int mo = WS_ST + (hw * 8 + c8) * 128;
      ml = ld_sc1_64(rs, mo);
      SB();
#pragma unroll
      for (int m = 0; m < 8; ++m) { prepack(wt[2 * m], wt[2 * m + 1]); pin(wt[2 * m]); pin(wt[2 * m + 1]); }
      float acc[8] = {0.f, 0.f, 0.f, 0.f, 0.f, 0.f, 0.f, 0.f};
      asm volatile("" : "+v"(acc[0]), "+v"(acc[1]), "+v"(acc[2]), "+v"(acc[3]), "+v"(acc[4]), "+v"(acc[5]), "+v"(acc[6]), "+v"(acc[7]));
      float wj[4];
      NOUNROLL for (unsigned s = 0; s < SPIN_G; ++s) {
        MEMBAR();
#pragma unroll
        for (int i = 0; i < 4; ++i) a[i] = ld_sc1(rs, off + i * 512);
        const bool mok = __all(max(ml[0], ml[1]) != SENT);
        {
          float M = bf(ml[0]);
          M = fmaxf(M, dpp<XOR1>(M)); M = fmaxf(M, dpp<XOR2>(M)); M = fmaxf(M, dpp<XOR7>(M));
          const float e = __expf(bf(ml[0]) - M);
          float Ls = bf(ml[1]) * e;
          Ls += dpp<XOR1>(Ls); Ls += dpp<XOR2>(Ls); Ls += dpp<XOR7>(Ls);
          const float wgt = e * __builtin_amdgcn_rcpf(Ls);
#pragma unroll
          for (int i = 0; i < 4; ++i) {
            const float lo = bf(__builtin_amdgcn_readlane(fb(wgt), i)), hi = bf(__builtin_amdgcn_readlane(fb(wgt), 4 + i));
            wj[i] = lane < 32 ? lo : hi;
          }
          asm volatile("" : "+v"(wj[0]), "+v"(wj[1]), "+v"(wj[2]), "+v"(wj[3]));
        }
        if (!mok) ml = ld_sc1_64(rs, mo);
        if (mok && __all(max(max(mx4(a[0]), mx4(a[1])), max(mx4(a[2]), mx4(a[3]))) != SENT)) break;
      }
      ST_(6);
      float t4[4];
#pragma unroll
      for (int k = 0; k < 4; ++k) t4[k] = fmaf(wj[0], bf(a[0][k]), wj[1] * bf(a[1][k])) + fmaf(wj[2], bf(a[2][k]), wj[3] * bf(a[3][k]));
      LAS unsigned* OB = (LAS unsigned*)(L + L_OB) + w * 64;
      OB[2 * (lane & 31) + (lane >> 5)] = pack2(tr32(t4[0], t4[2]), tr32(t4[1], t4[3]));
      MEMBAR();
      const u32x4 oa = *(volatile LAS u32x4*)(OB + 8 * r8), ob = *(volatile LAS u32x4*)(OB + 8 * r8 + 4);
#pragma unroll
      for (int m = 0; m < 4; ++m) mac_packed(acc, wt[2 * m], wt[2 * m + 1], oa[m]);
#pragma unroll
      for (int m = 0; m < 4; ++m) mac_packed(acc, wt[8 + 2 * m], wt[8 + 2 * m + 1], ob[m]);
      const float y = red8_b345(acc, lane);
      YR[w * 64 + col64] = y;
      HBAR();
      if (w == 0 && lane < 16) st_sc1(rs, WS_YP + (g * 640 + cgp * 64 + 4 * lane) * 4, nosent4(sum4(YR + 4 * lane, 64)));
      ST_(7);
    } else if (role < N_M2 + N_WO + NF) {
      const int f = role - (N_M2 + N_WO);
      const int row0 = 160 * w + 10 * r16;
      const int t = w * 64 + lane;
      const int tq = t < 160 ? t : 159;
      const bool fin = f < 20 && w == 3;
      const int oc = (f < 20 ? 32 * f : 0) + 4 * c8 + ((lane >> 4) & 1) * 2 + (lane >> 5);
      const u32x2 xq = __builtin_nontemporal_load((const u32x2*)(x + 4 * tq));
      LAS unsigned* X1 = (LAS unsigned*)(L + L_X1);
      u32x4 wt[18]; unsigned w2t[8];
      TSTART(TA_FFN);
      const u16* wb = w1 + (size_t)row0 * DFF + f * 32 + c4 * 8;
      const u16* w2b = w2 + (size_t)(f * 32 + 8 * w) * D;
#pragma unroll
      for (int i = 0; i < 18; ++i) {
        if (i < 10) wt[i] = ldnt(wb + (size_t)i * DFF);
        else {
          wt[i] = ldnt(w2b + (i - 10) * D + lane * 8);
          w2t[i - 10] = __builtin_nontemporal_load((const unsigned*)(w2b + (i - 10) * D + 512 + lane * 2));
        }
        if (i >= TDEPF && i < 17) vmwait<TDEPF>();
        SB();
      }
      SB();
      ST_(5);
      u32x4 ga; u32x2 sbA, sbB, cbA, cbB; unsigned gbw;
      NOUNROLL for (unsigned s = 0; s < SPIN_G; ++s) {
        MEMBAR();
        ga = ld_sc1(rs, WS_M2 + tq * 16);
        gbw = __builtin_amdgcn_raw_buffer_load_b32(rs, WS_M2 + 7680 + oc * 4, 0, 16);
        sbA = ld_sc1_64(rs, WS_M2 + 2560 + (row0 + 2 * c4) * 4); sbB = ld_sc1_64(rs, WS_M2 + 2560 + (row0 + 8) * 4);
        cbA = ld_sc1_64(rs, WS_M2 + 5120 + (row0 + 2 * c4) * 4); cbB = ld_sc1_64(rs, WS_M2 + 5120 + (row0 + 8) * 4);
        const unsigned mm = max(max(mx4(ga), gbw), max(max(max(sbA[0], sbA[1]), max(sbB[0], sbB[1])), max(max(cbA[0], cbA[1]), max(cbB[0], cbB[1]))));
        if (__all(mm != SENT)) break;
        __builtin_amdgcn_s_sleep(2);
      }
      float c1[4] = {rbf(1.f + bf(cbA[0])), rbf(1.f + bf(cbA[1])), rbf(1.f + bf(cbB[0])), rbf(1.f + bf(cbB[1]))};
      float xv[4] = {bflo(xq[0]), bfhi(xq[0]), bflo(xq[1]), bfhi(xq[1])};
      asm volatile("" : "+v"(c1[0]), "+v"(c1[1]), "+v"(c1[2]), "+v"(c1[3]), "+v"(xv[0]), "+v"(xv[1]), "+v"(xv[2]), "+v"(xv[3]));
      const float s1[4] = {bf(sbA[0]), bf(sbA[1]), bf(sbB[0]), bf(sbB[1])};
#pragma unroll
      for (int m = 0; m < 9; ++m) { prepack(wt[2 * m], wt[2 * m + 1]); pin(wt[2 * m]); pin(wt[2 * m + 1]); }
#pragma unroll
      for (int m = 0; m < 4; ++m) {
        const unsigned lo = __builtin_amdgcn_perm(w2t[2 * m + 1], w2t[2 * m], 0x05040100u), hi = __builtin_amdgcn_perm(w2t[2 * m + 1], w2t[2 * m], 0x07060302u);
        w2t[2 * m] = lo; w2t[2 * m + 1] = hi;
        asm volatile("" : "+v"(w2t[2 * m]), "+v"(w2t[2 * m + 1]));
      }
      float acc[8] = {0.f, 0.f, 0.f, 0.f, 0.f, 0.f, 0.f, 0.f};
      asm volatile("" : "+v"(acc[0]), "+v"(acc[1]), "+v"(acc[2]), "+v"(acc[3]), "+v"(acc[4]), "+v"(acc[5]), "+v"(acc[6]), "+v"(acc[7]));
      if (w < 3) {
        u32x4 a[8];
        NOUNROLL for (unsigned s = 0; s < SPIN_G; ++s) {
          MEMBAR();
#pragma unroll
          for (int i = 0; i < 8; ++i) a[i] = ld_sc1(rs, WS_YP + i * 2560 + tq * 16);
          unsigned mm = mx4(a[0]);
#pragma unroll
          for (int i = 1; i < 8; ++i) mm = max(mm, mx4(a[i]));
          if (__all(mm != SENT)) break;
        }
        ST_(6);
        float x1[4];
#pragma unroll
        for (int k = 0; k < 4; ++k) {
          const float at = ((bf(a[0][k]) + bf(a[1][k])) + (bf(a[2][k]) + bf(a[3][k]))) + ((bf(a[4][k]) + bf(a[5][k])) + (bf(a[6][k]) + bf(a[7][k])));
          x1[k] = xv[k] + rbf(bf(ga[k]) * rbf(at));
        }
        *(LAS u32x2*)(X1 + 2 * tq) = u32x2{pack2(x1[0], x1[1]), pack2(x1[2], x1[3])};
      }
      HBAR();
      unsigned hp5[5];
      {
        const u32x4 ya = *(LAS u32x4*)(X1 + 4 * lane);
        const unsigned yb = X1[256 + lane];
        const unsigned hA = X1[(row0 >> 1) + c4], hB = X1[(row0 >> 1) + 4];
        float sa_ = dot2(yb, 0x3F803F80u, 0.f), sq_ = dot2(yb, yb, 0.f);
#pragma unroll
        for (int k = 0; k < 4; ++k) { sa_ = dot2(ya[k], 0x3F803F80u, sa_); sq_ = dot2(ya[k], ya[k], sq_); }
        float r = tr32(sa_, sq_);
        r = tr16(r, r);
        r = row_sum(r);
        const float mean = bf(__builtin_amdgcn_readlane(fb(r), 0)) * (1.f / 640.f);
        float var = fmaf(-mean, mean, bf(__builtin_amdgcn_readlane(fb(r), 32)) * (1.f / 640.f));
        if (!(var * 1024.f > mean * mean)) {
          float q2 = 0.f;
#pragma unroll
          for (int k = 0; k < 4; ++k) { const float d0 = bflo(ya[k]) - mean, d1 = bfhi(ya[k]) - mean; q2 += d0 * d0 + d1 * d1; }
          const float d0 = bflo(yb) - mean, d1 = bfhi(yb) - mean; q2 += d0 * d0 + d1 * d1;
          var = wave_sum(q2) * (1.f / 640.f);
        }
        const float mr = rbf(mean);
        const float rden = __builtin_amdgcn_rcpf(rbf(__builtin_amdgcn_sqrtf(rbf(rbf(var) + 1e-5f))));
        const float xr2[4] = {bflo(hA), bfhi(hA), bflo(hB), bfhi(hB)};
        float hv[4];
#pragma unroll
        for (int k = 0; k < 4; ++k) hv[k] = rbf(rbf(rbf(xr2[k] - mr) * rden) * c1[k]) + s1[k];
        const unsigned pA = pack2(hv[0], hv[1]);
        hp5[4] = pack2(hv[2], hv[3]);
        hp5[0] = __builtin_amdgcn_mov_dpp((int)pA, 0x00, 0xf, 0xf, true);
        hp5[1] = __builtin_amdgcn_mov_dpp((int)pA, 0x55, 0xf, 0xf, true);
        hp5[2] = __builtin_amdgcn_mov_dpp((int)pA, 0xAA, 0xf, 0xf, true);
        hp5[3] = __builtin_amdgcn_mov_dpp((int)pA, 0xFF, 0xf, 0xf, true);
      }
      ST_(7);
      {
#pragma unroll
        for (int m = 0; m < 5; ++m) mac_packed(acc, wt[2 * m], wt[2 * m + 1], hp5[m]);
        float y = red8_b345(acc, lane);
        y += xor4(y);
        YR[w * 64 + col32] = y;
        float z[8] = {0.f, 0.f, 0.f, 0.f, 0.f, 0.f, 0.f, 0.f};
        float z8 = 0.f, z9 = 0.f;
        asm volatile("" : "+v"(z[0]), "+v"(z[1]), "+v"(z[2]), "+v"(z[3]), "+v"(z[4]), "+v"(z[5]), "+v"(z[6]), "+v"(z[7]), "+v"(z8), "+v"(z9));
        HBAR();
        const LAS float* yp = YR + 8 * w + c8;
        float hd = (yp[0] + yp[64]) + (yp[128] + yp[192]);
        hd = hd < 0.f ? 0.f : hd;
        const unsigned hpk = pack2(hd, dpp<XOR1>(hd));
#pragma unroll
        for (int m = 0; m < 4; ++m) {
          const unsigned hp = __builtin_amdgcn_readlane(hpk, 2 * m);
          mac_packed(z, wt[10 + 2 * m], wt[10 + 2 * m + 1], hp);
          z8 = dot2(w2t[2 * m], hp, z8);
          z9 = dot2(w2t[2 * m + 1], hp, z9);
        }
        LAS float* ZR = (LAS float*)(L + L_ZR);
        *(LAS f32x4*)(ZR + w * 640 + 8 * lane) = f32x4{z[0], z[1], z[2], z[3]};
        *(LAS f32x4*)(ZR + w * 640 + 8 * lane + 4) = f32x4{z[4], z[5], z[6], z[7]};
        *(LAS f32x2*)(ZR + w * 640 + 512 + 2 * lane) = f32x2{z8, z9};
        HBAR();
        if (t < 160) st_sc1(rs, WS_ZP + f * 2560 + t * 16, nosent4(sum4(ZR + 4 * t, 640)));
      }
      ST_(8);
      if (fin) {
        u32x4 zp[7];
        const int zo = WS_ZP + r8 * 2560 + f * 128 + c8 * 16;
        const int z6 = r8 < NF - 48 ? zo + 48 * 2560 : 0x7FFFFF00;
        const float xo = bflo((unsigned)((LAS u16*)X1)[oc]);
        NOUNROLL for (unsigned s = 0; s < SPIN_G; ++s) {
          MEMBAR();
#pragma unroll
          for (int i = 0; i < 6; ++i) zp[i] = ld_sc1(rs, zo + i * 8 * 2560);
          zp[6] = ld_sc1(rs, z6);
          unsigned mm = mx4(zp[0]);
#pragma unroll
          for (int i = 1; i < 7; ++i) mm = max(mm, mx4(zp[i]));
          if (__all(mm != SENT)) break;
        }
        ST_(9);
        float r[4];
#pragma unroll
        for (int k = 0; k < 4; ++k) r[k] = ((bf(zp[0][k]) + bf(zp[1][k])) + (bf(zp[2][k]) + bf(zp[3][k]))) + ((bf(zp[4][k]) + bf(zp[5][k])) + bf(zp[6][k]));
        float v = tr32(tr16(r[0], r[2]), tr16(r[1], r[3]));
        v += dpp<XOR8>(v);
        out[oc] = (u16)f2bf(xo + rbf(bf(gbw) * rbf(v)));
        if (f == 0 && lane == 0) {
          const u64 e1 = (((u64)Ev[1] << 32) | (u64)Ev[0]) + 1ull;
          st_sc1_64(rs, WS_EPOCH, u32x2{(unsigned)e1, (unsigned)(e1 >> 32)});
        }
        ST_(10);
      }
    }
  }
}

extern "C" void kernel_launch(void* const* d_in, const int* in_sizes, int n_in, void* d_out, int out_size, void* d_ws,
                              size_t ws_size, hipStream_t stream) {
  (void)in_sizes; (void)n_in; (void)out_size; (void)ws_size;
  (void)hipFuncSetAttribute((const void*)mega, hipFuncAttributeMaxDynamicSharedMemorySize, LDS_BYTES);
  hipLaunchKernelGGL(mega, dim3(256), dim3(768), LDS_BYTES, stream, (const u16*)d_in[0], (const u16*)d_in[1], (const u16*)d_in[2],
                     (const u16*)d_in[3], (const u16*)d_in[4], (const u16*)d_in[6], (const u16*)d_in[7], (const u16*)d_in[8],
                     (const u16*)d_in[9], (u16*)d_out, (unsigned char*)d_ws);
}
#define STAMP_ROLE 1
#define STAMP_CLS(r) ((r) < N_M2 ? 3 : (r) < N_M2 + N_WO ? 2 : (r) < N_M2 + N_WO + NF ? 4 : 0)
#define NWAVES 12
#define NSTREAM 8
```

```cpp
#include <hip/hip_runtime.h>
#include <hip/hip_bf16.h>
#include <stdint.h>

#define STAMP 0
#define PSLEEP 16
#define I1 12
#define I2 21
#define I3 17
#define IT 31
#define IS 28
#define DEP 2
#define TDEP 40

#define GAS __attribute__((address_space(1)))
#define LAS __attribute__((address_space(3)))
#define RLX_AGENT __ATOMIC_RELAXED, __HIP_MEMORY_SCOPE_AGENT
typedef unsigned short u16;
typedef unsigned long long u64;
typedef unsigned u32x4 __attribute__((ext_vector_type(4)));
typedef unsigned u32x2 __attribute__((ext_vector_type(2)));
typedef float f32x4 __attribute__((ext_vector_type(4)));
typedef float f32x2 __attribute__((ext_vector_type(2)));
typedef __bf16 bf16x2 __attribute__((ext_vector_type(2)));
typedef GAS u64 gu64;
typedef __amdgpu_buffer_rsrc_t rsrc_t;

constexpr int D = 640, HQ = 32, SK = 16384, DH = 128, WIN = 4096, DFF = 1728;
constexpr int NF = 54;

constexpr int WS_EPOCH = 0;
constexpr int WS_FLAGS = 4096;
constexpr int WS_MP1 = 8192;
constexpr int WS_QP = 65536;
constexpr int WS_OP = WS_QP + 131072;
constexpr int WS_ST = WS_OP + 131072;
constexpr int WS_YP = WS_ST + 32768;
constexpr int WS_M2 = WS_YP + 20480;
constexpr int WS_ZP = WS_M2 + 10240;
constexpr int WS_END = WS_ZP + NF * 2560;
constexpr int WS_STAMP = 1 << 20;

constexpr unsigned SENT = 0xFFFFFFFFu;
constexpr unsigned SPIN_G = 1u << 18;
constexpr unsigned SPIN_L = 1u << 22;

constexpr int C_PROG = 0;
constexpr int C_MOD1 = 8, C_QP = 9, C_QREADY = 10, C_M = 11, C_L = 12, C_PV = 13;
constexpr int L_P = 128;
constexpr int L_MR = L_P + 1024;
constexpr int L_QR = L_MR + 1024;
constexpr int L_OR = L_QR + 2048;
constexpr int L_OB = L_OR + 4096;
constexpr int L_YR = L_OB + 1024;
constexpr int L_STAT = L_YR + 1024;
constexpr int L_Q = L_STAT + 64;
constexpr int L_X1 = L_Q + 256;
constexpr int L_ZR = L_X1 + 2560;
constexpr int L_END = L_ZR + 10240;
constexpr int LDS_BYTES = 96 * 1024;
static_assert(L_END <= LDS_BYTES, "lds");

__device__ __forceinline__ float bflo(unsigned w) { return __builtin_bit_cast(float, w << 16); }
__device__ __forceinline__ float bfhi(unsigned w) { return __builtin_bit_cast(float, w & 0xffff0000u); }
__device__ __forceinline__ unsigned f2bf(float f) { return (unsigned)__builtin_bit_cast(u16, (__bf16)f); }
__device__ __forceinline__ float rbf(float f) { return bflo(f2bf(f)); }
__device__ __forceinline__ unsigned pack2(float lo, float hi) {
  bf16x2 v = {(__bf16)lo, (__bf16)hi};
  return __builtin_bit_cast(unsigned, v);
}
__device__ __forceinline__ float dot2(unsigned a, unsigned b, float acc) {
  return __builtin_amdgcn_fdot2_f32_bf16(__builtin_bit_cast(bf16x2, a), __builtin_bit_cast(bf16x2, b), acc, false);
}
__device__ __forceinline__ unsigned fb(float f) { return __builtin_bit_cast(unsigned, f); }
__device__ __forceinline__ float bf(unsigned u) { return __builtin_bit_cast(float, u); }
__device__ __forceinline__ unsigned nosent(float f) { return min(fb(f), 0xFFFFFFFEu); }
__device__ __forceinline__ u32x4 nosent4(f32x4 s) { return u32x4{nosent(s[0]), nosent(s[1]), nosent(s[2]), nosent(s[3])}; }
template <int CTRL> __device__ __forceinline__ float dpp(float x) {
  return __builtin_bit_cast(float, __builtin_amdgcn_mov_dpp(__builtin_bit_cast(int, x), CTRL, 0xf, 0xf, true));
}
constexpr int XOR1 = 0xB1, XOR2 = 0x4E, XOR7 = 0x141, XOR8 = 0x128;
__device__ __forceinline__ float xor4(float x) {
  int v = __builtin_bit_cast(int, x);
  int r = __builtin_amdgcn_update_dpp(0, v, 0x114, 0xf, 0xa, false);
  r = __builtin_amdgcn_update_dpp(r, v, 0x104, 0xf, 0x5, false);
  return __builtin_bit_cast(float, r);
}
__device__ __forceinline__ float tr16(float a, float b) {
  auto s = __builtin_amdgcn_permlane16_swap(fb(a), fb(b), false, false);
  return bf(s[0]) + bf(s[1]);
}
__device__ __forceinline__ float tr32(float a, float b) {
  auto s = __builtin_amdgcn_permlane32_swap(fb(a), fb(b), false, false);
  return bf(s[0]) + bf(s[1]);
}
__device__ __forceinline__ float mx16(float a) {
  auto s = __builtin_amdgcn_permlane16_swap(fb(a), fb(a), false, false);
  return fmaxf(bf(s[0]), bf(s[1]));
}
__device__ __forceinline__ float mx32(float a) {
  auto s = __builtin_amdgcn_permlane32_swap(fb(a), fb(a), false, false);
  return fmaxf(bf(s[0]), bf(s[1]));
}
__device__ __forceinline__ float row_sum(float x) {
  x += dpp<XOR1>(x); x += dpp<XOR2>(x); x += dpp<XOR7>(x); x += dpp<XOR8>(x); return x;
}
__device__ __forceinline__ float row_max(float x) {
  x = fmaxf(x, dpp<XOR1>(x)); x = fmaxf(x, dpp<XOR2>(x)); x = fmaxf(x, dpp<XOR7>(x)); x = fmaxf(x, dpp<XOR8>(x)); return x;
}
__device__ __forceinline__ float wave_sum(float x) { x = row_sum(x); x = tr16(x, x); return tr32(x, x); }
__device__ __forceinline__ float wave_max(float x) { x = row_max(x); x = mx16(x); return mx32(x); }

__device__ __forceinline__ unsigned bij32(unsigned x) {
  x += 0x9E3779B9u; x ^= x >> 16; x *= 0x85EBCA6Bu; x ^= x >> 13; x *= 0xC2B2AE35u; x ^= x >> 16; return x;
}
__device__ __forceinline__ rsrc_t mkrs(const void* p, int bytes) { return __builtin_amdgcn_make_buffer_rsrc((void*)p, 0, bytes, 0x00020000); }
__device__ __forceinline__ u32x4 ldnt(const void* p) { return __builtin_nontemporal_load(reinterpret_cast<const u32x4*>(p)); }
__device__ __forceinline__ u32x4 ld_sc1(rsrc_t rs, int off) { return __builtin_amdgcn_raw_buffer_load_b128(rs, off, 0, 16); }
__device__ __forceinline__ u32x2 ld_sc1_64(rsrc_t rs, int off) { return __builtin_amdgcn_raw_buffer_load_b64(rs, off, 0, 16); }
__device__ __forceinline__ void st_sc1(rsrc_t rs, int off, u32x4 v) { __builtin_amdgcn_raw_buffer_store_b128(v, rs, off, 0, 16); }
__device__ __forceinline__ void st_sc1_64(rsrc_t rs, int off, u32x2 v) { __builtin_amdgcn_raw_buffer_store_b64(v, rs, off, 0, 16); }
__device__ __forceinline__ unsigned mx4(u32x4 v) { return max(max(v[0], v[1]), max(v[2], v[3])); }
#define MEMBAR() asm volatile("" ::: "memory")
#define LGKM0() asm volatile("s_waitcnt lgkmcnt(0)" ::: "memory")
#define SB() __builtin_amdgcn_sched_barrier(0)
#define NOUNROLL _Pragma("nounroll")

__device__ __forceinline__ void lds_put(LAS unsigned char* L, int word, unsigned v) { ((volatile LAS unsigned*)L)[word] = v; }
__device__ __forceinline__ void lds_wait(LAS unsigned char* L, int word, unsigned v) {
  NOUNROLL for (unsigned s = 0; s < SPIN_L; ++s) {
    if (((volatile LAS unsigned*)L)[word] >= v) break;
    __builtin_amdgcn_s_sleep(1);
  }
  MEMBAR();
}
__device__ __forceinline__ void lds_wait4(LAS unsigned char* L, int word, unsigned v) {
  NOUNROLL for (unsigned s = 0; s < SPIN_L; ++s) {
    const u32x4 q = *(volatile LAS u32x4*)(L + word * 4);
    if (min(min(q[0], q[1]), min(q[2], q[3])) >= v) break;
    __builtin_amdgcn_s_sleep(1);
  }
  MEMBAR();
}
__device__ __forceinline__ void lds_spin(LAS unsigned char* L, int word, unsigned v) {
  NOUNROLL for (unsigned s = 0; s < SPIN_L; ++s) {
    if (((volatile LAS unsigned*)L)[word] >= v) break;
  }
  MEMBAR();
}
__device__ __forceinline__ unsigned lds_arrive(int word) {
  unsigned old;
  asm volatile("s_mov_b64 exec, 1\n\tds_add_rtn_u32 %0, %1, %2\n\ts_mov_b64 exec, -1\n\ts_waitcnt lgkmcnt(0)"
               : "=&v"(old) : "v"(word * 4), "v"(1u) : "memory");
  return __builtin_amdgcn_readfirstlane(old);
}
__device__ __forceinline__ void lds_arrive_nr(int word) {
  asm volatile("s_mov_b64 exec, 1\n\tds_add_u32 %0, %1\n\ts_mov_b64 exec, -1" :: "v"(word * 4), "v"(1u) : "memory");
}
__device__ __forceinline__ void lds_wait8(LAS unsigned char* L, unsigned v) {
  NOUNROLL for (unsigned s = 0; s < SPIN_L; ++s) {
    const u32x4 q = *(volatile LAS u32x4*)L, r = *(volatile LAS u32x4*)(L + 16);
    if (min(min(min(q[0], q[1]), min(q[2], q[3])), min(min(r[0], r[1]), min(r[2], r[3]))) >= v) break;
    __builtin_amdgcn_s_sleep(2);
  }
  MEMBAR();
}
#define HBAR() do { LGKM0(); __builtin_amdgcn_s_barrier(); MEMBAR(); } while (0)
template <int N> __device__ __forceinline__ void vmwait() { asm volatile("s_waitcnt vmcnt(%0)" :: "i"(N) : "memory"); }

#define ST_(k) do {} while (0)
#define CNT_(sh) do {} while (0)

__device__ __forceinline__ float red8_b345(const float (&a)[8], int lane) {
  float v[4];
  const bool hi = (lane & 8) != 0;
#pragma unroll
  for (int k = 0; k < 4; ++k) {
    const float t1 = a[k] + dpp<XOR8>(a[k]), t2 = a[k + 4] + dpp<XOR8>(a[k + 4]);
    v[k] = hi ? t2 : t1;
  }
  const float u0 = tr16(v[0], v[2]), u1 = tr16(v[1], v[3]);
  return tr32(u0, u1);
}
__device__ __forceinline__ void prepack(u32x4& wa, u32x4& wb) {
#pragma unroll
  for (int d = 0; d < 4; ++d) {
    const unsigned lo = __builtin_amdgcn_perm(wb[d], wa[d], 0x05040100u);
    const unsigned hi = __builtin_amdgcn_perm(wb[d], wa[d], 0x07060302u);
    wa[d] = lo; wb[d] = hi;
  }
}
__device__ __forceinline__ void pin(u32x4& a) { asm volatile("" : "+v"(a)); }
__device__ __forceinline__ void mac_packed(float (&acc)[8], u32x4 wa, u32x4 wb, unsigned xp) {
#pragma unroll
  for (int d = 0; d < 4; ++d) { acc[2 * d] = dot2(wa[d], xp, acc[2 * d]); acc[2 * d + 1] = dot2(wb[d], xp, acc[2 * d + 1]); }
}
__device__ __forceinline__ void mac_pair(float (&acc)[8], u32x4 wa, u32x4 wb, unsigned xp) { prepack(wa, wb); mac_packed(acc, wa, wb, xp); }
__device__ __forceinline__ float silu_bf(float v) { return rbf(v / (1.f + __expf(-v))); }
__device__ __forceinline__ f32x4 sum4(const LAS float* p, int stride) {
  return (*(const LAS f32x4*)p + *(const LAS f32x4*)(p + stride)) + (*(const LAS f32x4*)(p + 2 * stride) + *(const LAS f32x4*)(p + 3 * stride));
}

__global__ void __launch_bounds__(768, 3)
mega(const u16* __restrict__ x, const u16* __restrict__ kc, const u16* __restrict__ vc, const u16* __restrict__ wq,
     const u16* __restrict__ wo, const u16* __restrict__ w1, const u16* __restrict__ w2, const u16* __restrict__ cnd,
     const u16* __restrict__ wmod, u16* __restrict__ out, unsigned char* ws) {
  extern __shared__ __attribute__((aligned(16))) unsigned char smem[];
  LAS unsigned char* L = (LAS unsigned char*)smem;
  const int tid = threadIdx.x, lane = tid & 63, wave = __builtin_amdgcn_readfirstlane(tid >> 6);
  const int b = blockIdx.x;
  const int h = (b & 7) * 4 + (b >> 6), j = (b >> 3) & 7, u = h * 8 + j;
  const int r4 = lane >> 4, c16 = lane & 15;
  const rsrc_t rs = mkrs(ws, WS_END);
  ST_(0);

  if (wave < 8) {
    const size_t kvoff = ((size_t)h * SK + (SK - WIN) + j * 512) * DH;
    const rsrc_t rk = mkrs(kc + kvoff, 131072), rv = mkrs(vc + kvoff, 131072);
    const int rot = (b * 37 + wave) * 1024;
    u32x4 big[32];
    u32x4 pp0, pp1;
    float mw;
    LAS float* STAT = (LAS float*)(L + L_STAT);
#define LOAD_STEP(i) do { \
      big[i] = __builtin_amdgcn_raw_buffer_load_b128((i) < 16 ? rk : rv, lane * 16, (((i) & 15) * 8192 + rot) & 131071, 2); \
      if ((i) == DEP) __builtin_amdgcn_s_barrier();     \
      if ((i) == I1) lds_put(L, C_PROG + wave, 1u); \
      if ((i) == I3) lds_put(L, C_PROG + wave, 2u); \
      if ((i) == I2) lds_put(L, C_PROG + wave, 3u); \
      if ((i) == IT) lds_put(L, C_PROG + wave, 4u); \
      if ((i) >= 16 + DEP + 2 && (((i) - DEP) & 1) == 0 && (i) < 31) { prepack(big[(i) - DEP - 2], big[(i) - DEP - 1]); pin(big[(i) - DEP - 2]); pin(big[(i) - DEP - 1]); } \
      if ((i) >= DEP && (i) < 31 && (i) != IS) vmwait<DEP>(); \
      SB(); } while (0)
#pragma unroll
    for (int i = 0; i <= IS; ++i) LOAD_STEP(i);
    {
      ST_(2); lds_wait(L, C_QREADY, 1u); ST_(3);
      const u32x4 qp = *(LAS u32x4*)(L + L_Q + c16 * 16);
      float xs[16];
#pragma unroll
      for (int ii = 0; ii < 16; ++ii) {
        float d = 0.f;
#pragma unroll
        for (int k = 0; k < 4; ++k) d = dot2(big[ii][k], qp[k], d);
        xs[ii] = d;
      }
#define TR_STEP(HALF, CTRL, BIT) _Pragma("unroll") for (int q = 0; q < (HALF); ++q) { \
      const float lo_ = xs[q], hi_ = xs[q + (HALF)]; const float t1_ = lo_ + dpp<CTRL>(lo_), t2_ = hi_ + dpp<CTRL>(hi_); \
      xs[q] = (c16 & (BIT)) ? t2_ : t1_; }
      TR_STEP(8, XOR8, 8) TR_STEP(4, XOR7, 4) TR_STEP(2, XOR2, 2) TR_STEP(1, XOR1, 1)
#undef TR_STEP
      const float sc = rbf(rbf(xs[0]) * 0.08838834764831845f);
      mw = wave_max(sc);
      const float e = __expf(sc - mw);
      LAS u16* P = (LAS u16*)(L + L_P) + wave * 64;
      P[lane] = (u16)f2bf(e);
      MEMBAR();
      pp0 = *(volatile LAS u32x4*)(P + r4 * 16); pp1 = *(volatile LAS u32x4*)(P + r4 * 16 + 8);
      STAT[wave] = mw;
      STAT[8 + wave] = wave_sum(e);
      if (lds_arrive(C_M) == 7u) {
        const float m8 = STAT[lane & 7], l8 = STAT[8 + (lane & 7)];
        float mc = fmaxf(m8, dpp<XOR1>(m8)); mc = fmaxf(mc, dpp<XOR2>(mc)); mc = fmaxf(mc, dpp<XOR7>(mc));
        float lc = l8 * __expf(m8 - mc);
        lc += dpp<XOR1>(lc); lc += dpp<XOR2>(lc); lc += dpp<XOR7>(lc);
        if (lane == 0) st_sc1_64(rs, WS_ST + u * 128, u32x2{nosent(mc), nosent(lc)});
      }
          ST_(4);
      }
#pragma unroll
    for (int i = IS + 1; i < 32; ++i) LOAD_STEP(i);
    ST_(1);
    {
      float acc[8] = {0.f, 0.f, 0.f, 0.f, 0.f, 0.f, 0.f, 0.f};
      constexpr int NPRE = (14 - DEP) / 2;
      float scl;
#pragma unroll
      for (int m = 0; m < 8; ++m) {
        if (m == NPRE) {
          lds_spin(L, C_M, 8u);
          const f32x4 ma = *(LAS f32x4*)STAT, mb = *(LAS f32x4*)(STAT + 4);
          const float mc = fmaxf(fmaxf(fmaxf(ma[0], ma[1]), fmaxf(ma[2], ma[3])), fmaxf(fmaxf(mb[0], mb[1]), fmaxf(mb[2], mb[3])));
          scl = __expf(mw - mc);
          asm volatile("" : "+v"(scl));
        }
        if (m >= NPRE) prepack(big[16 + 2 * m], big[16 + 2 * m + 1]);
        mac_packed(acc, big[16 + 2 * m], big[16 + 2 * m + 1], m < 4 ? pp0[m & 3] : pp1[m & 3]);
        SB();
      }
      ST_(5);
      float v[4];
#pragma unroll
      for (int k = 0; k < 4; ++k) v[k] = tr16(acc[k], acc[k + 4]);
      const float y0 = tr32(v[0], v[2]) * scl, y1 = tr32(v[1], v[3]) * scl;
      LAS float* OR = (LAS float*)(L + L_OR);
      *(LAS f32x2*)(OR + wave * 128 + c16 * 8 + (r4 & 1) * 4 + (r4 >> 1) * 2) = f32x2{y0, y1};
      if (lds_arrive(C_PV) == 7u) {
        const f32x4 s = sum4(OR + (lane >> 5) * 512 + 4 * (lane & 31), 128);
        f32x4 t;
#pragma unroll
        for (int k = 0; k < 4; ++k) t[k] = tr32(s[k], s[k]);
        if (lane < 32) st_sc1(rs, WS_OP + u * 512 + lane * 16, nosent4(t));
      }
      ST_(6);
    }
  } else {
    const int w = wave - 8;
    int zero_v;
    asm volatile("v_mov_b32 %0, 0" : "=v"(zero_v));
    const int r8 = lane >> 3, c8 = lane & 7;
    const bool m1 = b < 100;
    const int cg = b % 20, rg = b / 20;
    u32x4 wm[4]; u32x2 cw;
    if (m1) {
      const int row0 = 128 * rg + 32 * w + 4 * r8;
#pragma unroll
      for (int i = 0; i < 4; ++i) wm[i] = ldnt(wmod + (size_t)(row0 + i) * (6 * D) + cg * 64 + c8 * 8);
      cw = *(const u32x2*)(cnd + row0);
    }
    const u32x2 Ev = ld_sc1_64(rs, WS_EPOCH + zero_v);
    unsigned xw[5];
#pragma unroll
    for (int i = 0; i < 5; ++i) xw[i] = ((const unsigned*)x)[lane + 64 * i];
    const int l20 = (lane & 31) < 20 ? (lane & 31) : 19;
    const int hrow = 80 * j + 20 * w + l20;
    const float xr = bflo((unsigned)x[hrow]);
    if (w == 3) {
      if (lane < 32) lds_put(L, lane, 0u);
      const u32x4 sv = {SENT, SENT, SENT, SENT};
      st_sc1(rs, (lane < 32 ? WS_QP : WS_OP) + u * 512 + (lane & 31) * 16, sv);
      int o2 = 0x7FFFFF00;
      if (lane == 63) o2 = WS_ST + u * 128;
      else if (b < 80) { if (lane < 16) o2 = WS_YP + ((b & 7) * 640 + (b >> 3) * 64) * 4 + lane * 16; }
      else if (b < 160) { if (lane < 8) o2 = WS_M2 + (b - 80) * 128 + lane * 16; }
      else if (b < 160 + NF) { if (lane < 32) o2 = WS_ZP + (b - 160) * 2560 + 2048 + lane * 16; }
      st_sc1(rs, o2, sv);
      if (b >= 160 && b < 160 + NF) {
        st_sc1(rs, WS_ZP + (b - 160) * 2560 + lane * 16, sv);
        st_sc1(rs, WS_ZP + (b - 160) * 2560 + 1024 + lane * 16, sv);
      }
    }
    SB();
    LGKM0();
    __builtin_amdgcn_s_barrier();
    SB();
    const unsigned tag = bij32(Ev[0]), flo = bij32(Ev[0] ^ Ev[1] ^ 0x5bd1e995u);
    if (m1) {
      const unsigned s0 = pack2(silu_bf(bflo(cw[0])), silu_bf(bfhi(cw[0]))), s1 = pack2(silu_bf(bflo(cw[1])), silu_bf(bfhi(cw[1])));
      float acc[8] = {0.f, 0.f, 0.f, 0.f, 0.f, 0.f, 0.f, 0.f};
      mac_pair(acc, wm[0], wm[1], s0);
      mac_pair(acc, wm[2], wm[3], s1);
      const float y = red8_b345(acc, lane);
      const int col = c8 * 8 + ((lane >> 3) & 1) * 4 + ((lane >> 4) & 1) * 2 + (lane >> 5);
      LAS float* MR = (LAS float*)(L + L_MR);
      MR[w * 64 + col] = y;
      if (lds_arrive(C_MOD1) == 3u) {
        const float s = (MR[lane] + MR[64 + lane]) + (MR[128 + lane] + MR[192 + lane]);
        __hip_atomic_store((gu64*)(ws + WS_MP1) + rg * 1280 + cg * 64 + lane, ((u64)tag << 32) | (u64)fb(s), RLX_AGENT);
      }
    }
    ST_(1);
    float nrm;
    {
      float s = 0.f;
#pragma unroll
      for (int i = 0; i < 5; ++i) s += bflo(xw[i]) + bfhi(xw[i]);
      const float mean = wave_sum(s) * (1.f / 640.f);
      float q2 = 0.f;
#pragma unroll
      for (int i = 0; i < 5; ++i) { const float d0 = bflo(xw[i]) - mean, d1 = bfhi(xw[i]) - mean; q2 += d0 * d0 + d1 * d1; }
      const float var = wave_sum(q2) * (1.f / 640.f);
      const float den = rbf(sqrtf(rbf(rbf(var) + 1e-5f)));
      nrm = rbf(rbf(xr - rbf(mean)) / den);
    }
    if (w == 3) {
      vmwait<0>();
      if (lane == 0) __hip_atomic_store((gu64*)(ws + WS_FLAGS) + b, ((u64)tag << 32) | (u64)flo, RLX_AGENT);
    }
    {
      u32x4 wqv[5];
#pragma unroll
      for (int i = 0; i < 5; ++i) wqv[i] = ldnt(wq + (size_t)(80 * j + 20 * w + 5 * r4 + i) * (HQ * DH) + h * DH + c16 * 8);
      SB();
      gu64* mp = (gu64*)(ws + WS_MP1) + (lane >> 5) * 640 + hrow;
      u64 g[5];
      lds_wait(L, C_PROG + w, 1u);
      NOUNROLL for (unsigned s = 0; s < SPIN_G; ++s) {
        bool ok = true;
#pragma unroll
        for (int i = 0; i < 5; ++i) g[i] = __hip_atomic_load(mp + i * 1280, RLX_AGENT);
#pragma unroll
        for (int i = 0; i < 5; ++i) ok = ok & ((unsigned)(g[i] >> 32) == tag);
        CNT_(0);
        if (__all(ok)) break;
        __builtin_amdgcn_s_sleep(PSLEEP);
      }
      const float mv = rbf(((bf((unsigned)g[0]) + bf((unsigned)g[1])) + (bf((unsigned)g[2]) + bf((unsigned)g[3]))) + bf((unsigned)g[4]));
      auto sw = __builtin_amdgcn_permlane32_swap(fb(mv), fb(mv), false, false);
      const float hval = rbf(rbf(nrm * rbf(1.f + bf(sw[1]))) + bf(sw[0]));
      ST_(2);
      float acc[8] = {0.f, 0.f, 0.f, 0.f, 0.f, 0.f, 0.f, 0.f};
#pragma unroll
      for (int i = 0; i < 5; ++i) {
        const float hk = __shfl(hval, 5 * r4 + i);
#pragma unroll
        for (int d = 0; d < 4; ++d) { acc[2 * d] = fmaf(hk, bflo(wqv[i][d]), acc[2 * d]); acc[2 * d + 1] = fmaf(hk, bfhi(wqv[i][d]), acc[2 * d + 1]); }
      }
      float v[4];
#pragma unroll
      for (int k = 0; k < 4; ++k) v[k] = tr16(acc[k], acc[k + 4]);
      const float y0 = tr32(v[0], v[2]), y1 = tr32(v[1], v[3]);
      LAS float* QR = (LAS float*)(L + L_QR);
      *(LAS f32x2*)(QR + w * 128 + c16 * 8 + (r4 & 1) * 4 + (r4 >> 1) * 2) = f32x2{y0, y1};
      if (lds_arrive(C_QP) == 3u) {
        if (lane < 32) st_sc1(rs, WS_QP + u * 512 + lane * 16, nosent4(sum4(QR + 4 * lane, 128)));
      }
    }
    ST_(3);
    if (w == 0) {
      lds_wait(L, C_PROG, 2u);
      NOUNROLL for (unsigned s = 0; s < SPIN_G; ++s) {
        MEMBAR();
        const u32x4 g0 = ld_sc1(rs, WS_FLAGS + lane * 16), g1 = ld_sc1(rs, WS_FLAGS + 1024 + lane * 16);
        const unsigned bad = ((g0[0] ^ flo) | (g0[1] ^ tag)) | ((g0[2] ^ flo) | (g0[3] ^ tag)) | ((g1[0] ^ flo) | (g1[1] ^ tag)) | ((g1[2] ^ flo) | (g1[3] ^ tag));
        if (__all(bad == 0u)) break;
        __builtin_amdgcn_s_sleep(PSLEEP);
      }
      u32x4 a0, a1, b0, b1;
      const int off = WS_QP + (h * 8 + 2 * r4) * 512 + c16 * 32;
      lds_wait(L, C_PROG, 3u);
      NOUNROLL for (unsigned s = 0; s < SPIN_G; ++s) {
        MEMBAR();
        a0 = ld_sc1(rs, off); a1 = ld_sc1(rs, off + 16); b0 = ld_sc1(rs, off + 512); b1 = ld_sc1(rs, off + 528);
        CNT_(16);
        if (__all(max(max(mx4(a0), mx4(a1)), max(mx4(b0), mx4(b1))) != SENT)) break;
        __builtin_amdgcn_s_sleep(PSLEEP);
      }
      float v[4];
#pragma unroll
      for (int k = 0; k < 4; ++k) v[k] = tr16(bf(a0[k]) + bf(b0[k]), bf(a1[k]) + bf(b1[k]));
      const float y0 = tr32(v[0], v[2]), y1 = tr32(v[1], v[3]);
      ((LAS unsigned*)(L + L_Q))[c16 * 4 + (r4 & 1) * 2 + (r4 >> 1)] = pack2(y0, y1);
      MEMBAR();
      lds_put(L, C_QREADY, 1u);
    } else {
      lds_wait(L, C_QREADY, 1u);
    }
    ST_(4);
    if (b < 80) {
      const int g = b & 7, cgp = b >> 3, hw = 4 * g + w;
      u32x4 wt[16];
      lds_wait8(L, 4u);
      const u16* wb = wo + (size_t)(hw * 128 + 16 * r8) * D + cgp * 64 + c8 * 8;
#pragma unroll
      for (int i = 0; i < 16; ++i) {
        wt[i] = ldnt(wb + i * D);
        if (i >= TDEP && i < 15) vmwait<TDEP>();
        SB();
      }
      ST_(5);
      float wj[4];
      {
        u32x2 ml;
        NOUNROLL for (unsigned s = 0; s < SPIN_G; ++s) {
          MEMBAR();
          ml = ld_sc1_64(rs, WS_ST + (hw * 8 + c8) * 128);
          if (__all(max(ml[0], ml[1]) != SENT)) break;
        }
        float M = bf(ml[0]);
        M = fmaxf(M, dpp<XOR1>(M)); M = fmaxf(M, dpp<XOR2>(M)); M = fmaxf(M, dpp<XOR7>(M));
        const float e = __expf(bf(ml[0]) - M);
        float Ls = bf(ml[1]) * e;
        Ls += dpp<XOR1>(Ls); Ls += dpp<XOR2>(Ls); Ls += dpp<XOR7>(Ls);
        const float wgt = e / Ls;
#pragma unroll
        for (int i = 0; i < 4; ++i) wj[i] = __shfl(wgt, 4 * (lane >> 5) + i);
      }
#pragma unroll
      for (int m = 0; m < 8; ++m) { prepack(wt[2 * m], wt[2 * m + 1]); pin(wt[2 * m]); pin(wt[2 * m + 1]); }
      u32x4 a[4];
      const int off = WS_OP + (hw * 8 + 4 * (lane >> 5)) * 512 + (lane & 31) * 16;
      NOUNROLL for (unsigned s = 0; s < SPIN_G; ++s) {
        MEMBAR();
#pragma unroll
        for (int i = 0; i < 4; ++i) a[i] = ld_sc1(rs, off + i * 512);
        if (__all(max(max(mx4(a[0]), mx4(a[1])), max(mx4(a[2]), mx4(a[3]))) != SENT)) break;
      }
      ST_(6);
      float o4[4];
#pragma unroll
      for (int k = 0; k < 4; ++k) {
        const float t = fmaf(wj[0], bf(a[0][k]), wj[1] * bf(a[1][k])) + fmaf(wj[2], bf(a[2][k]), wj[3] * bf(a[3][k]));
        o4[k] = tr32(t, t);
      }
      LAS unsigned* OB = (LAS unsigned*)(L + L_OB) + w * 64;
      *(LAS u32x2*)(OB + 2 * (lane & 31)) = u32x2{pack2(o4[0], o4[1]), pack2(o4[2], o4[3])};
      MEMBAR();
      const u32x4 oa = *(volatile LAS u32x4*)(OB + 8 * r8), ob = *(volatile LAS u32x4*)(OB + 8 * r8 + 4);
      float acc[8] = {0.f, 0.f, 0.f, 0.f, 0.f, 0.f, 0.f, 0.f};
#pragma unroll
      for (int m = 0; m < 4; ++m) mac_packed(acc, wt[2 * m], wt[2 * m + 1], oa[m]);
#pragma unroll
      for (int m = 0; m < 4; ++m) mac_packed(acc, wt[8 + 2 * m], wt[8 + 2 * m + 1], ob[m]);
      const float y = red8_b345(acc, lane);
      const int col = c8 * 8 + ((lane >> 3) & 1) * 4 + ((lane >> 4) & 1) * 2 + (lane >> 5);
      LAS float* YR = (LAS float*)(L + L_YR);
      YR[w * 64 + col] = y;
      HBAR();
      if (w == 0 && lane < 16) st_sc1(rs, WS_YP + (g * 640 + cgp * 64 + 4 * lane) * 4, nosent4(sum4(YR + 4 * lane, 64)));
      ST_(7);
    } else if (b < 160) {
      const int m2 = b - 80;
      const int r16 = lane >> 2, c4 = lane & 3;
      const int row0 = 160 * w + 10 * r16;
      unsigned sp[5];
#pragma unroll
      for (int i = 0; i < 5; ++i) {
        const unsigned cw2 = *(const unsigned*)(cnd + row0 + 2 * i);
        sp[i] = pack2(silu_bf(bflo(cw2)), silu_bf(bfhi(cw2)));
      }
      u32x4 wt[10];
      lds_wait8(L, 4u);
      const u16* wb = wmod + (size_t)row0 * (6 * D) + 1280 + m2 * 32 + c4 * 8;
#pragma unroll
      for (int i = 0; i < 10; ++i) {
        wt[i] = ldnt(wb + (size_t)i * (6 * D));
        if (i >= TDEP && i < 9) vmwait<TDEP>();
        SB();
      }
      ST_(5);
      float acc[8] = {0.f, 0.f, 0.f, 0.f, 0.f, 0.f, 0.f, 0.f};
#pragma unroll
      for (int m = 0; m < 5; ++m) mac_pair(acc, wt[2 * m], wt[2 * m + 1], sp[m]);
      float y = red8_b345(acc, lane);
      y += xor4(y);
      ST_(6);
      const int col = c4 * 8 + ((lane >> 3) & 1) * 4 + ((lane >> 4) & 1) * 2 + (lane >> 5);
      LAS float* YR = (LAS float*)(L + L_YR);
      YR[w * 64 + col] = y;
      HBAR();
      if (w == 0 && lane < 8) {
        const f32x4 s = sum4(YR + 4 * lane, 64);
        st_sc1(rs, WS_M2 + (m2 * 32 + 4 * lane) * 4, nosent4(f32x4{rbf(s[0]), rbf(s[1]), rbf(s[2]), rbf(s[3])}));
      }
      ST_(7);
    } else if (b < 160 + NF) {
      const int f = b - 160;
      const int r16 = lane >> 2, c4 = lane & 3;
      const int row0 = 160 * w + 10 * r16;
      const int t = w * 64 + lane;
      const int tq = t < 160 ? t : 159;
      const bool fin = f < 20 && w == 3;
      const int oc = (f < 20 ? 32 * f : 0) + 4 * c8 + ((lane >> 4) & 1) * 2 + (lane >> 5);
      const u32x2 xq = *(const u32x2*)(x + 4 * tq);
      LAS float* X1 = (LAS float*)(L + L_X1);
      u32x4 wt[18]; unsigned w2t[8];
      lds_wait8(L, 4u);
      const u16* wb = w1 + (size_t)row0 * DFF + f * 32 + c4 * 8;
      const u16* w2b = w2 + (size_t)(f * 32 + 8 * w) * D;
#pragma unroll
      for (int i = 0; i < 18; ++i) {
        if (i < 10) wt[i] = ldnt(wb + (size_t)i * DFF);
        else {
          wt[i] = ldnt(w2b + (i - 10) * D + lane * 8);
          w2t[i - 10] = __builtin_nontemporal_load((const unsigned*)(w2b + (i - 10) * D + 512 + lane * 2));
        }
        if (i >= TDEP && i < 17) vmwait<TDEP>();
        SB();
      }
      ST_(5);
      u32x4 ga; u32x2 sbA, sbB, cbA, cbB; unsigned gbw;
      NOUNROLL for (unsigned s = 0; s < SPIN_G; ++s) {
        MEMBAR();
        ga = ld_sc1(rs, WS_M2 + tq * 16);
        gbw = __builtin_amdgcn_raw_buffer_load_b32(rs, WS_M2 + 7680 + oc * 4, 0, 16);
        sbA = ld_sc1_64(rs, WS_M2 + 2560 + (row0 + 2 * c4) * 4); sbB = ld_sc1_64(rs, WS_M2 + 2560 + (row0 + 8) * 4);
        cbA = ld_sc1_64(rs, WS_M2 + 5120 + (row0 + 2 * c4) * 4); cbB = ld_sc1_64(rs, WS_M2 + 5120 + (row0 + 8) * 4);
        const unsigned mm = max(max(mx4(ga), gbw), max(max(max(sbA[0], sbA[1]), max(sbB[0], sbB[1])), max(max(cbA[0], cbA[1]), max(cbB[0], cbB[1]))));
        if (__all(mm != SENT)) break;
        __builtin_amdgcn_s_sleep(2);
      }
      float c1[4] = {rbf(1.f + bf(cbA[0])), rbf(1.f + bf(cbA[1])), rbf(1.f + bf(cbB[0])), rbf(1.f + bf(cbB[1]))};
      asm volatile("" : "+v"(c1[0]), "+v"(c1[1]), "+v"(c1[2]), "+v"(c1[3]));
      const float s1[4] = {bf(sbA[0]), bf(sbA[1]), bf(sbB[0]), bf(sbB[1])};
#pragma unroll
      for (int m = 0; m < 9; ++m) { prepack(wt[2 * m], wt[2 * m + 1]); pin(wt[2 * m]); pin(wt[2 * m + 1]); }
#pragma unroll
      for (int m = 0; m < 4; ++m) {
        const unsigned lo = __builtin_amdgcn_perm(w2t[2 * m + 1], w2t[2 * m], 0x05040100u), hi = __builtin_amdgcn_perm(w2t[2 * m + 1], w2t[2 * m], 0x07060302u);
        w2t[2 * m] = lo; w2t[2 * m + 1] = hi;
        asm volatile("" : "+v"(w2t[2 * m]), "+v"(w2t[2 * m + 1]));
      }
      if (w < 3) {
        u32x4 a[8];
        NOUNROLL for (unsigned s = 0; s < SPIN_G; ++s) {
          MEMBAR();
#pragma unroll
          for (int i = 0; i < 8; ++i) a[i] = ld_sc1(rs, WS_YP + i * 2560 + tq * 16);
          unsigned mm = mx4(a[0]);
#pragma unroll
          for (int i = 1; i < 8; ++i) mm = max(mm, mx4(a[i]));
          if (__all(mm != SENT)) break;
        }
        ST_(6);
        const float xv[4] = {bflo(xq[0]), bfhi(xq[0]), bflo(xq[1]), bfhi(xq[1])};
        f32x4 x1;
#pragma unroll
        for (int k = 0; k < 4; ++k) {
          const float at = ((bf(a[0][k]) + bf(a[1][k])) + (bf(a[2][k]) + bf(a[3][k]))) + ((bf(a[4][k]) + bf(a[5][k])) + (bf(a[6][k]) + bf(a[7][k])));
          x1[k] = rbf(xv[k] + rbf(bf(ga[k]) * rbf(at)));
        }
        *(LAS f32x4*)(X1 + 4 * tq) = x1;
      }
      HBAR();
      unsigned hp5[5];
      {
        const f32x4 xa = *(LAS f32x4*)(X1 + 8 * lane), xb = *(LAS f32x4*)(X1 + 8 * lane + 4);
        const f32x2 xc = *(LAS f32x2*)(X1 + 512 + 2 * lane);
        const f32x2 hA = *(LAS f32x2*)(X1 + row0 + 2 * c4), hB = *(LAS f32x2*)(X1 + row0 + 8);
        const float mean = wave_sum((((xa[0] + xa[1]) + (xa[2] + xa[3])) + ((xb[0] + xb[1]) + (xb[2] + xb[3]))) + (xc[0] + xc[1])) * (1.f / 640.f);
        float q0 = 0.f, q1 = 0.f;
#pragma unroll
        for (int k = 0; k < 4; ++k) { const float d0 = xa[k] - mean, d1 = xb[k] - mean; q0 = fmaf(d0, d0, q0); q1 = fmaf(d1, d1, q1); }
        { const float d0 = xc[0] - mean, d1 = xc[1] - mean; q0 = fmaf(d0, d0, q0); q1 = fmaf(d1, d1, q1); }
        const float var = wave_sum(q0 + q1) * (1.f / 640.f);
        const float mr = rbf(mean);
        const float rden = __builtin_amdgcn_rcpf(rbf(__builtin_amdgcn_sqrtf(rbf(rbf(var) + 1e-5f))));
        const float xr2[4] = {hA[0], hA[1], hB[0], hB[1]};
        float hv[4];
#pragma unroll
        for (int k = 0; k < 4; ++k) hv[k] = rbf(rbf(rbf(xr2[k] - mr) * rden) * c1[k]) + s1[k];
        const unsigned pA = pack2(hv[0], hv[1]);
        hp5[4] = pack2(hv[2], hv[3]);
        hp5[0] = __builtin_amdgcn_mov_dpp((int)pA, 0x00, 0xf, 0xf, true);
        hp5[1] = __builtin_amdgcn_mov_dpp((int)pA, 0x55, 0xf, 0xf, true);
        hp5[2] = __builtin_amdgcn_mov_dpp((int)pA, 0xAA, 0xf, 0xf, true);
        hp5[3] = __builtin_amdgcn_mov_dpp((int)pA, 0xFF, 0xf, 0xf, true);
      }
      ST_(7);
      {
        float acc[8] = {0.f, 0.f, 0.f, 0.f, 0.f, 0.f, 0.f, 0.f};
#pragma unroll
        for (int m = 0; m < 5; ++m) mac_packed(acc, wt[2 * m], wt[2 * m + 1], hp5[m]);
        float y = red8_b345(acc, lane);
        y += xor4(y);
        const int col = c4 * 8 + ((lane >> 3) & 1) * 4 + ((lane >> 4) & 1) * 2 + (lane >> 5);
        LAS float* YR = (LAS float*)(L + L_YR);
        YR[w * 64 + col] = y;
        HBAR();
        unsigned hp[4];
        {
          const f32x4 s0 = sum4(YR + 8 * w, 64), s1v = sum4(YR + 8 * w + 4, 64);
          auto relu = [](float v) { return v < 0.f ? 0.f : v; };
          hp[0] = pack2(relu(s0[0]), relu(s0[1])); hp[1] = pack2(relu(s0[2]), relu(s0[3]));
          hp[2] = pack2(relu(s1v[0]), relu(s1v[1])); hp[3] = pack2(relu(s1v[2]), relu(s1v[3]));
        }
        float z[8] = {0.f, 0.f, 0.f, 0.f, 0.f, 0.f, 0.f, 0.f};
        float z8 = 0.f, z9 = 0.f;
#pragma unroll
        for (int m = 0; m < 4; ++m) {
          mac_packed(z, wt[10 + 2 * m], wt[10 + 2 * m + 1], hp[m]);
          z8 = dot2(w2t[2 * m], hp[m], z8);
          z9 = dot2(w2t[2 * m + 1], hp[m], z9);
        }
        LAS float* ZR = (LAS float*)(L + L_ZR);
        *(LAS f32x4*)(ZR + w * 640 + 8 * lane) = f32x4{z[0], z[1], z[2], z[3]};
        *(LAS f32x4*)(ZR + w * 640 + 8 * lane + 4) = f32x4{z[4], z[5], z[6], z[7]};
        *(LAS f32x2*)(ZR + w * 640 + 512 + 2 * lane) = f32x2{z8, z9};
        HBAR();
        if (t < 160) st_sc1(rs, WS_ZP + f * 2560 + t * 16, nosent4(sum4(ZR + 4 * t, 640)));
      }
      ST_(8);
      if (fin) {
        u32x4 zp[7];
        const int zo = WS_ZP + r8 * 2560 + f * 128 + c8 * 16;
        const int z6 = r8 < NF - 48 ? zo + 48 * 2560 : 0x7FFFFF00;
        const float xo = X1[oc];
        NOUNROLL for (unsigned s = 0; s < SPIN_G; ++s) {
          MEMBAR();
#pragma unroll
          for (int i = 0; i < 6; ++i) zp[i] = ld_sc1(rs, zo + i * 8 * 2560);
          zp[6] = ld_sc1(rs, z6);
          unsigned mm = mx4(zp[0]);
#pragma unroll
          for (int i = 1; i < 7; ++i) mm = max(mm, mx4(zp[i]));
          if (__all(mm != SENT)) break;
        }
        ST_(9);
        float r[4];
#pragma unroll
        for (int k = 0; k < 4; ++k) r[k] = ((bf(zp[0][k]) + bf(zp[1][k])) + (bf(zp[2][k]) + bf(zp[3][k]))) + ((bf(zp[4][k]) + bf(zp[5][k])) + bf(zp[6][k]));
        float v = tr32(tr16(r[0], r[2]), tr16(r[1], r[3]));
        v += dpp<XOR8>(v);
        out[oc] = (u16)f2bf(xo + rbf(bf(gbw) * rbf(v)));
        if (f == 0 && lane == 0) {
          const u64 e1 = (((u64)Ev[1] << 32) | (u64)Ev[0]) + 1ull;
          st_sc1_64(rs, WS_EPOCH, u32x2{(unsigned)e1, (unsigned)(e1 >> 32)});
        }
        ST_(10);
      }
    }
  }
}

extern "C" void kernel_launch(void* const* d_in, const int* in_sizes, int n_in, void* d_out, int out_size, void* d_ws,
                              size_t ws_size, hipStream_t stream) {
  (void)in_sizes; (void)n_in; (void)out_size; (void)ws_size;
  (void)hipFuncSetAttribute((const void*)mega, hipFuncAttributeMaxDynamicSharedMemorySize, LDS_BYTES);
  hipLaunchKernelGGL(mega, dim3(256), dim3(768), LDS_BYTES, stream, (const u16*)d_in[0], (const u16*)d_in[1], (const u16*)d_in[2],
                     (const u16*)d_in[3], (const u16*)d_in[4], (const u16*)d_in[6], (const u16*)d_in[7], (const u16*)d_in[8],
                     (const u16*)d_in[9], (u16*)d_out, (unsigned char*)d_ws);
}
#define STAMP_CLS(b) ((b) < 80 ? 2 : (b) < 160 ? 3 : (b) < 160 + NF ? 4 : 0)
#define NWAVES 12
#define NSTREAM 8
```

```cpp
#include <hip/hip_runtime.h>
#include <hip/hip_bf16.h>
#include <stdint.h>

#define STAMP 1
#define PSLEEP 16
#define I1 12
#define I2 21
#define I3 17
#define IT 31
#define IS 31
#define ITK 24
#define PREB 1
#define IB 25
#define TA_M2 0
#define TA_WO 2
#define TA_FFN 2
#define TDEPF 40
#define TSTART(m) do { if ((m) == 2) lds_wait(L, C_PV, 9u); else if ((m) == 1) lds_wait8(L, IT); } while (0)
#define MP10 1
#define T1 400
#define T3 560
#define T2 740
#define TM2 150
#define DEPA 1
#define ISW 14
#define WQE 1
#define DEP 2
#define TDEP 40

#define GAS __attribute__((address_space(1)))
#define LAS __attribute__((address_space(3)))
#define RLX_AGENT __ATOMIC_RELAXED, __HIP_MEMORY_SCOPE_AGENT
typedef unsigned short u16;
typedef unsigned long long u64;
typedef unsigned u32x4 __attribute__((ext_vector_type(4)));
typedef unsigned u32x2 __attribute__((ext_vector_type(2)));
typedef float f32x4 __attribute__((ext_vector_type(4)));
typedef float f32x2 __attribute__((ext_vector_type(2)));
typedef __bf16 bf16x2 __attribute__((ext_vector_type(2)));
typedef GAS u64 gu64;
typedef __amdgpu_buffer_rsrc_t rsrc_t;

constexpr int D = 640, HQ = 32, SK = 16384, DH = 128, WIN = 4096, DFF = 1728;
constexpr int NF = 54;

constexpr int WS_EPOCH = 0;
constexpr int WS_FLAGS = 4096;
constexpr int WS_TICKET = 2048;
constexpr int NMP = 4;
constexpr int WS_MP = 8192;
constexpr int WS_QP = 131072;
constexpr int WS_OP = WS_QP + 131072;
constexpr int WS_ST = WS_OP + 131072;
constexpr int WS_YP = WS_ST + 32768;
constexpr int WS_M2 = WS_YP + 20480;
constexpr int WS_ZP = WS_M2 + 10240;
constexpr int WS_END = WS_ZP + NF * 2560;
constexpr int N_M2 = 0, N_WO = 80;
constexpr int WS_STAMP = 1 << 20;

constexpr unsigned SENT = 0xFFFFFFFFu;
constexpr unsigned SPIN_G = 1u << 18;
constexpr unsigned SPIN_L = 1u << 22;

constexpr int C_PROG = 0;
constexpr int C_MOD1 = 8, C_QP = 9, C_QREADY = 10, C_M = 11, C_ROLE = 12, C_PV = 13, C_FLAGOK = 14;
constexpr int L_P = 128;
constexpr int L_MR = L_P + 1024;
constexpr int L_QR = L_MR + 1024;
constexpr int L_OR = L_QR + 2048;
constexpr int L_OB = L_OR + 4096;
constexpr int L_YR = L_OB + 1024;
constexpr int L_STAT = L_YR + 1024;
constexpr int L_Q = L_STAT + 64;
constexpr int L_X1 = L_Q + 256;
constexpr int L_ZR = L_X1 + 2560;
constexpr int L_END = L_ZR + 10240;
constexpr int LDS_BYTES = 96 * 1024;
static_assert(L_END <= LDS_BYTES, "lds");

__device__ __forceinline__ float bflo(unsigned w) { return __builtin_bit_cast(float, w << 16); }
__device__ __forceinline__ float bfhi(unsigned w) { return __builtin_bit_cast(float, w & 0xffff0000u); }
__device__ __forceinline__ unsigned f2bf(float f) { return (unsigned)__builtin_bit_cast(u16, (__bf16)f); }
__device__ __forceinline__ float rbf(float f) { return bflo(f2bf(f)); }
__device__ __forceinline__ unsigned pack2(float lo, float hi) {
  bf16x2 v = {(__bf16)lo, (__bf16)hi};
  return __builtin_bit_cast(unsigned, v);
}
__device__ __forceinline__ float dot2(unsigned a, unsigned b, float acc) {
  return __builtin_amdgcn_fdot2_f32_bf16(__builtin_bit_cast(bf16x2, a), __builtin_bit_cast(bf16x2, b), acc, false);
}
__device__ __forceinline__ unsigned fb(float f) { return __builtin_bit_cast(unsigned, f); }
__device__ __forceinline__ float bf(unsigned u) { return __builtin_bit_cast(float, u); }
__device__ __forceinline__ unsigned nosent(float f) { return min(fb(f), 0xFFFFFFFEu); }
__device__ __forceinline__ u32x4 nosent4(f32x4 s) { return u32x4{nosent(s[0]), nosent(s[1]), nosent(s[2]), nosent(s[3])}; }
template <int CTRL> __device__ __forceinline__ float dpp(float x) {
  return __builtin_bit_cast(float, __builtin_amdgcn_mov_dpp(__builtin_bit_cast(int, x), CTRL, 0xf, 0xf, true));
}
constexpr int XOR1 = 0xB1, XOR2 = 0x4E, XOR7 = 0x141, XOR8 = 0x128;
__device__ __forceinline__ float xor4(float x) {
  int v = __builtin_bit_cast(int, x);
  int r = __builtin_amdgcn_update_dpp(0, v, 0x114, 0xf, 0xa, false);
  r = __builtin_amdgcn_update_dpp(r, v, 0x104, 0xf, 0x5, false);
  return __builtin_bit_cast(float, r);
}
__device__ __forceinline__ float tr16(float a, float b) {
  auto s = __builtin_amdgcn_permlane16_swap(fb(a), fb(b), false, false);
  return bf(s[0]) + bf(s[1]);
}
__device__ __forceinline__ float tr32(float a, float b) {
  auto s = __builtin_amdgcn_permlane32_swap(fb(a), fb(b), false, false);
  return bf(s[0]) + bf(s[1]);
}
__device__ __forceinline__ float mx16(float a) {
  auto s = __builtin_amdgcn_permlane16_swap(fb(a), fb(a), false, false);
  return fmaxf(bf(s[0]), bf(s[1]));
}
__device__ __forceinline__ float mx32(float a) {
  auto s = __builtin_amdgcn_permlane32_swap(fb(a), fb(a), false, false);
  return fmaxf(bf(s[0]), bf(s[1]));
}
__device__ __forceinline__ float row_sum(float x) {
  x += dpp<XOR1>(x); x += dpp<XOR2>(x); x += dpp<XOR7>(x); x += dpp<XOR8>(x); return x;
}
__device__ __forceinline__ float row_max(float x) {
  x = fmaxf(x, dpp<XOR1>(x)); x = fmaxf(x, dpp<XOR2>(x)); x = fmaxf(x, dpp<XOR7>(x)); x = fmaxf(x, dpp<XOR8>(x)); return x;
}
__device__ __forceinline__ float wave_sum(float x) { x = row_sum(x); x = tr16(x, x); return tr32(x, x); }
__device__ __forceinline__ float wave_max(float x) { x = row_max(x); x = mx16(x); return mx32(x); }

__device__ __forceinline__ unsigned bij32(unsigned x) {
  x += 0x9E3779B9u; x ^= x >> 16; x *= 0x85EBCA6Bu; x ^= x >> 13; x *= 0xC2B2AE35u; x ^= x >> 16; return x;
}
__device__ __forceinline__ rsrc_t mkrs(const void* p, int bytes) { return __builtin_amdgcn_make_buffer_rsrc((void*)p, 0, bytes, 0x00020000); }
__device__ __forceinline__ u32x4 ldnt(const void* p) { return __builtin_nontemporal_load(reinterpret_cast<const u32x4*>(p)); }
__device__ __forceinline__ u32x4 ld_sc1(rsrc_t rs, int off) { return __builtin_amdgcn_raw_buffer_load_b128(rs, off, 0, 16); }
__device__ __forceinline__ u32x2 ld_sc1_64(rsrc_t rs, int off) { return __builtin_amdgcn_raw_buffer_load_b64(rs, off, 0, 16); }
__device__ __forceinline__ void st_sc1(rsrc_t rs, int off, u32x4 v) { __builtin_amdgcn_raw_buffer_store_b128(v, rs, off, 0, 16); }
__device__ __forceinline__ void st_sc1_64(rsrc_t rs, int off, u32x2 v) { __builtin_amdgcn_raw_buffer_store_b64(v, rs, off, 0, 16); }
__device__ __forceinline__ unsigned mx4(u32x4 v) { return max(max(v[0], v[1]), max(v[2], v[3])); }
#define MEMBAR() asm volatile("" ::: "memory")
#define LGKM0() asm volatile("s_waitcnt lgkmcnt(0)" ::: "memory")
#define SB() __builtin_amdgcn_sched_barrier(0)
#define NOUNROLL _Pragma("nounroll")

__device__ __forceinline__ void lds_put(LAS unsigned char* L, int word, unsigned v) { ((volatile LAS unsigned*)L)[word] = v; }
__device__ __forceinline__ void lds_wait(LAS unsigned char* L, int word, unsigned v) {
  NOUNROLL for (unsigned s = 0; s < SPIN_L; ++s) {
    if (((volatile LAS unsigned*)L)[word] >= v) break;
    __builtin_amdgcn_s_sleep(1);
  }
  MEMBAR();
}
__device__ __forceinline__ void lds_wait4(LAS unsigned char* L, int word, unsigned v) {
  NOUNROLL for (unsigned s = 0; s < SPIN_L; ++s) {
    const u32x4 q = *(volatile LAS u32x4*)(L + word * 4);
    if (min(min(q[0], q[1]), min(q[2], q[3])) >= v) break;
    __builtin_amdgcn_s_sleep(1);
  }
  MEMBAR();
}
__device__ __forceinline__ void lds_spin(LAS unsigned char* L, int word, unsigned v) {
  NOUNROLL for (unsigned s = 0; s < SPIN_L; ++s) {
    if (((volatile LAS unsigned*)L)[word] >= v) break;
  }
  MEMBAR();
}
__device__ __forceinline__ unsigned lds_arrive(int word) {
  unsigned old;
  asm volatile("s_mov_b64 exec, 1\n\tds_add_rtn_u32 %0, %1, %2\n\ts_mov_b64 exec, -1\n\ts_waitcnt lgkmcnt(0)"
               : "=&v"(old) : "v"(word * 4), "v"(1u) : "memory");
  return __builtin_amdgcn_readfirstlane(old);
}
__device__ __forceinline__ void lds_arrive_nr(int word) {
  asm volatile("s_mov_b64 exec, 1\n\tds_add_u32 %0, %1\n\ts_mov_b64 exec, -1" :: "v"(word * 4), "v"(1u) : "memory");
}
__device__ __forceinline__ void lds_wait8(LAS unsigned char* L, unsigned v) {
  NOUNROLL for (unsigned s = 0; s < SPIN_L; ++s) {
    const u32x4 q = *(volatile LAS u32x4*)L, r = *(volatile LAS u32x4*)(L + 16);
    if (min(min(min(q[0], q[1]), min(q[2], q[3])), min(min(r[0], r[1]), min(r[2], r[3]))) >= v) break;
    __builtin_amdgcn_s_sleep(2);
  }
  MEMBAR();
}
#define HBAR() do { LGKM0(); __builtin_amdgcn_s_barrier(); MEMBAR(); } while (0)
__device__ __forceinline__ void wait_until(u64 t) {
  NOUNROLL for (unsigned s = 0; s < 4096u; ++s) {
    if (__builtin_amdgcn_s_memrealtime() >= t) break;
    __builtin_amdgcn_s_sleep(2);
  }
  MEMBAR();
}
template <int N> __device__ __forceinline__ void vmwait() { asm volatile("s_waitcnt vmcnt(%0)" :: "i"(N) : "memory"); }

#define ST_(k) do { SB(); asm volatile("s_memrealtime %0\n\ts_waitcnt lgkmcnt(0)" : "=s"(stamp[k]) :: "memory"); SB(); } while (0)
#define CNT_(sh) do { stamp[11] += 1ull << (sh); } while (0)

__device__ __forceinline__ float red8_b345(const float (&a)[8], int lane) {
  float v[4];
  const bool hi = (lane & 8) != 0;
#pragma unroll
  for (int k = 0; k < 4; ++k) {
    const float t1 = a[k] + dpp<XOR8>(a[k]), t2 = a[k + 4] + dpp<XOR8>(a[k + 4]);
    v[k] = hi ? t2 : t1;
  }
  const float u0 = tr16(v[0], v[2]), u1 = tr16(v[1], v[3]);
  return tr32(u0, u1);
}
__device__ __forceinline__ void prepack(u32x4& wa, u32x4& wb) {
#pragma unroll
  for (int d = 0; d < 4; ++d) {
    const unsigned lo = __builtin_amdgcn_perm(wb[d], wa[d], 0x05040100u);
    const unsigned hi = __builtin_amdgcn_perm(wb[d], wa[d], 0x07060302u);
    wa[d] = lo; wb[d] = hi;
  }
}
__device__ __forceinline__ void pin(u32x4& a) { asm volatile("" : "+v"(a)); }
__device__ __forceinline__ void mac_packed(float (&acc)[8], u32x4 wa, u32x4 wb, unsigned xp) {
#pragma unroll
  for (int d = 0; d < 4; ++d) { acc[2 * d] = dot2(wa[d], xp, acc[2 * d]); acc[2 * d + 1] = dot2(wb[d], xp, acc[2 * d + 1]); }
}
__device__ __forceinline__ void mac_pair(float (&acc)[8], u32x4 wa, u32x4 wb, unsigned xp) { prepack(wa, wb); mac_packed(acc, wa, wb, xp); }
__device__ __forceinline__ float silu_f(float v) { return v * __builtin_amdgcn_rcpf(1.f + __expf(-v)); }
__device__ __forceinline__ f32x4 sum4(const LAS float* p, int stride) {
  return (*(const LAS f32x4*)p + *(const LAS f32x4*)(p + stride)) + (*(const LAS f32x4*)(p + 2 * stride) + *(const LAS f32x4*)(p + 3 * stride));
}

__global__ void __launch_bounds__(768, 3)
mega(const u16* __restrict__ x, const u16* __restrict__ kc, const u16* __restrict__ vc, const u16* __restrict__ wq,
     const u16* __restrict__ wo, const u16* __restrict__ w1, const u16* __restrict__ w2, const u16* __restrict__ cnd,
     const u16* __restrict__ wmod, u16* __restrict__ out, unsigned char* ws) {
  extern __shared__ __attribute__((aligned(16))) unsigned char smem[];
  LAS unsigned char* L = (LAS unsigned char*)smem;
  const int tid = threadIdx.x, lane = tid & 63, wave = __builtin_amdgcn_readfirstlane(tid >> 6);
  const int b = blockIdx.x;
  const int h = (b & 7) * 4 + (b >> 6), j = (b >> 3) & 7, u = h * 8 + j;
  const int r4 = lane >> 4, c16 = lane & 15;
  const rsrc_t rs = mkrs(ws, WS_END);
  u64 stamp[12];
#pragma unroll
  for (int k = 0; k < 12; ++k) stamp[k] = 0;
  ST_(0);

  if (wave < 8) {
    const size_t kvoff = ((size_t)h * SK + (SK - WIN) + j * 512) * DH;
    const rsrc_t rk = mkrs(kc + kvoff, 131072), rv = mkrs(vc + kvoff, 131072);
    const int rot = (b * 8 + (b >> 4) + wave) * 1024;
    u32x4 big[32];
    u32x4 pp0, pp1;
    float mw;
    LAS float* STAT = (LAS float*)(L + L_STAT);
#define LOAD_STEP(i) do { \
      big[i] = __builtin_amdgcn_raw_buffer_load_b128((i) < 16 ? rk : rv, lane * 16, (((i) & 15) * 8192 + rot) & 131071, 2); \
      if ((i) == PREB - 1) __builtin_amdgcn_s_barrier();     \
      if ((i) == ITK || (i) == IT) lds_put(L, C_PROG + wave, (unsigned)(i)); \
      if ((i) >= 16 + DEP + 2 && (((i) - DEP) & 1) == 0 && (i) <= IB) { prepack(big[(i) - DEP - 2], big[(i) - DEP - 1]); pin(big[(i) - DEP - 2]); pin(big[(i) - DEP - 1]); } \
      if ((i) < ISW) { if ((i) >= DEPA) vmwait<DEPA>(); } else if ((i) < IB && (i) != IS) vmwait<DEP>(); \
      SB(); } while (0)
#pragma unroll
    for (int i = 0; i <= IS; ++i) LOAD_STEP(i);
    {
      ST_(2); lds_wait(L, C_QREADY, 1u); ST_(3);
      const u32x4 qp = *(LAS u32x4*)(L + L_Q + c16 * 16);
      float xs[16];
#pragma unroll
      for (int ii = 0; ii < 16; ++ii) {
        float d = 0.f;
#pragma unroll
        for (int k = 0; k < 4; ++k) d = dot2(big[ii][k], qp[k], d);
        xs[ii] = d;
      }
#define TR_STEP(HALF, CTRL, BIT) _Pragma("unroll") for (int q = 0; q < (HALF); ++q) { \
      const float lo_ = xs[q], hi_ = xs[q + (HALF)]; const float t1_ = lo_ + dpp<CTRL>(lo_), t2_ = hi_ + dpp<CTRL>(hi_); \
      xs[q] = (c16 & (BIT)) ? t2_ : t1_; }
      TR_STEP(8, XOR8, 8) TR_STEP(4, XOR7, 4) TR_STEP(2, XOR2, 2) TR_STEP(1, XOR1, 1)
#undef TR_STEP
      const float sc = rbf(rbf(xs[0]) * 0.08838834764831845f);
      mw = wave_max(sc);
      const float e = __expf(sc - mw);
      LAS u16* P = (LAS u16*)(L + L_P) + wave * 64;
      P[lane] = (u16)f2bf(e);
      MEMBAR();
      pp0 = *(volatile LAS u32x4*)(P + r4 * 16); pp1 = *(volatile LAS u32x4*)(P + r4 * 16 + 8);
      STAT[wave] = mw;
      STAT[8 + wave] = wave_sum(e);
      if (lds_arrive(C_M) == 7u) {
        const float m8 = STAT[lane & 7], l8 = STAT[8 + (lane & 7)];
        float mc = fmaxf(m8, dpp<XOR1>(m8)); mc = fmaxf(mc, dpp<XOR2>(mc)); mc = fmaxf(mc, dpp<XOR7>(mc));
        float lc = l8 * __expf(m8 - mc);
        lc += dpp<XOR1>(lc); lc += dpp<XOR2>(lc); lc += dpp<XOR7>(lc);
        if (lane == 0) st_sc1_64(rs, WS_ST + u * 128, u32x2{nosent(mc), nosent(lc)});
      }
          ST_(4);
      }
#pragma unroll
    for (int i = IS + 1; i < 32; ++i) LOAD_STEP(i);
    ST_(1);
    __builtin_amdgcn_s_setprio(2);
    {
      float acc[8] = {0.f, 0.f, 0.f, 0.f, 0.f, 0.f, 0.f, 0.f};
      constexpr int NPRE = ((IB < 30 ? IB : 30) - 16 - DEP) / 2;
      float scl;
#pragma unroll
      for (int m = 0; m < 8; ++m) {
        if (m == NPRE) {
          lds_spin(L, C_M, 8u);
          const f32x4 ma = *(LAS f32x4*)STAT, mb = *(LAS f32x4*)(STAT + 4);
          const float mc = fmaxf(fmaxf(fmaxf(ma[0], ma[1]), fmaxf(ma[2], ma[3])), fmaxf(fmaxf(mb[0], mb[1]), fmaxf(mb[2], mb[3])));
          scl = __expf(mw - mc);
          asm volatile("" : "+v"(scl));
        }
        if (m >= NPRE) prepack(big[16 + 2 * m], big[16 + 2 * m + 1]);
        mac_packed(acc, big[16 + 2 * m], big[16 + 2 * m + 1], m < 4 ? pp0[m & 3] : pp1[m & 3]);
        SB();
      }
      ST_(5);
      float v[4];
#pragma unroll
      for (int k = 0; k < 4; ++k) v[k] = tr16(acc[k], acc[k + 4]);
      const float y0 = tr32(v[0], v[2]) * scl, y1 = tr32(v[1], v[3]) * scl;
      LAS float* OR = (LAS float*)(L + L_OR);
      *(LAS f32x2*)(OR + wave * 128 + c16 * 8 + (r4 & 1) * 4 + (r4 >> 1) * 2) = f32x2{y0, y1};
      if (lds_arrive(C_PV) == 7u) {
        const f32x4 s = sum4(OR + (lane >> 5) * 512 + 4 * (lane & 31), 128);
        f32x4 t;
#pragma unroll
        for (int k = 0; k < 4; ++k) t[k] = tr32(s[k], s[k]);
        if (lane < 32) st_sc1(rs, WS_OP + u * 512 + lane * 16, nosent4(t));
        MEMBAR();
        lds_put(L, C_PV, 9u);
      }
      ST_(6);
    }
  } else {
    const int w = wave - 8;
    int zero_v;
    asm volatile("v_mov_b32 %0, 0" : "=v"(zero_v));
    const int r8 = lane >> 3, c8 = lane & 7;
    __builtin_amdgcn_s_setprio(3);
    const u64 tstart = __builtin_amdgcn_s_memrealtime();
    const bool m1 = b < 240;
    const int cg = b % 60, rg = b / 60;
    u32x4 wm[5]; unsigned cw0, cw1, cw2;
#define MOD_TILE_LOAD() do {  \
      const int row0 = 160 * rg + 40 * w; \
      const u16* wb = wmod + (size_t)row0 * (6 * D) + cg * 64 + c8 * 8; \
      wm[0] = ldnt(wb + (size_t)(2 * r8) * (6 * D)); wm[1] = ldnt(wb + (size_t)(2 * r8 + 1) * (6 * D)); \
      wm[2] = ldnt(wb + (size_t)(16 + 2 * r8) * (6 * D)); wm[3] = ldnt(wb + (size_t)(17 + 2 * r8) * (6 * D)); \
      wm[4] = ldnt(wb + (size_t)(32 + r8) * (6 * D)); \
      cw0 = __builtin_nontemporal_load((const unsigned*)(cnd + row0 + 2 * r8)); \
      cw1 = __builtin_nontemporal_load((const unsigned*)(cnd + row0 + 16 + 2 * r8)); \
      cw2 = __builtin_nontemporal_load(cnd + row0 + 32 + r8); \
     } while (0)
    if (m1 && cg < 20) MOD_TILE_LOAD();
    const u32x2 Ev = ld_sc1_64(rs, WS_EPOCH + zero_v);
    const int l20 = (lane & 31) < 20 ? (lane & 31) : 19;
    const int hrow = 80 * j + 20 * w + l20;
    const u32x4 xa = ldnt(x + lane * 8);
    const unsigned xb = __builtin_nontemporal_load((const unsigned*)x + 256 + lane);
    const float xr = bflo((unsigned)__builtin_nontemporal_load(x + hrow));
    if (w == 3) {
      if (lane < 32) lds_put(L, lane, 0u);
    }
    SB();
    LGKM0();
    __builtin_amdgcn_s_barrier();
    SB();
    u32x4 wqv[5];
    if (WQE) {
#pragma unroll
      for (int i = 0; i < 5; ++i) wqv[i] = ldnt(wq + (size_t)(80 * j + 20 * w + 5 * r4 + i) * (HQ * DH) + h * DH + c16 * 8);
      SB();
    }
    if (m1 && cg >= 20) { wait_until(tstart + TM2); MOD_TILE_LOAD(); }
    const unsigned tag = bij32(Ev[0]), flo = bij32(Ev[0] ^ Ev[1] ^ 0x5bd1e995u);
    if (m1) {
      float acc[8] = {0.f, 0.f, 0.f, 0.f, 0.f, 0.f, 0.f, 0.f};
      mac_pair(acc, wm[0], wm[1], pack2(silu_f(bflo(cw0)), silu_f(bfhi(cw0))));
      mac_pair(acc, wm[2], wm[3], pack2(silu_f(bflo(cw1)), silu_f(bfhi(cw1))));
      {
        const unsigned sl = f2bf(silu_f(bflo(cw2))), sh = sl << 16;
#pragma unroll
        for (int d = 0; d < 4; ++d) { acc[2 * d] = dot2(wm[4][d], sl, acc[2 * d]); acc[2 * d + 1] = dot2(wm[4][d], sh, acc[2 * d + 1]); }
      }
      const float y = red8_b345(acc, lane);
      const int col = c8 * 8 + ((lane >> 3) & 1) * 4 + ((lane >> 4) & 1) * 2 + (lane >> 5);
      LAS float* MR = (LAS float*)(L + L_MR);
      MR[w * 64 + col] = y;
      if (lds_arrive(C_MOD1) == 3u) {
        const float s = (MR[lane] + MR[64 + lane]) + (MR[128 + lane] + MR[192 + lane]);
        __hip_atomic_store((gu64*)(ws + WS_MP) + rg * 3840 + cg * 64 + lane, ((u64)tag << 32) | (u64)fb(s), RLX_AGENT);
      }
    }
    ST_(1);
    if (w == 3) {
      u32x4 sv = {SENT, SENT, SENT, SENT};
      st_sc1(rs, (lane < 32 ? WS_QP : WS_OP) + u * 512 + (lane & 31) * 16, sv);
      if (b >= 128 && b < 128 + NF) {
        st_sc1(rs, WS_ZP + (b - 128) * 2560 + lane * 16, sv);
        st_sc1(rs, WS_ZP + (b - 128) * 2560 + 1024 + lane * 16, sv);
      }
      int o2 = 0x7FFFFF00;
      if (lane == 63) o2 = WS_ST + u * 128;
      else if (b < 120) { if (lane < 16) o2 = WS_YP + b * 256 + lane * 16; }
      else if (b >= 128 && b < 128 + NF) { if (lane < 32) o2 = WS_ZP + (b - 128) * 2560 + 2048 + lane * 16; }
      else if (b == 255 && lane < 8) { o2 = WS_TICKET + lane * 128; sv = u32x4{0u, 0u, 0u, 0u}; }
      st_sc1(rs, o2, sv);
          vmwait<0>();
      if (lane == 0) __hip_atomic_store((gu64*)(ws + WS_FLAGS) + b, ((u64)tag << 32) | (u64)flo, RLX_AGENT);
    }
    {
      if (!WQE) {
#pragma unroll
        for (int i = 0; i < 5; ++i) wqv[i] = ldnt(wq + (size_t)(80 * j + 20 * w + 5 * r4 + i) * (HQ * DH) + h * DH + c16 * 8);
      }
      SB();
      float nrm;
      {
        float s = bflo(xb) + bfhi(xb);
#pragma unroll
        for (int i = 0; i < 4; ++i) s += bflo(xa[i]) + bfhi(xa[i]);
        const float mean = wave_sum(s) * (1.f / 640.f);
        float q2 = (bflo(xb) - mean) * (bflo(xb) - mean) + (bfhi(xb) - mean) * (bfhi(xb) - mean);
#pragma unroll
        for (int i = 0; i < 4; ++i) { const float d0 = bflo(xa[i]) - mean, d1 = bfhi(xa[i]) - mean; q2 += d0 * d0 + d1 * d1; }
        const float var = wave_sum(q2) * (1.f / 640.f);
        const float den = rbf(sqrtf(rbf(rbf(var) + 1e-5f)));
        nrm = rbf(rbf(xr - rbf(mean)) / den);
      }
      gu64* mp = (gu64*)(ws + WS_MP) + (lane >> 5) * 640 + hrow;
      u64 g[NMP];
      wait_until(tstart + T1);
      NOUNROLL for (unsigned s = 0; s < SPIN_G; ++s) {
        bool ok = true;
#pragma unroll
        for (int i = 0; i < NMP; ++i) g[i] = __hip_atomic_load(mp + i * 3840, RLX_AGENT);
#pragma unroll
        for (int i = 0; i < NMP; ++i) ok = ok & ((unsigned)(g[i] >> 32) == tag);
        CNT_(0);
        if (__all(ok)) break;
        __builtin_amdgcn_s_sleep(PSLEEP);
      }
      const float mv = rbf((bf((unsigned)g[0]) + bf((unsigned)g[1])) + (bf((unsigned)g[2]) + bf((unsigned)g[3])));
      auto sw = __builtin_amdgcn_permlane32_swap(fb(mv), fb(mv), false, false);
      const float hval = rbf(rbf(nrm * rbf(1.f + bf(sw[1]))) + bf(sw[0]));
      ST_(2);
      float acc[8] = {0.f, 0.f, 0.f, 0.f, 0.f, 0.f, 0.f, 0.f};
#pragma unroll
      for (int i = 0; i < 5; ++i) {
        const float hk = __shfl(hval, 5 * r4 + i);
#pragma unroll
        for (int d = 0; d < 4; ++d) { acc[2 * d] = fmaf(hk, bflo(wqv[i][d]), acc[2 * d]); acc[2 * d + 1] = fmaf(hk, bfhi(wqv[i][d]), acc[2 * d + 1]); }
      }
      float v[4];
#pragma unroll
      for (int k = 0; k < 4; ++k) v[k] = tr16(acc[k], acc[k + 4]);
      const float y0 = tr32(v[0], v[2]), y1 = tr32(v[1], v[3]);
      LAS float* QR = (LAS float*)(L + L_QR);
      *(LAS f32x2*)(QR + w * 128 + c16 * 8 + (r4 & 1) * 4 + (r4 >> 1) * 2) = f32x2{y0, y1};
      if (lds_arrive(C_QP) == 3u) {
        if (lane < 32) st_sc1(rs, WS_QP + u * 512 + lane * 16, nosent4(sum4(QR + 4 * lane, 128)));
      }
    }
    ST_(3);
    if (w == 1) {
      wait_until(tstart + T3);
      NOUNROLL for (unsigned s = 0; s < SPIN_G; ++s) {
        MEMBAR();
        const u32x4 g0 = ld_sc1(rs, WS_FLAGS + lane * 16), g1 = ld_sc1(rs, WS_FLAGS + 1024 + lane * 16);
        const unsigned bad = ((g0[0] ^ flo) | (g0[1] ^ tag)) | ((g0[2] ^ flo) | (g0[3] ^ tag)) | ((g1[0] ^ flo) | (g1[1] ^ tag)) | ((g1[2] ^ flo) | (g1[3] ^ tag));
        if (__all(bad == 0u)) break;
        __builtin_amdgcn_s_sleep(PSLEEP);
      }
      lds_put(L, C_FLAGOK, 1u);
      lds_wait(L, C_PROG, ITK);
      if (lane == 0) {
        const unsigned tk = __hip_atomic_fetch_add((GAS unsigned*)(ws + WS_TICKET + j * 128), 1u, RLX_AGENT);
        lds_put(L, C_ROLE, tk * 8u + (unsigned)j + 1u);
      }
    } else if (w == 0) {
      u32x4 a0, a1, b0, b1;
      const int off = WS_QP + (h * 8 + 2 * r4) * 512 + c16 * 32;
      lds_wait(L, C_FLAGOK, 1u);
      wait_until(tstart + T2);
      NOUNROLL for (unsigned s = 0; s < SPIN_G; ++s) {
        MEMBAR();
        a0 = ld_sc1(rs, off); a1 = ld_sc1(rs, off + 16); b0 = ld_sc1(rs, off + 512); b1 = ld_sc1(rs, off + 528);
        CNT_(16);
        if (__all(max(max(mx4(a0), mx4(a1)), max(mx4(b0), mx4(b1))) != SENT)) break;
        __builtin_amdgcn_s_sleep(PSLEEP);
      }
      float v[4];
#pragma unroll
      for (int k = 0; k < 4; ++k) v[k] = tr16(bf(a0[k]) + bf(b0[k]), bf(a1[k]) + bf(b1[k]));
      const float y0 = tr32(v[0], v[2]), y1 = tr32(v[1], v[3]);
      ((LAS unsigned*)(L + L_Q))[c16 * 4 + (r4 & 1) * 2 + (r4 >> 1)] = pack2(y0, y1);
      MEMBAR();
      lds_put(L, C_QREADY, 1u);
      ST_(4);
    } else if (b >= 240) {
      const int mc = ((b - 240) * 2 + (w - 2)) * 80 + 2 * (lane < 40 ? lane : 39);
      u32x4 g[4];
      lds_wait(L, C_FLAGOK, 1u);
      NOUNROLL for (unsigned s = 0; s < SPIN_G; ++s) {
        MEMBAR();
        unsigned bad = 0u;
#pragma unroll
        for (int i = 0; i < 4; ++i) g[i] = ld_sc1(rs, WS_MP + (i * 3840 + 1280 + mc) * 8);
#pragma unroll
        for (int i = 0; i < 4; ++i) bad |= (g[i][1] ^ tag) | (g[i][3] ^ tag);
        if (__all(bad == 0u)) break;
        __builtin_amdgcn_s_sleep(PSLEEP);
      }
      const float m0 = rbf((bf(g[0][0]) + bf(g[1][0])) + (bf(g[2][0]) + bf(g[3][0]))), m1v = rbf((bf(g[0][2]) + bf(g[1][2])) + (bf(g[2][2]) + bf(g[3][2])));
      st_sc1_64(rs, WS_M2 + mc * 4, u32x2{nosent(m0), nosent(m1v)});
    }
    lds_wait(L, C_ROLE, 1u);
    const int role = __builtin_amdgcn_readfirstlane(((volatile LAS unsigned*)L)[C_ROLE]) - 1;
    __builtin_amdgcn_s_setprio(0);
    stamp[11] |= (u64)role << 32;
    const int r16 = lane >> 2, c4 = lane & 3;
    const int col32 = c4 * 8 + ((lane >> 3) & 1) * 4 + ((lane >> 4) & 1) * 2 + (lane >> 5);
    const int col64 = c8 * 8 + ((lane >> 3) & 1) * 4 + ((lane >> 4) & 1) * 2 + (lane >> 5);
    LAS float* YR = (LAS float*)(L + L_YR);
    if (role < N_M2 + N_WO) {
      const int g = (role - N_M2) & 7, cgp = (role - N_M2) >> 3, hw = 4 * g + w;
      u32x4 wt[16];
      TSTART(TA_WO);
      const u16* wb = wo + (size_t)(hw * 128 + 16 * r8) * D + cgp * 64 + c8 * 8;
#pragma unroll
      for (int i = 0; i < 16; ++i) { wt[i] = ldnt(wb + i * D); if (i >= TDEP && i < 15) vmwait<TDEP>(); SB(); }
      SB();
      ST_(5);
      u32x4 a[4]; u32x2 ml;
      const int off = WS_OP + (hw * 8 + 4 * (lane >> 5)) * 512 + (lane & 31) * 16;
      const int mo = WS_ST + (hw * 8 + c8) * 128;
      ml = ld_sc1_64(rs, mo);
      SB();
#pragma unroll
      for (int m = 0; m < 8; ++m) { prepack(wt[2 * m], wt[2 * m + 1]); pin(wt[2 * m]); pin(wt[2 * m + 1]); }
      float acc[8] = {0.f, 0.f, 0.f, 0.f, 0.f, 0.f, 0.f, 0.f};
      asm volatile("" : "+v"(acc[0]), "+v"(acc[1]), "+v"(acc[2]), "+v"(acc[3]), "+v"(acc[4]), "+v"(acc[5]), "+v"(acc[6]), "+v"(acc[7]));
      float wj[4];
      NOUNROLL for (unsigned s = 0; s < SPIN_G; ++s) {
        MEMBAR();
#pragma unroll
        for (int i = 0; i < 4; ++i) a[i] = ld_sc1(rs, off + i * 512);
        const bool mok = __all(max(ml[0], ml[1]) != SENT);
        {
          float M = bf(ml[0]);
          M = fmaxf(M, dpp<XOR1>(M)); M = fmaxf(M, dpp<XOR2>(M)); M = fmaxf(M, dpp<XOR7>(M));
          const float e = __expf(bf(ml[0]) - M);
          float Ls = bf(ml[1]) * e;
          Ls += dpp<XOR1>(Ls); Ls += dpp<XOR2>(Ls); Ls += dpp<XOR7>(Ls);
          const float wgt = e * __builtin_amdgcn_rcpf(Ls);
#pragma unroll
          for (int i = 0; i < 4; ++i) {
            const float lo = bf(__builtin_amdgcn_readlane(fb(wgt), i)), hi = bf(__builtin_amdgcn_readlane(fb(wgt), 4 + i));
            wj[i] = lane < 32 ? lo : hi;
          }
          asm volatile("" : "+v"(wj[0]), "+v"(wj[1]), "+v"(wj[2]), "+v"(wj[3]));
        }
        if (!mok) ml = ld_sc1_64(rs, mo);
        if (mok && __all(max(max(mx4(a[0]), mx4(a[1])), max(mx4(a[2]), mx4(a[3]))) != SENT)) break;
      }
      ST_(6);
      float t4[4];
#pragma unroll
      for (int k = 0; k < 4; ++k) t4[k] = fmaf(wj[0], bf(a[0][k]), wj[1] * bf(a[1][k])) + fmaf(wj[2], bf(a[2][k]), wj[3] * bf(a[3][k]));
      LAS unsigned* OB = (LAS unsigned*)(L + L_OB) + w * 64;
      OB[2 * (lane & 31) + (lane >> 5)] = pack2(tr32(t4[0], t4[2]), tr32(t4[1], t4[3]));
      MEMBAR();
      const u32x4 oa = *(volatile LAS u32x4*)(OB + 8 * r8), ob = *(volatile LAS u32x4*)(OB + 8 * r8 + 4);
#pragma unroll
      for (int m = 0; m < 4; ++m) mac_packed(acc, wt[2 * m], wt[2 * m + 1], oa[m]);
#pragma unroll
      for (int m = 0; m < 4; ++m) mac_packed(acc, wt[8 + 2 * m], wt[8 + 2 * m + 1], ob[m]);
      const float y = red8_b345(acc, lane);
      YR[w * 64 + col64] = y;
      HBAR();
      if (w == 0 && lane < 16) st_sc1(rs, WS_YP + (g * 640 + cgp * 64 + 4 * lane) * 4, nosent4(sum4(YR + 4 * lane, 64)));
      ST_(7);
    } else if (role < N_M2 + N_WO + NF) {
      const int f = role - (N_M2 + N_WO);
      const int row0 = 160 * w + 10 * r16;
      const int t = w * 64 + lane;
      const int tq = t < 160 ? t : 159;
      const bool fin = f < 20 && w == 3;
      const int oc = (f < 20 ? 32 * f : 0) + 4 * c8 + ((lane >> 4) & 1) * 2 + (lane >> 5);
      const u32x2 xq = __builtin_nontemporal_load((const u32x2*)(x + 4 * tq));
      LAS unsigned* X1 = (LAS unsigned*)(L + L_X1);
      u32x4 wt[18]; unsigned w2t[8];
      TSTART(TA_FFN);
      const u16* wb = w1 + (size_t)row0 * DFF + f * 32 + c4 * 8;
      const u16* w2b = w2 + (size_t)(f * 32 + 8 * w) * D;
#pragma unroll
      for (int i = 0; i < 18; ++i) {
        if (i < 10) wt[i] = ldnt(wb + (size_t)i * DFF);
        else {
          wt[i] = ldnt(w2b + (i - 10) * D + lane * 8);
          w2t[i - 10] = __builtin_nontemporal_load((const unsigned*)(w2b + (i - 10) * D + 512 + lane * 2));
        }
        if (i >= TDEPF && i < 17) vmwait<TDEPF>();
        SB();
      }
      SB();
      ST_(5);
      u32x4 ga; u32x2 sbA, sbB, cbA, cbB; unsigned gbw;
      NOUNROLL for (unsigned s = 0; s < SPIN_G; ++s) {
        MEMBAR();
        ga = ld_sc1(rs, WS_M2 + tq * 16);
        gbw = __builtin_amdgcn_raw_buffer_load_b32(rs, WS_M2 + 7680 + oc * 4, 0, 16);
        sbA = ld_sc1_64(rs, WS_M2 + 2560 + (row0 + 2 * c4) * 4); sbB = ld_sc1_64(rs, WS_M2 + 2560 + (row0 + 8) * 4);
        cbA = ld_sc1_64(rs, WS_M2 + 5120 + (row0 + 2 * c4) * 4); cbB = ld_sc1_64(rs, WS_M2 + 5120 + (row0 + 8) * 4);
        const unsigned mm = max(max(mx4(ga), gbw), max(max(max(sbA[0], sbA[1]), max(sbB[0], sbB[1])), max(max(cbA[0], cbA[1]), max(cbB[0], cbB[1]))));
        if (__all(mm != SENT)) break;
        __builtin_amdgcn_s_sleep(2);
      }
      float c1[4] = {rbf(1.f + bf(cbA[0])), rbf(1.f + bf(cbA[1])), rbf(1.f + bf(cbB[0])), rbf(1.f + bf(cbB[1]))};
      float xv[4] = {bflo(xq[0]), bfhi(xq[0]), bflo(xq[1]), bfhi(xq[1])};
      asm volatile("" : "+v"(c1[0]), "+v"(c1[1]), "+v"(c1[2]), "+v"(c1[3]), "+v"(xv[0]), "+v"(xv[1]), "+v"(xv[2]), "+v"(xv[3]));
      const float s1[4] = {bf(sbA[0]), bf(sbA[1]), bf(sbB[0]), bf(sbB[1])};
#pragma unroll
      for (int m = 0; m < 9; ++m) { prepack(wt[2 * m], wt[2 * m + 1]); pin(wt[2 * m]); pin(wt[2 * m + 1]); }
#pragma unroll
      for (int m = 0; m < 4; ++m) {
        const unsigned lo = __builtin_amdgcn_perm(w2t[2 * m + 1], w2t[2 * m], 0x05040100u), hi = __builtin_amdgcn_perm(w2t[2 * m + 1], w2t[2 * m], 0x07060302u);
        w2t[2 * m] = lo; w2t[2 * m + 1] = hi;
        asm volatile("" : "+v"(w2t[2 * m]), "+v"(w2t[2 * m + 1]));
      }
      float acc[8] = {0.f, 0.f, 0.f, 0.f, 0.f, 0.f, 0.f, 0.f};
      asm volatile("" : "+v"(acc[0]), "+v"(acc[1]), "+v"(acc[2]), "+v"(acc[3]), "+v"(acc[4]), "+v"(acc[5]), "+v"(acc[6]), "+v"(acc[7]));
      if (w < 3) {
        u32x4 a[8];
        NOUNROLL for (unsigned s = 0; s < SPIN_G; ++s) {
          MEMBAR();
#pragma unroll
          for (int i = 0; i < 8; ++i) a[i] = ld_sc1(rs, WS_YP + i * 2560 + tq * 16);
          unsigned mm = mx4(a[0]);
#pragma unroll
          for (int i = 1; i < 8; ++i) mm = max(mm, mx4(a[i]));
          if (__all(mm != SENT)) break;
        }
        ST_(6);
        float x1[4];
#pragma unroll
        for (int k = 0; k < 4; ++k) {
          const float at = ((bf(a[0][k]) + bf(a[1][k])) + (bf(a[2][k]) + bf(a[3][k]))) + ((bf(a[4][k]) + bf(a[5][k])) + (bf(a[6][k]) + bf(a[7][k])));
          x1[k] = xv[k] + rbf(bf(ga[k]) * rbf(at));
        }
        *(LAS u32x2*)(X1 + 2 * tq) = u32x2{pack2(x1[0], x1[1]), pack2(x1[2], x1[3])};
      }
      HBAR();
      unsigned hp5[5];
      {
        const u32x4 ya = *(LAS u32x4*)(X1 + 4 * lane);
        const unsigned yb = X1[256 + lane];
        const unsigned hA = X1[(row0 >> 1) + c4], hB = X1[(row0 >> 1) + 4];
        float sa_ = dot2(yb, 0x3F803F80u, 0.f), sq_ = dot2(yb, yb, 0.f);
#pragma unroll
        for (int k = 0; k < 4; ++k) { sa_ = dot2(ya[k], 0x3F803F80u, sa_); sq_ = dot2(ya[k], ya[k], sq_); }
        float r = tr32(sa_, sq_);
        r = tr16(r, r);
        r = row_sum(r);
        const float mean = bf(__builtin_amdgcn_readlane(fb(r), 0)) * (1.f / 640.f);
        float var = fmaf(-mean, mean, bf(__builtin_amdgcn_readlane(fb(r), 32)) * (1.f / 640.f));
        if (!(var * 1024.f > mean * mean)) {
          float q2 = 0.f;
#pragma unroll
          for (int k = 0; k < 4; ++k) { const float d0 = bflo(ya[k]) - mean, d1 = bfhi(ya[k]) - mean; q2 += d0 * d0 + d1 * d1; }
          const float d0 = bflo(yb) - mean, d1 = bfhi(yb) - mean; q2 += d0 * d0 + d1 * d1;
          var = wave_sum(q2) * (1.f / 640.f);
        }
        const float mr = rbf(mean);
        const float rden = __builtin_amdgcn_rcpf(rbf(__builtin_amdgcn_sqrtf(rbf(rbf(var) + 1e-5f))));
        const float xr2[4] = {bflo(hA), bfhi(hA), bflo(hB), bfhi(hB)};
        float hv[4];
#pragma unroll
        for (int k = 0; k < 4; ++k) hv[k] = rbf(rbf(rbf(xr2[k] - mr) * rden) * c1[k]) + s1[k];
        const unsigned pA = pack2(hv[0], hv[1]);
        hp5[4] = pack2(hv[2], hv[3]);
        hp5[0] = __builtin_amdgcn_mov_dpp((int)pA, 0x00, 0xf, 0xf, true);
        hp5[1] = __builtin_amdgcn_mov_dpp((int)pA, 0x55, 0xf, 0xf, true);
        hp5[2] = __builtin_amdgcn_mov_dpp((int)pA, 0xAA, 0xf, 0xf, true);
        hp5[3] = __builtin_amdgcn_mov_dpp((int)pA, 0xFF, 0xf, 0xf, true);
      }
      ST_(7);
      {
#pragma unroll
        for (int m = 0; m < 5; ++m) mac_packed(acc, wt[2 * m], wt[2 * m + 1], hp5[m]);
        float y = red8_b345(acc, lane);
        y += xor4(y);
        YR[w * 64 + col32] = y;
        float z[8] = {0.f, 0.f, 0.f, 0.f, 0.f, 0.f, 0.f, 0.f};
        float z8 = 0.f, z9 = 0.f;
        asm volatile("" : "+v"(z[0]), "+v"(z[1]), "+v"(z[2]), "+v"(z[3]), "+v"(z[4]), "+v"(z[5]), "+v"(z[6]), "+v"(z[7]), "+v"(z8), "+v"(z9));
        HBAR();
        const LAS float* yp = YR + 8 * w + c8;
        float hd = (yp[0] + yp[64]) + (yp[128] + yp[192]);
        hd = hd < 0.f ? 0.f : hd;
        const unsigned hpk = pack2(hd, dpp<XOR1>(hd));
#pragma unroll
        for (int m = 0; m < 4; ++m) {
          const unsigned hp = __builtin_amdgcn_readlane(hpk, 2 * m);
          mac_packed(z, wt[10 + 2 * m], wt[10 + 2 * m + 1], hp);
          z8 = dot2(w2t[2 * m], hp, z8);
          z9 = dot2(w2t[2 * m + 1], hp, z9);
        }
        LAS float* ZR = (LAS float*)(L + L_ZR);
        *(LAS f32x4*)(ZR + w * 640 + 8 * lane) = f32x4{z[0], z[1], z[2], z[3]};
        *(LAS f32x4*)(ZR + w * 640 + 8 * lane + 4) = f32x4{z[4], z[5], z[6], z[7]};
        *(LAS f32x2*)(ZR + w * 640 + 512 + 2 * lane) = f32x2{z8, z9};
        HBAR();
        if (t < 160) st_sc1(rs, WS_ZP + f * 2560 + t * 16, nosent4(sum4(ZR + 4 * t, 640)));
      }
      ST_(8);
      if (fin) {
        u32x4 zp[7];
        const int zo = WS_ZP + r8 * 2560 + f * 128 + c8 * 16;
        const int z6 = r8 < NF - 48 ? zo + 48 * 2560 : 0x7FFFFF00;
        const float xo = bflo((unsigned)((LAS u16*)X1)[oc]);
        NOUNROLL for (unsigned s = 0; s < SPIN_G; ++s) {
          MEMBAR();
#pragma unroll
          for (int i = 0; i < 6; ++i) zp[i] = ld_sc1(rs, zo + i * 8 * 2560);
          zp[6] = ld_sc1(rs, z6);
          unsigned mm = mx4(zp[0]);
#pragma unroll
          for (int i = 1; i < 7; ++i) mm = max(mm, mx4(zp[i]));
          if (__all(mm != SENT)) break;
        }
        ST_(9);
        float r[4];
#pragma unroll
        for (int k = 0; k < 4; ++k) r[k] = ((bf(zp[0][k]) + bf(zp[1][k])) + (bf(zp[2][k]) + bf(zp[3][k]))) + ((bf(zp[4][k]) + bf(zp[5][k])) + bf(zp[6][k]));
        float v = tr32(tr16(r[0], r[2]), tr16(r[1], r[3]));
        v += dpp<XOR8>(v);
        out[oc] = (u16)f2bf(xo + rbf(bf(gbw) * rbf(v)));
        if (f == 0 && lane == 0) {
          const u64 e1 = (((u64)Ev[1] << 32) | (u64)Ev[0]) + 1ull;
          st_sc1_64(rs, WS_EPOCH, u32x2{(unsigned)e1, (unsigned)(e1 >> 32)});
        }
        ST_(10);
      }
    }
  }
  if (lane == 0) {
    u64* sp = (u64*)(ws + WS_STAMP) + (size_t)(b * 12 + wave) * 12;
#pragma unroll
    for (int k = 0; k < 12; ++k) sp[k] = stamp[k];
  }
}

#include <cstdio>
#include <vector>
#include <algorithm>
__global__ void dbg_fill(unsigned* p, size_t n, unsigned seed) {
  size_t i = (size_t)blockIdx.x * blockDim.x + threadIdx.x;
  size_t stride = (size_t)gridDim.x * blockDim.x;
  for (; i < n; i += stride) { unsigned x = (unsigned)i * 2654435761u + seed * 0x9E3779B9u; x ^= x >> 15; x *= 0x2c1b3c6du; x ^= x >> 12; p[i] = x; }
}
static void launch(void* const* d_in, void* d_out, void* d_ws, hipStream_t stream) {
  (void)hipFuncSetAttribute((const void*)mega, hipFuncAttributeMaxDynamicSharedMemorySize, LDS_BYTES);
  hipLaunchKernelGGL(mega, dim3(256), dim3(768), LDS_BYTES, stream, (const u16*)d_in[0], (const u16*)d_in[1], (const u16*)d_in[2],
                     (const u16*)d_in[3], (const u16*)d_in[4], (const u16*)d_in[6], (const u16*)d_in[7], (const u16*)d_in[8],
                     (const u16*)d_in[9], (u16*)d_out, (unsigned char*)d_ws);
}
extern "C" void kernel_launch(void* const* d_in, const int* in_sizes, int n_in, void* d_out, int out_size, void* d_ws,
                              size_t ws_size, hipStream_t stream) {
  hipStreamCaptureStatus cs = hipStreamCaptureStatusNone;
  (void)hipStreamIsCapturing(stream, &cs);
  if (cs != hipStreamCaptureStatusNone) { launch(d_in, d_out, d_ws, stream); return; }
  for (int i = 0; i < n_in; ++i) fprintf(stderr, "in[%d]=%p ", i, d_in[i]);
  fprintf(stderr, "out=%p ws=%p\n", d_out, d_ws);
  hipPointerAttribute_t at; if (hipPointerGetAttributes(&at, d_ws) == hipSuccess) fprintf(stderr, "ws attr type %d flags %u managed %d\n", (int)at.type, at.allocationFlags, at.isManaged);
  if (hipPointerGetAttributes(&at, d_in[1]) == hipSuccess) fprintf(stderr, "k attr type %d flags %u\n", (int)at.type, at.allocationFlags);
  const int NS = 12, NW = 12;
  for (int it = 0; it < 6; ++it) {
    if (it > 0 && it < 4) { dbg_fill<<<2048, 256, 0, stream>>>((unsigned*)d_ws, ws_size / 4, it); }
    (void)hipStreamSynchronize(stream);
    launch(d_in, d_out, d_ws, stream);
    (void)hipStreamSynchronize(stream);
    std::vector<u64> hs(256 * NW * NS);
    (void)hipMemcpy(hs.data(), (char*)d_ws + WS_STAMP, hs.size() * 8, hipMemcpyDeviceToHost);
    u64 t0 = ~0ull; for (int i = 0; i < 256 * NW; ++i) t0 = std::min(t0, hs[i * NS]);
    fprintf(stderr, "it %d:", it);
    for (int cls = 0; cls < 2; ++cls) for (int k = 0; k < 11; ++k) {
      std::vector<double> v;
      for (int b = 0; b < 256; ++b) for (int wv = 0; wv < NW; ++wv) { bool in = cls == 0 ? wv >= 8 : wv < 8; u64 t = hs[(b * NW + wv) * NS + k]; if (in && t) v.push_back((t - t0) * 0.01); }
      if (!v.empty()) { std::sort(v.begin(), v.end()); fprintf(stderr, " %c%d %.1f/%.1f/%.1f", cls ? 'S' : 'H', k, v[0], v[v.size() / 2], v.back()); }
    }
    fprintf(stderr, "\n");
  }
}
```

```cpp
#define DBGSEL 1
#define DBGTHR 4.0f
#include <hip/hip_runtime.h>
#include <hip/hip_bf16.h>
#include <stdint.h>

#define STAMP 0
#define PSLEEP 16
#define I1 12
#define I2 21
#define I3 17
#define IT 31
#define IS 31
#define ITK 24
#define PREB 1
#define IB 25
#define TA_M2 0
#define TA_WO 2
#define TA_FFN 2
#define TDEPF 40
#define TSTART(m) do { if ((m) == 2) lds_wait(L, C_PV, 9u); else if ((m) == 1) lds_wait8(L, IT); } while (0)
#define MP10 1
#define T1 400
#define T3 560
#define T2 740
#define TM2 150
#define DEPA 1
#define ISW 14
#define WQE 1
#define DEP 2
#define TDEP 40

#define GAS __attribute__((address_space(1)))
#define LAS __attribute__((address_space(3)))
#define RLX_AGENT __ATOMIC_RELAXED, __HIP_MEMORY_SCOPE_AGENT
typedef unsigned short u16;
typedef unsigned long long u64;
typedef unsigned u32x4 __attribute__((ext_vector_type(4)));
typedef unsigned u32x2 __attribute__((ext_vector_type(2)));
typedef float f32x4 __attribute__((ext_vector_type(4)));
typedef float f32x2 __attribute__((ext_vector_type(2)));
typedef __bf16 bf16x2 __attribute__((ext_vector_type(2)));
typedef GAS u64 gu64;
typedef __amdgpu_buffer_rsrc_t rsrc_t;

constexpr int D = 640, HQ = 32, SK = 16384, DH = 128, WIN = 4096, DFF = 1728;
constexpr int NF = 54;

constexpr int WS_EPOCH = 0;
constexpr int WS_FLAGS = 4096;
constexpr int WS_TICKET = 2048;
constexpr int NMP = 4;
constexpr int WS_MP = 8192;
constexpr int WS_QP = 131072;
constexpr int WS_OP = WS_QP + 131072;
constexpr int WS_ST = WS_OP + 131072;
constexpr int WS_YP = WS_ST + 32768;
constexpr int WS_M2 = WS_YP + 20480;
constexpr int WS_ZP = WS_M2 + 10240;
constexpr int WS_END = WS_ZP + NF * 2560;
constexpr int N_M2 = 0, N_WO = 80;
constexpr int WS_STAMP = 1 << 20;

constexpr unsigned SENT = 0xFFFFFFFFu;
constexpr unsigned SPIN_G = 1u << 18;
constexpr unsigned SPIN_L = 1u << 22;

constexpr int C_PROG = 0;
constexpr int C_MOD1 = 8, C_QP = 9, C_QREADY = 10, C_M = 11, C_ROLE = 12, C_PV = 13, C_FLAGOK = 14;
constexpr int L_P = 128;
constexpr int L_MR = L_P + 1024;
constexpr int L_QR = L_MR + 1024;
constexpr int L_OR = L_QR + 2048;
constexpr int L_OB = L_OR + 4096;
constexpr int L_YR = L_OB + 1024;
constexpr int L_STAT = L_YR + 1024;
constexpr int L_Q = L_STAT + 64;
constexpr int L_X1 = L_Q + 256;
constexpr int L_ZR = L_X1 + 2560;
constexpr int L_END = L_ZR + 10240;
constexpr int LDS_BYTES = 96 * 1024;
static_assert(L_END <= LDS_BYTES, "lds");

__device__ __forceinline__ float bflo(unsigned w) { return __builtin_bit_cast(float, w << 16); }
__device__ __forceinline__ float bfhi(unsigned w) { return __builtin_bit_cast(float, w & 0xffff0000u); }
__device__ __forceinline__ unsigned f2bf(float f) { return (unsigned)__builtin_bit_cast(u16, (__bf16)f); }
__device__ __forceinline__ float rbf(float f) { return bflo(f2bf(f)); }
__device__ __forceinline__ unsigned pack2(float lo, float hi) {
  bf16x2 v = {(__bf16)lo, (__bf16)hi};
  return __builtin_bit_cast(unsigned, v);
}
__device__ __forceinline__ float dot2(unsigned a, unsigned b, float acc) {
  return __builtin_amdgcn_fdot2_f32_bf16(__builtin_bit_cast(bf16x2, a), __builtin_bit_cast(bf16x2, b), acc, false);
}
__device__ __forceinline__ unsigned fb(float f) { return __builtin_bit_cast(unsigned, f); }
__device__ __forceinline__ float bf(unsigned u) { return __builtin_bit_cast(float, u); }
__device__ __forceinline__ unsigned nosent(float f) { return min(fb(f), 0xFFFFFFFEu); }
__device__ __forceinline__ u32x4 nosent4(f32x4 s) { return u32x4{nosent(s[0]), nosent(s[1]), nosent(s[2]), nosent(s[3])}; }
template <int CTRL> __device__ __forceinline__ float dpp(float x) {
  return __builtin_bit_cast(float, __builtin_amdgcn_mov_dpp(__builtin_bit_cast(int, x), CTRL, 0xf, 0xf, true));
}
constexpr int XOR1 = 0xB1, XOR2 = 0x4E, XOR7 = 0x141, XOR8 = 0x128;
__device__ __forceinline__ float xor4(float x) {
  int v = __builtin_bit_cast(int, x);
  int r = __builtin_amdgcn_update_dpp(0, v, 0x114, 0xf, 0xa, false);
  r = __builtin_amdgcn_update_dpp(r, v, 0x104, 0xf, 0x5, false);
  return __builtin_bit_cast(float, r);
}
__device__ __forceinline__ float tr16(float a, float b) {
  auto s = __builtin_amdgcn_permlane16_swap(fb(a), fb(b), false, false);
  return bf(s[0]) + bf(s[1]);
}
__device__ __forceinline__ float tr32(float a, float b) {
  auto s = __builtin_amdgcn_permlane32_swap(fb(a), fb(b), false, false);
  return bf(s[0]) + bf(s[1]);
}
__device__ __forceinline__ float mx16(float a) {
  auto s = __builtin_amdgcn_permlane16_swap(fb(a), fb(a), false, false);
  return fmaxf(bf(s[0]), bf(s[1]));
}
__device__ __forceinline__ float mx32(float a) {
  auto s = __builtin_amdgcn_permlane32_swap(fb(a), fb(a), false, false);
  return fmaxf(bf(s[0]), bf(s[1]));
}
__device__ __forceinline__ float row_sum(float x) {
  x += dpp<XOR1>(x); x += dpp<XOR2>(x); x += dpp<XOR7>(x); x += dpp<XOR8>(x); return x;
}
__device__ __forceinline__ float row_max(float x) {
  x = fmaxf(x, dpp<XOR1>(x)); x = fmaxf(x, dpp<XOR2>(x)); x = fmaxf(x, dpp<XOR7>(x)); x = fmaxf(x, dpp<XOR8>(x)); return x;
}
__device__ __forceinline__ float wave_sum(float x) { x = row_sum(x); x = tr16(x, x); return tr32(x, x); }
__device__ __forceinline__ float wave_max(float x) { x = row_max(x); x = mx16(x); return mx32(x); }

__device__ __forceinline__ unsigned bij32(unsigned x) {
  x += 0x9E3779B9u; x ^= x >> 16; x *= 0x85EBCA6Bu; x ^= x >> 13; x *= 0xC2B2AE35u; x ^= x >> 16; return x;
}
__device__ __forceinline__ rsrc_t mkrs(const void* p, int bytes) { return __builtin_amdgcn_make_buffer_rsrc((void*)p, 0, bytes, 0x00020000); }
__device__ __forceinline__ u32x4 ldnt(const void* p) { return __builtin_nontemporal_load(reinterpret_cast<const u32x4*>(p)); }
__device__ __forceinline__ u32x4 ld_sc1(rsrc_t rs, int off) { return __builtin_amdgcn_raw_buffer_load_b128(rs, off, 0, 16); }
__device__ __forceinline__ u32x2 ld_sc1_64(rsrc_t rs, int off) { return __builtin_amdgcn_raw_buffer_load_b64(rs, off, 0, 16); }
__device__ __forceinline__ void st_sc1(rsrc_t rs, int off, u32x4 v) { __builtin_amdgcn_raw_buffer_store_b128(v, rs, off, 0, 16); }
__device__ __forceinline__ void st_sc1_64(rsrc_t rs, int off, u32x2 v) { __builtin_amdgcn_raw_buffer_store_b64(v, rs, off, 0, 16); }
__device__ __forceinline__ unsigned mx4(u32x4 v) { return max(max(v[0], v[1]), max(v[2], v[3])); }
#define MEMBAR() asm volatile("" ::: "memory")
#define LGKM0() asm volatile("s_waitcnt lgkmcnt(0)" ::: "memory")
#define SB() __builtin_amdgcn_sched_barrier(0)
#define NOUNROLL _Pragma("nounroll")

__device__ __forceinline__ void lds_put(LAS unsigned char* L, int word, unsigned v) { ((volatile LAS unsigned*)L)[word] = v; }
__device__ __forceinline__ void lds_wait(LAS unsigned char* L, int word, unsigned v) {
  NOUNROLL for (unsigned s = 0; s < SPIN_L; ++s) {
    if (((volatile LAS unsigned*)L)[word] >= v) break;
    __builtin_amdgcn_s_sleep(1);
  }
  MEMBAR();
}
__device__ __forceinline__ void lds_wait4(LAS unsigned char* L, int word, unsigned v) {
  NOUNROLL for (unsigned s = 0; s < SPIN_L; ++s) {
    const u32x4 q = *(volatile LAS u32x4*)(L + word * 4);
    if (min(min(q[0], q[1]), min(q[2], q[3])) >= v) break;
    __builtin_amdgcn_s_sleep(1);
  }
  MEMBAR();
}
__device__ __forceinline__ void lds_spin(LAS unsigned char* L, int word, unsigned v) {
  NOUNROLL for (unsigned s = 0; s < SPIN_L; ++s) {
    if (((volatile LAS unsigned*)L)[word] >= v) break;
  }
  MEMBAR();
}
__device__ __forceinline__ unsigned lds_arrive(int word) {
  unsigned old;
  asm volatile("s_mov_b64 exec, 1\n\tds_add_rtn_u32 %0, %1, %2\n\ts_mov_b64 exec, -1\n\ts_waitcnt lgkmcnt(0)"
               : "=&v"(old) : "v"(word * 4), "v"(1u) : "memory");
  return __builtin_amdgcn_readfirstlane(old);
}
__device__ __forceinline__ void lds_arrive_nr(int word) {
  asm volatile("s_mov_b64 exec, 1\n\tds_add_u32 %0, %1\n\ts_mov_b64 exec, -1" :: "v"(word * 4), "v"(1u) : "memory");
}
__device__ __forceinline__ void lds_wait8(LAS unsigned char* L, unsigned v) {
  NOUNROLL for (unsigned s = 0; s < SPIN_L; ++s) {
    const u32x4 q = *(volatile LAS u32x4*)L, r = *(volatile LAS u32x4*)(L + 16);
    if (min(min(min(q[0], q[1]), min(q[2], q[3])), min(min(r[0], r[1]), min(r[2], r[3]))) >= v) break;
    __builtin_amdgcn_s_sleep(2);
  }
  MEMBAR();
}
#define HBAR() do { LGKM0(); __builtin_amdgcn_s_barrier(); MEMBAR(); } while (0)
__device__ __forceinline__ void wait_until(u64 t) {
  NOUNROLL for (unsigned s = 0; s < 4096u; ++s) {
    if (__builtin_amdgcn_s_memrealtime() >= t) break;
    __builtin_amdgcn_s_sleep(2);
  }
  MEMBAR();
}
template <int N> __device__ __forceinline__ void vmwait() { asm volatile("s_waitcnt vmcnt(%0)" :: "i"(N) : "memory"); }

#define ST_(k) do {} while (0)
#define CNT_(sh) do {} while (0)

__device__ __forceinline__ float red8_b345(const float (&a)[8], int lane) {
  float v[4];
  const bool hi = (lane & 8) != 0;
#pragma unroll
  for (int k = 0; k < 4; ++k) {
    const float t1 = a[k] + dpp<XOR8>(a[k]), t2 = a[k + 4] + dpp<XOR8>(a[k + 4]);
    v[k] = hi ? t2 : t1;
  }
  const float u0 = tr16(v[0], v[2]), u1 = tr16(v[1], v[3]);
  return tr32(u0, u1);
}
__device__ __forceinline__ void prepack(u32x4& wa, u32x4& wb) {
#pragma unroll
  for (int d = 0; d < 4; ++d) {
    const unsigned lo = __builtin_amdgcn_perm(wb[d], wa[d], 0x05040100u);
    const unsigned hi = __builtin_amdgcn_perm(wb[d], wa[d], 0x07060302u);
    wa[d] = lo; wb[d] = hi;
  }
}
__device__ __forceinline__ void pin(u32x4& a) { asm volatile("" : "+v"(a)); }
__device__ __forceinline__ void mac_packed(float (&acc)[8], u32x4 wa, u32x4 wb, unsigned xp) {
#pragma unroll
  for (int d = 0; d < 4; ++d) { acc[2 * d] = dot2(wa[d], xp, acc[2 * d]); acc[2 * d + 1] = dot2(wb[d], xp, acc[2 * d + 1]); }
}
__device__ __forceinline__ void mac_pair(float (&acc)[8], u32x4 wa, u32x4 wb, unsigned xp) { prepack(wa, wb); mac_packed(acc, wa, wb, xp); }
__device__ __forceinline__ float silu_f(float v) { return v * __builtin_amdgcn_rcpf(1.f + __expf(-v)); }
__device__ __forceinline__ f32x4 sum4(const LAS float* p, int stride) {
  return (*(const LAS f32x4*)p + *(const LAS f32x4*)(p + stride)) + (*(const LAS f32x4*)(p + 2 * stride) + *(const LAS f32x4*)(p + 3 * stride));
}

__global__ void __launch_bounds__(768, 3)
mega(const u16* __restrict__ x, const u16* __restrict__ kc, const u16* __restrict__ vc, const u16* __restrict__ wq,
     const u16* __restrict__ wo, const u16* __restrict__ w1, const u16* __restrict__ w2, const u16* __restrict__ cnd,
     const u16* __restrict__ wmod, u16* __restrict__ out, unsigned char* ws) {
  extern __shared__ __attribute__((aligned(16))) unsigned char smem[];
  LAS unsigned char* L = (LAS unsigned char*)smem;
  const int tid = threadIdx.x, lane = tid & 63, wave = __builtin_amdgcn_readfirstlane(tid >> 6);
  const int b = blockIdx.x;
  const int h = (b & 7) * 4 + (b >> 6), j = (b >> 3) & 7, u = h * 8 + j;
  const int r4 = lane >> 4, c16 = lane & 15;
  const rsrc_t rs = mkrs(ws, WS_END);
  ST_(0);

  if (wave < 8) {
    const size_t kvoff = ((size_t)h * SK + (SK - WIN) + j * 512) * DH;
    const rsrc_t rk = mkrs(kc + kvoff, 131072), rv = mkrs(vc + kvoff, 131072);
    const int rot = (b * 8 + (b >> 4) + wave) * 1024;
    u32x4 big[32];
    u32x4 pp0, pp1;
    float mw;
    LAS float* STAT = (LAS float*)(L + L_STAT);
#define LOAD_STEP(i) do { \
      big[i] = __builtin_amdgcn_raw_buffer_load_b128((i) < 16 ? rk : rv, lane * 16, (((i) & 15) * 8192 + rot) & 131071, 2); \
      if ((i) == PREB - 1) __builtin_amdgcn_s_barrier();     \
      if ((i) == ITK || (i) == IT) lds_put(L, C_PROG + wave, (unsigned)(i)); \
      if ((i) >= 16 + DEP + 2 && (((i) - DEP) & 1) == 0 && (i) <= IB) { prepack(big[(i) - DEP - 2], big[(i) - DEP - 1]); pin(big[(i) - DEP - 2]); pin(big[(i) - DEP - 1]); } \
      if ((i) < ISW) { if ((i) >= DEPA) vmwait<DEPA>(); } else if ((i) < IB && (i) != IS) vmwait<DEP>(); \
      SB(); } while (0)
#pragma unroll
    for (int i = 0; i <= IS; ++i) LOAD_STEP(i);
    {
      ST_(2); lds_wait(L, C_QREADY, 1u); ST_(3);
      const u32x4 qp = *(LAS u32x4*)(L + L_Q + c16 * 16);
      float xs[16];
#pragma unroll
      for (int ii = 0; ii < 16; ++ii) {
        float d = 0.f;
#pragma unroll
        for (int k = 0; k < 4; ++k) d = dot2(big[ii][k], qp[k], d);
        xs[ii] = d;
      }
#define TR_STEP(HALF, CTRL, BIT) _Pragma("unroll") for (int q = 0; q < (HALF); ++q) { \
      const float lo_ = xs[q], hi_ = xs[q + (HALF)]; const float t1_ = lo_ + dpp<CTRL>(lo_), t2_ = hi_ + dpp<CTRL>(hi_); \
      xs[q] = (c16 & (BIT)) ? t2_ : t1_; }
      TR_STEP(8, XOR8, 8) TR_STEP(4, XOR7, 4) TR_STEP(2, XOR2, 2) TR_STEP(1, XOR1, 1)
#undef TR_STEP
      const float sc = rbf(rbf(xs[0]) * 0.08838834764831845f);
      mw = wave_max(sc);
      const float e = __expf(sc - mw);
      LAS u16* P = (LAS u16*)(L + L_P) + wave * 64;
      P[lane] = (u16)f2bf(e);
      MEMBAR();
      pp0 = *(volatile LAS u32x4*)(P + r4 * 16); pp1 = *(volatile LAS u32x4*)(P + r4 * 16 + 8);
      STAT[wave] = mw;
      STAT[8 + wave] = wave_sum(e);
      if (lds_arrive(C_M) == 7u) {
        const float m8 = STAT[lane & 7], l8 = STAT[8 + (lane & 7)];
        float mc = fmaxf(m8, dpp<XOR1>(m8)); mc = fmaxf(mc, dpp<XOR2>(mc)); mc = fmaxf(mc, dpp<XOR7>(mc));
        float lc = l8 * __expf(m8 - mc);
        lc += dpp<XOR1>(lc); lc += dpp<XOR2>(lc); lc += dpp<XOR7>(lc);
        if (lane == 0) st_sc1_64(rs, WS_ST + u * 128, u32x2{nosent(mc), nosent(lc)});
      }
          ST_(4);
      }
#pragma unroll
    for (int i = IS + 1; i < 32; ++i) LOAD_STEP(i);
    ST_(1);
    __builtin_amdgcn_s_setprio(2);
    {
      float acc[8] = {0.f, 0.f, 0.f, 0.f, 0.f, 0.f, 0.f, 0.f};
      constexpr int NPRE = ((IB < 30 ? IB : 30) - 16 - DEP) / 2;
      float scl;
#pragma unroll
      for (int m = 0; m < 8; ++m) {
        if (m == NPRE) {
          lds_spin(L, C_M, 8u);
          const f32x4 ma = *(LAS f32x4*)STAT, mb = *(LAS f32x4*)(STAT + 4);
          const float mc = fmaxf(fmaxf(fmaxf(ma[0], ma[1]), fmaxf(ma[2], ma[3])), fmaxf(fmaxf(mb[0], mb[1]), fmaxf(mb[2], mb[3])));
          scl = __expf(mw - mc);
          asm volatile("" : "+v"(scl));
        }
        if (m >= NPRE) prepack(big[16 + 2 * m], big[16 + 2 * m + 1]);
        mac_packed(acc, big[16 + 2 * m], big[16 + 2 * m + 1], m < 4 ? pp0[m & 3] : pp1[m & 3]);
        SB();
      }
      ST_(5);
      float v[4];
#pragma unroll
      for (int k = 0; k < 4; ++k) v[k] = tr16(acc[k], acc[k + 4]);
      const float y0 = tr32(v[0], v[2]) * scl, y1 = tr32(v[1], v[3]) * scl;
      LAS float* OR = (LAS float*)(L + L_OR);
      *(LAS f32x2*)(OR + wave * 128 + c16 * 8 + (r4 & 1) * 4 + (r4 >> 1) * 2) = f32x2{y0, y1};
      if (lds_arrive(C_PV) == 7u) {
        const f32x4 s = sum4(OR + (lane >> 5) * 512 + 4 * (lane & 31), 128);
        f32x4 t;
#pragma unroll
        for (int k = 0; k < 4; ++k) t[k] = tr32(s[k], s[k]);
        if (lane < 32) st_sc1(rs, WS_OP + u * 512 + lane * 16, nosent4(t));
        MEMBAR();
        lds_put(L, C_PV, 9u);
      }
      ST_(6);
    }
  } else {
    const int w = wave - 8;
    int zero_v;
    asm volatile("v_mov_b32 %0, 0" : "=v"(zero_v));
    const int r8 = lane >> 3, c8 = lane & 7;
    __builtin_amdgcn_s_setprio(3);
    const u64 tstart = __builtin_amdgcn_s_memrealtime();
    const bool m1 = b < 240;
    const int cg = b % 60, rg = b / 60;
    u32x4 wm[5]; unsigned cw0, cw1, cw2;
#define MOD_TILE_LOAD() do {  \
      const int row0 = 160 * rg + 40 * w; \
      const u16* wb = wmod + (size_t)row0 * (6 * D) + cg * 64 + c8 * 8; \
      wm[0] = ldnt(wb + (size_t)(2 * r8) * (6 * D)); wm[1] = ldnt(wb + (size_t)(2 * r8 + 1) * (6 * D)); \
      wm[2] = ldnt(wb + (size_t)(16 + 2 * r8) * (6 * D)); wm[3] = ldnt(wb + (size_t)(17 + 2 * r8) * (6 * D)); \
      wm[4] = ldnt(wb + (size_t)(32 + r8) * (6 * D)); \
      cw0 = __builtin_nontemporal_load((const unsigned*)(cnd + row0 + 2 * r8)); \
      cw1 = __builtin_nontemporal_load((const unsigned*)(cnd + row0 + 16 + 2 * r8)); \
      cw2 = __builtin_nontemporal_load(cnd + row0 + 32 + r8); \
     } while (0)
    if (m1 && cg < 20) MOD_TILE_LOAD();
    const u32x2 Ev = ld_sc1_64(rs, WS_EPOCH + zero_v);
    const int l20 = (lane & 31) < 20 ? (lane & 31) : 19;
    const int hrow = 80 * j + 20 * w + l20;
    const u32x4 xa = ldnt(x + lane * 8);
    const unsigned xb = __builtin_nontemporal_load((const unsigned*)x + 256 + lane);
    const float xr = bflo((unsigned)__builtin_nontemporal_load(x + hrow));
    if (w == 3) {
      if (lane < 32) lds_put(L, lane, 0u);
    }
    SB();
    LGKM0();
    __builtin_amdgcn_s_barrier();
    SB();
    u32x4 wqv[5];
    if (WQE) {
#pragma unroll
      for (int i = 0; i < 5; ++i) wqv[i] = ldnt(wq + (size_t)(80 * j + 20 * w + 5 * r4 + i) * (HQ * DH) + h * DH + c16 * 8);
      SB();
    }
    if (m1 && cg >= 20) { wait_until(tstart + TM2); MOD_TILE_LOAD(); }
    const unsigned tag = bij32(Ev[0]), flo = bij32(Ev[0] ^ Ev[1] ^ 0x5bd1e995u);
    if (m1) {
      float acc[8] = {0.f, 0.f, 0.f, 0.f, 0.f, 0.f, 0.f, 0.f};
      mac_pair(acc, wm[0], wm[1], pack2(silu_f(bflo(cw0)), silu_f(bfhi(cw0))));
      mac_pair(acc, wm[2], wm[3], pack2(silu_f(bflo(cw1)), silu_f(bfhi(cw1))));
      {
        const unsigned sl = f2bf(silu_f(bflo(cw2))), sh = sl << 16;
#pragma unroll
        for (int d = 0; d < 4; ++d) { acc[2 * d] = dot2(wm[4][d], sl, acc[2 * d]); acc[2 * d + 1] = dot2(wm[4][d], sh, acc[2 * d + 1]); }
      }
      const float y = red8_b345(acc, lane);
      const int col = c8 * 8 + ((lane >> 3) & 1) * 4 + ((lane >> 4) & 1) * 2 + (lane >> 5);
      LAS float* MR = (LAS float*)(L + L_MR);
      MR[w * 64 + col] = y;
      if (lds_arrive(C_MOD1) == 3u) {
        const float s = (MR[lane] + MR[64 + lane]) + (MR[128 + lane] + MR[192 + lane]);
        __hip_atomic_store((gu64*)(ws + WS_MP) + rg * 3840 + cg * 64 + lane, ((u64)tag << 32) | (u64)fb(s), RLX_AGENT);
      }
    }
    ST_(1);
    if (w == 3) {
      u32x4 sv = {SENT, SENT, SENT, SENT};
      st_sc1(rs, (lane < 32 ? WS_QP : WS_OP) + u * 512 + (lane & 31) * 16, sv);
      if (b >= 128 && b < 128 + NF) {
        st_sc1(rs, WS_ZP + (b - 128) * 2560 + lane * 16, sv);
        st_sc1(rs, WS_ZP + (b - 128) * 2560 + 1024 + lane * 16, sv);
      }
      int o2 = 0x7FFFFF00;
      if (lane == 63) o2 = WS_ST + u * 128;
      else if (b < 120) { if (lane < 16) o2 = WS_YP + b * 256 + lane * 16; }
      else if (b >= 128 && b < 128 + NF) { if (lane < 32) o2 = WS_ZP + (b - 128) * 2560 + 2048 + lane * 16; }
      else if (b == 255 && lane < 8) { o2 = WS_TICKET + lane * 128; sv = u32x4{0u, 0u, 0u, 0u}; }
      st_sc1(rs, o2, sv);
          vmwait<0>();
      if (lane == 0) { __hip_atomic_store((gu64*)(ws + (3 << 20)) + b, tstart, RLX_AGENT); vmwait<0>(); __hip_atomic_store((gu64*)(ws + WS_FLAGS) + b, ((u64)tag << 32) | (u64)flo, RLX_AGENT); }
    }
    {
      if (!WQE) {
#pragma unroll
        for (int i = 0; i < 5; ++i) wqv[i] = ldnt(wq + (size_t)(80 * j + 20 * w + 5 * r4 + i) * (HQ * DH) + h * DH + c16 * 8);
      }
      SB();
      float nrm;
      {
        float s = bflo(xb) + bfhi(xb);
#pragma unroll
        for (int i = 0; i < 4; ++i) s += bflo(xa[i]) + bfhi(xa[i]);
        const float mean = wave_sum(s) * (1.f / 640.f);
        float q2 = (bflo(xb) - mean) * (bflo(xb) - mean) + (bfhi(xb) - mean) * (bfhi(xb) - mean);
#pragma unroll
        for (int i = 0; i < 4; ++i) { const float d0 = bflo(xa[i]) - mean, d1 = bfhi(xa[i]) - mean; q2 += d0 * d0 + d1 * d1; }
        const float var = wave_sum(q2) * (1.f / 640.f);
        const float den = rbf(sqrtf(rbf(rbf(var) + 1e-5f)));
        nrm = rbf(rbf(xr - rbf(mean)) / den);
      }
      gu64* mp = (gu64*)(ws + WS_MP) + (lane >> 5) * 640 + hrow;
      u64 g[NMP];
      wait_until(tstart + T1);
      NOUNROLL for (unsigned s = 0; s < SPIN_G; ++s) {
        bool ok = true;
#pragma unroll
        for (int i = 0; i < NMP; ++i) g[i] = __hip_atomic_load(mp + i * 3840, RLX_AGENT);
#pragma unroll
        for (int i = 0; i < NMP; ++i) ok = ok & ((unsigned)(g[i] >> 32) == tag);
        CNT_(0);
        if (__all(ok)) break;
        __builtin_amdgcn_s_sleep(PSLEEP);
      }
      const float mv = rbf((bf((unsigned)g[0]) + bf((unsigned)g[1])) + (bf((unsigned)g[2]) + bf((unsigned)g[3])));
      auto sw = __builtin_amdgcn_permlane32_swap(fb(mv), fb(mv), false, false);
      const float hval = rbf(rbf(nrm * rbf(1.f + bf(sw[1]))) + bf(sw[0]));
      ST_(2);
      float acc[8] = {0.f, 0.f, 0.f, 0.f, 0.f, 0.f, 0.f, 0.f};
#pragma unroll
      for (int i = 0; i < 5; ++i) {
        const float hk = __shfl(hval, 5 * r4 + i);
#pragma unroll
        for (int d = 0; d < 4; ++d) { acc[2 * d] = fmaf(hk, bflo(wqv[i][d]), acc[2 * d]); acc[2 * d + 1] = fmaf(hk, bfhi(wqv[i][d]), acc[2 * d + 1]); }
      }
      float v[4];
#pragma unroll
      for (int k = 0; k < 4; ++k) v[k] = tr16(acc[k], acc[k + 4]);
      const float y0 = tr32(v[0], v[2]), y1 = tr32(v[1], v[3]);
      LAS float* QR = (LAS float*)(L + L_QR);
      *(LAS f32x2*)(QR + w * 128 + c16 * 8 + (r4 & 1) * 4 + (r4 >> 1) * 2) = f32x2{y0, y1};
      if (lds_arrive(C_QP) == 3u) {
        if (lane < 32) st_sc1(rs, WS_QP + u * 512 + lane * 16, nosent4(sum4(QR + 4 * lane, 128)));
      }
    }
    ST_(3);
    if (w == 1) {
      wait_until(tstart + T3);
      NOUNROLL for (unsigned s = 0; s < SPIN_G; ++s) {
        MEMBAR();
        const u32x4 g0 = ld_sc1(rs, WS_FLAGS + lane * 16), g1 = ld_sc1(rs, WS_FLAGS + 1024 + lane * 16);
        const unsigned bad = ((g0[0] ^ flo) | (g0[1] ^ tag)) | ((g0[2] ^ flo) | (g0[3] ^ tag)) | ((g1[0] ^ flo) | (g1[1] ^ tag)) | ((g1[2] ^ flo) | (g1[3] ^ tag));
        if (__all(bad == 0u)) break;
        __builtin_amdgcn_s_sleep(PSLEEP);
      }
      lds_put(L, C_FLAGOK, 1u);
      lds_wait(L, C_PROG, ITK);
      if (lane == 0) {
        const unsigned tk = __hip_atomic_fetch_add((GAS unsigned*)(ws + WS_TICKET + j * 128), 1u, RLX_AGENT);
        lds_put(L, C_ROLE, tk * 8u + (unsigned)j + 1u);
      }
    } else if (w == 0) {
      u32x4 a0, a1, b0, b1;
      const int off = WS_QP + (h * 8 + 2 * r4) * 512 + c16 * 32;
      lds_wait(L, C_FLAGOK, 1u);
      wait_until(tstart + T2);
      NOUNROLL for (unsigned s = 0; s < SPIN_G; ++s) {
        MEMBAR();
        a0 = ld_sc1(rs, off); a1 = ld_sc1(rs, off + 16); b0 = ld_sc1(rs, off + 512); b1 = ld_sc1(rs, off + 528);
        CNT_(16);
        if (__all(max(max(mx4(a0), mx4(a1)), max(mx4(b0), mx4(b1))) != SENT)) break;
        __builtin_amdgcn_s_sleep(PSLEEP);
      }
      float v[4];
#pragma unroll
      for (int k = 0; k < 4; ++k) v[k] = tr16(bf(a0[k]) + bf(b0[k]), bf(a1[k]) + bf(b1[k]));
      const float y0 = tr32(v[0], v[2]), y1 = tr32(v[1], v[3]);
      ((LAS unsigned*)(L + L_Q))[c16 * 4 + (r4 & 1) * 2 + (r4 >> 1)] = pack2(y0, y1);
      MEMBAR();
      lds_put(L, C_QREADY, 1u);
      ST_(4);
    } else if (b >= 240) {
      const int mc = ((b - 240) * 2 + (w - 2)) * 80 + 2 * (lane < 40 ? lane : 39);
      u32x4 g[4];
      lds_wait(L, C_FLAGOK, 1u);
      NOUNROLL for (unsigned s = 0; s < SPIN_G; ++s) {
        MEMBAR();
        unsigned bad = 0u;
#pragma unroll
        for (int i = 0; i < 4; ++i) g[i] = ld_sc1(rs, WS_MP + (i * 3840 + 1280 + mc) * 8);
#pragma unroll
        for (int i = 0; i < 4; ++i) bad |= (g[i][1] ^ tag) | (g[i][3] ^ tag);
        if (__all(bad == 0u)) break;
        __builtin_amdgcn_s_sleep(PSLEEP);
      }
      const float m0 = rbf((bf(g[0][0]) + bf(g[1][0])) + (bf(g[2][0]) + bf(g[3][0]))), m1v = rbf((bf(g[0][2]) + bf(g[1][2])) + (bf(g[2][2]) + bf(g[3][2])));
      st_sc1_64(rs, WS_M2 + mc * 4, u32x2{nosent(m0), nosent(m1v)});
    }
    lds_wait(L, C_ROLE, 1u);
    const int role = __builtin_amdgcn_readfirstlane(((volatile LAS unsigned*)L)[C_ROLE]) - 1;
    __builtin_amdgcn_s_setprio(0);
    const int r16 = lane >> 2, c4 = lane & 3;
    const int col32 = c4 * 8 + ((lane >> 3) & 1) * 4 + ((lane >> 4) & 1) * 2 + (lane >> 5);
    const int col64 = c8 * 8 + ((lane >> 3) & 1) * 4 + ((lane >> 4) & 1) * 2 + (lane >> 5);
    LAS float* YR = (LAS float*)(L + L_YR);
    if (role < N_M2 + N_WO) {
      const int g = (role - N_M2) & 7, cgp = (role - N_M2) >> 3, hw = 4 * g + w;
      u32x4 wt[16];
      TSTART(TA_WO);
      const u16* wb = wo + (size_t)(hw * 128 + 16 * r8) * D + cgp * 64 + c8 * 8;
#pragma unroll
      for (int i = 0; i < 16; ++i) { wt[i] = ldnt(wb + i * D); if (i >= TDEP && i < 15) vmwait<TDEP>(); SB(); }
      SB();
      ST_(5);
      u32x4 a[4]; u32x2 ml;
      const int off = WS_OP + (hw * 8 + 4 * (lane >> 5)) * 512 + (lane & 31) * 16;
      const int mo = WS_ST + (hw * 8 + c8) * 128;
      ml = ld_sc1_64(rs, mo);
      SB();
#pragma unroll
      for (int m = 0; m < 8; ++m) { prepack(wt[2 * m], wt[2 * m + 1]); pin(wt[2 * m]); pin(wt[2 * m + 1]); }
      float acc[8] = {0.f, 0.f, 0.f, 0.f, 0.f, 0.f, 0.f, 0.f};
      asm volatile("" : "+v"(acc[0]), "+v"(acc[1]), "+v"(acc[2]), "+v"(acc[3]), "+v"(acc[4]), "+v"(acc[5]), "+v"(acc[6]), "+v"(acc[7]));
      float wj[4];
      NOUNROLL for (unsigned s = 0; s < SPIN_G; ++s) {
        MEMBAR();
#pragma unroll
        for (int i = 0; i < 4; ++i) a[i] = ld_sc1(rs, off + i * 512);
        const bool mok = __all(max(ml[0], ml[1]) != SENT);
        {
          float M = bf(ml[0]);
          M = fmaxf(M, dpp<XOR1>(M)); M = fmaxf(M, dpp<XOR2>(M)); M = fmaxf(M, dpp<XOR7>(M));
          const float e = __expf(bf(ml[0]) - M);
          float Ls = bf(ml[1]) * e;
          Ls += dpp<XOR1>(Ls); Ls += dpp<XOR2>(Ls); Ls += dpp<XOR7>(Ls);
          const float wgt = e * __builtin_amdgcn_rcpf(Ls);
#pragma unroll
          for (int i = 0; i < 4; ++i) {
            const float lo = bf(__builtin_amdgcn_readlane(fb(wgt), i)), hi = bf(__builtin_amdgcn_readlane(fb(wgt), 4 + i));
            wj[i] = lane < 32 ? lo : hi;
          }
          asm volatile("" : "+v"(wj[0]), "+v"(wj[1]), "+v"(wj[2]), "+v"(wj[3]));
        }
        if (!mok) ml = ld_sc1_64(rs, mo);
        if (mok && __all(max(max(mx4(a[0]), mx4(a[1])), max(mx4(a[2]), mx4(a[3]))) != SENT)) break;
      }
      ST_(6);
      float t4[4];
#pragma unroll
      for (int k = 0; k < 4; ++k) t4[k] = fmaf(wj[0], bf(a[0][k]), wj[1] * bf(a[1][k])) + fmaf(wj[2], bf(a[2][k]), wj[3] * bf(a[3][k]));
      LAS unsigned* OB = (LAS unsigned*)(L + L_OB) + w * 64;
      OB[2 * (lane & 31) + (lane >> 5)] = pack2(tr32(t4[0], t4[2]), tr32(t4[1], t4[3]));
      MEMBAR();
      const u32x4 oa = *(volatile LAS u32x4*)(OB + 8 * r8), ob = *(volatile LAS u32x4*)(OB + 8 * r8 + 4);
#pragma unroll
      for (int m = 0; m < 4; ++m) mac_packed(acc, wt[2 * m], wt[2 * m + 1], oa[m]);
#pragma unroll
      for (int m = 0; m < 4; ++m) mac_packed(acc, wt[8 + 2 * m], wt[8 + 2 * m + 1], ob[m]);
      const float y = red8_b345(acc, lane);
      YR[w * 64 + col64] = y;
      HBAR();
      if (w == 0 && lane < 16) st_sc1(rs, WS_YP + (g * 640 + cgp * 64 + 4 * lane) * 4, nosent4(sum4(YR + 4 * lane, 64)));
      ST_(7);
    } else if (role < N_M2 + N_WO + NF) {
      const int f = role - (N_M2 + N_WO);
      const int row0 = 160 * w + 10 * r16;
      const int t = w * 64 + lane;
      const int tq = t < 160 ? t : 159;
      const bool fin = f < 20 && w == 3;
      const int oc = (f < 20 ? 32 * f : 0) + 4 * c8 + ((lane >> 4) & 1) * 2 + (lane >> 5);
      const u32x2 xq = __builtin_nontemporal_load((const u32x2*)(x + 4 * tq));
      LAS unsigned* X1 = (LAS unsigned*)(L + L_X1);
      u32x4 wt[18]; unsigned w2t[8];
      TSTART(TA_FFN);
      const u16* wb = w1 + (size_t)row0 * DFF + f * 32 + c4 * 8;
      const u16* w2b = w2 + (size_t)(f * 32 + 8 * w) * D;
#pragma unroll
      for (int i = 0; i < 18; ++i) {
        if (i < 10) wt[i] = ldnt(wb + (size_t)i * DFF);
        else {
          wt[i] = ldnt(w2b + (i - 10) * D + lane * 8);
          w2t[i - 10] = __builtin_nontemporal_load((const unsigned*)(w2b + (i - 10) * D + 512 + lane * 2));
        }
        if (i >= TDEPF && i < 17) vmwait<TDEPF>();
        SB();
      }
      SB();
      ST_(5);
      u32x4 ga; u32x2 sbA, sbB, cbA, cbB; unsigned gbw;
      NOUNROLL for (unsigned s = 0; s < SPIN_G; ++s) {
        MEMBAR();
        ga = ld_sc1(rs, WS_M2 + tq * 16);
        gbw = __builtin_amdgcn_raw_buffer_load_b32(rs, WS_M2 + 7680 + oc * 4, 0, 16);
        sbA = ld_sc1_64(rs, WS_M2 + 2560 + (row0 + 2 * c4) * 4); sbB = ld_sc1_64(rs, WS_M2 + 2560 + (row0 + 8) * 4);
        cbA = ld_sc1_64(rs, WS_M2 + 5120 + (row0 + 2 * c4) * 4); cbB = ld_sc1_64(rs, WS_M2 + 5120 + (row0 + 8) * 4);
        const unsigned mm = max(max(mx4(ga), gbw), max(max(max(sbA[0], sbA[1]), max(sbB[0], sbB[1])), max(max(cbA[0], cbA[1]), max(cbB[0], cbB[1]))));
        if (__all(mm != SENT)) break;
        __builtin_amdgcn_s_sleep(2);
      }
      float c1[4] = {rbf(1.f + bf(cbA[0])), rbf(1.f + bf(cbA[1])), rbf(1.f + bf(cbB[0])), rbf(1.f + bf(cbB[1]))};
      float xv[4] = {bflo(xq[0]), bfhi(xq[0]), bflo(xq[1]), bfhi(xq[1])};
      asm volatile("" : "+v"(c1[0]), "+v"(c1[1]), "+v"(c1[2]), "+v"(c1[3]), "+v"(xv[0]), "+v"(xv[1]), "+v"(xv[2]), "+v"(xv[3]));
      const float s1[4] = {bf(sbA[0]), bf(sbA[1]), bf(sbB[0]), bf(sbB[1])};
#pragma unroll
      for (int m = 0; m < 9; ++m) { prepack(wt[2 * m], wt[2 * m + 1]); pin(wt[2 * m]); pin(wt[2 * m + 1]); }
#pragma unroll
      for (int m = 0; m < 4; ++m) {
        const unsigned lo = __builtin_amdgcn_perm(w2t[2 * m + 1], w2t[2 * m], 0x05040100u), hi = __builtin_amdgcn_perm(w2t[2 * m + 1], w2t[2 * m], 0x07060302u);
        w2t[2 * m] = lo; w2t[2 * m + 1] = hi;
        asm volatile("" : "+v"(w2t[2 * m]), "+v"(w2t[2 * m + 1]));
      }
      float acc[8] = {0.f, 0.f, 0.f, 0.f, 0.f, 0.f, 0.f, 0.f};
      asm volatile("" : "+v"(acc[0]), "+v"(acc[1]), "+v"(acc[2]), "+v"(acc[3]), "+v"(acc[4]), "+v"(acc[5]), "+v"(acc[6]), "+v"(acc[7]));
      if (w < 3) {
        u32x4 a[8];
        NOUNROLL for (unsigned s = 0; s < SPIN_G; ++s) {
          MEMBAR();
#pragma unroll
          for (int i = 0; i < 8; ++i) a[i] = ld_sc1(rs, WS_YP + i * 2560 + tq * 16);
          unsigned mm = mx4(a[0]);
#pragma unroll
          for (int i = 1; i < 8; ++i) mm = max(mm, mx4(a[i]));
          if (__all(mm != SENT)) break;
        }
        ST_(6);
        float x1[4];
#pragma unroll
        for (int k = 0; k < 4; ++k) {
          const float at = ((bf(a[0][k]) + bf(a[1][k])) + (bf(a[2][k]) + bf(a[3][k]))) + ((bf(a[4][k]) + bf(a[5][k])) + (bf(a[6][k]) + bf(a[7][k])));
          x1[k] = xv[k] + rbf(bf(ga[k]) * rbf(at));
        }
        *(LAS u32x2*)(X1 + 2 * tq) = u32x2{pack2(x1[0], x1[1]), pack2(x1[2], x1[3])};
      }
      HBAR();
      unsigned hp5[5];
      {
        const u32x4 ya = *(LAS u32x4*)(X1 + 4 * lane);
        const unsigned yb = X1[256 + lane];
        const unsigned hA = X1[(row0 >> 1) + c4], hB = X1[(row0 >> 1) + 4];
        float sa_ = dot2(yb, 0x3F803F80u, 0.f), sq_ = dot2(yb, yb, 0.f);
#pragma unroll
        for (int k = 0; k < 4; ++k) { sa_ = dot2(ya[k], 0x3F803F80u, sa_); sq_ = dot2(ya[k], ya[k], sq_); }
        float r = tr32(sa_, sq_);
        r = tr16(r, r);
        r = row_sum(r);
        const float mean = bf(__builtin_amdgcn_readlane(fb(r), 0)) * (1.f / 640.f);
        float var = fmaf(-mean, mean, bf(__builtin_amdgcn_readlane(fb(r), 32)) * (1.f / 640.f));
        if (!(var * 1024.f > mean * mean)) {
          float q2 = 0.f;
#pragma unroll
          for (int k = 0; k < 4; ++k) { const float d0 = bflo(ya[k]) - mean, d1 = bfhi(ya[k]) - mean; q2 += d0 * d0 + d1 * d1; }
          const float d0 = bflo(yb) - mean, d1 = bfhi(yb) - mean; q2 += d0 * d0 + d1 * d1;
          var = wave_sum(q2) * (1.f / 640.f);
        }
        const float mr = rbf(mean);
        const float rden = __builtin_amdgcn_rcpf(rbf(__builtin_amdgcn_sqrtf(rbf(rbf(var) + 1e-5f))));
        const float xr2[4] = {bflo(hA), bfhi(hA), bflo(hB), bfhi(hB)};
        float hv[4];
#pragma unroll
        for (int k = 0; k < 4; ++k) hv[k] = rbf(rbf(rbf(xr2[k] - mr) * rden) * c1[k]) + s1[k];
        const unsigned pA = pack2(hv[0], hv[1]);
        hp5[4] = pack2(hv[2], hv[3]);
        hp5[0] = __builtin_amdgcn_mov_dpp((int)pA, 0x00, 0xf, 0xf, true);
        hp5[1] = __builtin_amdgcn_mov_dpp((int)pA, 0x55, 0xf, 0xf, true);
        hp5[2] = __builtin_amdgcn_mov_dpp((int)pA, 0xAA, 0xf, 0xf, true);
        hp5[3] = __builtin_amdgcn_mov_dpp((int)pA, 0xFF, 0xf, 0xf, true);
      }
      ST_(7);
      {
#pragma unroll
        for (int m = 0; m < 5; ++m) mac_packed(acc, wt[2 * m], wt[2 * m + 1], hp5[m]);
        float y = red8_b345(acc, lane);
        y += xor4(y);
        YR[w * 64 + col32] = y;
        float z[8] = {0.f, 0.f, 0.f, 0.f, 0.f, 0.f, 0.f, 0.f};
        float z8 = 0.f, z9 = 0.f;
        asm volatile("" : "+v"(z[0]), "+v"(z[1]), "+v"(z[2]), "+v"(z[3]), "+v"(z[4]), "+v"(z[5]), "+v"(z[6]), "+v"(z[7]), "+v"(z8), "+v"(z9));
        HBAR();
        const LAS float* yp = YR + 8 * w + c8;
        float hd = (yp[0] + yp[64]) + (yp[128] + yp[192]);
        hd = hd < 0.f ? 0.f : hd;
        const unsigned hpk = pack2(hd, dpp<XOR1>(hd));
#pragma unroll
        for (int m = 0; m < 4; ++m) {
          const unsigned hp = __builtin_amdgcn_readlane(hpk, 2 * m);
          mac_packed(z, wt[10 + 2 * m], wt[10 + 2 * m + 1], hp);
          z8 = dot2(w2t[2 * m], hp, z8);
          z9 = dot2(w2t[2 * m + 1], hp, z9);
        }
        LAS float* ZR = (LAS float*)(L + L_ZR);
        *(LAS f32x4*)(ZR + w * 640 + 8 * lane) = f32x4{z[0], z[1], z[2], z[3]};
        *(LAS f32x4*)(ZR + w * 640 + 8 * lane + 4) = f32x4{z[4], z[5], z[6], z[7]};
        *(LAS f32x2*)(ZR + w * 640 + 512 + 2 * lane) = f32x2{z8, z9};
        HBAR();
        if (t < 160) st_sc1(rs, WS_ZP + f * 2560 + t * 16, nosent4(sum4(ZR + 4 * t, 640)));
      }
      ST_(8);
      if (fin) {
        u32x4 zp[7];
        const int zo = WS_ZP + r8 * 2560 + f * 128 + c8 * 16;
        const int z6 = r8 < NF - 48 ? zo + 48 * 2560 : 0x7FFFFF00;
        const float xo = bflo((unsigned)((LAS u16*)X1)[oc]);
        NOUNROLL for (unsigned s = 0; s < SPIN_G; ++s) {
          MEMBAR();
#pragma unroll
          for (int i = 0; i < 6; ++i) zp[i] = ld_sc1(rs, zo + i * 8 * 2560);
          zp[6] = ld_sc1(rs, z6);
          unsigned mm = mx4(zp[0]);
#pragma unroll
          for (int i = 1; i < 7; ++i) mm = max(mm, mx4(zp[i]));
          if (__all(mm != SENT)) break;
        }
        ST_(9);
        float r[4];
#pragma unroll
        for (int k = 0; k < 4; ++k) r[k] = ((bf(zp[0][k]) + bf(zp[1][k])) + (bf(zp[2][k]) + bf(zp[3][k]))) + ((bf(zp[4][k]) + bf(zp[5][k])) + bf(zp[6][k]));
        float v = tr32(tr16(r[0], r[2]), tr16(r[1], r[3]));
        v += dpp<XOR8>(v);
        float dbgv = 0.f;
        if (f == 0) {
          const u64 now = __builtin_amdgcn_s_memrealtime();
          float mn = 1e30f, mx = -1e30f;
          for (int i = 0; i < 4; ++i) { const u64 t = __hip_atomic_load((gu64*)(ws + (3 << 20)) + lane + 64 * i, RLX_AGENT); const float d = (float)(long long)(now - t) * 0.01f; mn = fminf(mn, d); mx = fmaxf(mx, d); }
          mx = wave_max(mx); mn = -wave_max(-mn);
          const float val = DBGSEL == 0 ? mx : (mx - mn);
          if (val > DBGTHR && oc == 0) dbgv = val;
        }
        out[oc] = (u16)f2bf(xo + rbf(bf(gbw) * rbf(v)) + dbgv);
        if (f == 0 && lane == 0) {
          const u64 e1 = (((u64)Ev[1] << 32) | (u64)Ev[0]) + 1ull;
          st_sc1_64(rs, WS_EPOCH, u32x2{(unsigned)e1, (unsigned)(e1 >> 32)});
        }
        ST_(10);
      }
    }
  }
}

extern "C" void kernel_launch(void* const* d_in, const int* in_sizes, int n_in, void* d_out, int out_size, void* d_ws,
                              size_t ws_size, hipStream_t stream) {
  (void)in_sizes; (void)n_in; (void)out_size; (void)ws_size;
  (void)hipFuncSetAttribute((const void*)mega, hipFuncAttributeMaxDynamicSharedMemorySize, LDS_BYTES);
  hipLaunchKernelGGL(mega, dim3(256), dim3(768), LDS_BYTES, stream, (const u16*)d_in[0], (const u16*)d_in[1], (const u16*)d_in[2],
                     (const u16*)d_in[3], (const u16*)d_in[4], (const u16*)d_in[6], (const u16*)d_in[7], (const u16*)d_in[8],
                     (const u16*)d_in[9], (u16*)d_out, (unsigned char*)d_ws);
}
#define STAMP_ROLE 1
#define STAMP_CLS(r) ((r) < N_M2 ? 3 : (r) < N_M2 + N_WO ? 2 : (r) < N_M2 + N_WO + NF ? 4 : 0)
#define NWAVES 12
#define NSTREAM 8
```

```cpp
#include <hip/hip_runtime.h>
#include <hip/hip_bf16.h>
#include <stdint.h>

#define STAMP 0
#define PSLEEP 16
#define I1 12
#define I2 21
#define I3 17
#define IT 31
#define IS 31
#define ITK 24
#define PREB 1
#define IB 25
#define TA_M2 0
#define TA_WO 2
#define TA_FFN 2
#define TDEPF 40
#define TSTART(m) do { if ((m) == 2) lds_wait(L, C_PV, 9u); else if ((m) == 1) lds_wait8(L, IT); } while (0)
#define MP10 1
#define T1 380
#define T3 500
#define T2 680
#define TM2 950
#define DEPA 1
#define ISW 14
#define WQE 1
#define FAUX 16
#define MLAUX 16
#define CAUX 16
#define CPF 0
#define CLEN 17408
#define DEP 2
#define TDEP 40

#define GAS __attribute__((address_space(1)))
#define LAS __attribute__((address_space(3)))
#define RLX_AGENT __ATOMIC_RELAXED, __HIP_MEMORY_SCOPE_AGENT
typedef unsigned short u16;
typedef unsigned long long u64;
typedef unsigned u32x4 __attribute__((ext_vector_type(4)));
typedef unsigned u32x2 __attribute__((ext_vector_type(2)));
typedef float f32x4 __attribute__((ext_vector_type(4)));
typedef float f32x2 __attribute__((ext_vector_type(2)));
typedef __bf16 bf16x2 __attribute__((ext_vector_type(2)));
typedef GAS u64 gu64;
typedef __amdgpu_buffer_rsrc_t rsrc_t;

constexpr int D = 640, HQ = 32, SK = 16384, DH = 128, WIN = 4096, DFF = 1728;
constexpr int NF = 54;

constexpr int WS_EPOCH = 0;
constexpr int WS_FLAGS = 4096;
constexpr int WS_TICKET = 2048;
constexpr int NMP = 4;
constexpr int WS_MP = 8192;
constexpr int WS_QP = 131072;
constexpr int WS_OP = WS_QP + 131072;
constexpr int WS_ST = WS_OP + 131072;
constexpr int WS_YP = WS_ST + 32768;
constexpr int WS_M2 = WS_YP + 20480;
constexpr int WS_ZP = WS_M2 + 10240;
constexpr int WS_END = WS_ZP + NF * 2560;
constexpr int N_M2 = 0, N_WO = 80;
constexpr int WS_STAMP = 2 << 20;

constexpr unsigned SENT = 0xFFFFFFFFu;
constexpr unsigned SPIN_G = 1u << 18;
constexpr unsigned SPIN_L = 1u << 22;

constexpr int C_PROG = 0;
constexpr int C_MOD1 = 8, C_QP = 9, C_QREADY = 10, C_M = 11, C_ROLE = 12, C_PV = 13, C_FLAGOK = 14;
constexpr int L_P = 128;
constexpr int L_MR = L_P + 1024;
constexpr int L_QR = L_MR + 1024;
constexpr int L_OR = L_QR + 2048;
constexpr int L_OB = L_OR + 4096;
constexpr int L_YR = L_OB + 1024;
constexpr int L_STAT = L_YR + 1024;
constexpr int L_Q = L_STAT + 64;
constexpr int L_X1 = L_Q + 256;
constexpr int L_ZR = L_X1 + 2560;
constexpr int L_END = L_ZR + 10240;
constexpr int LDS_BYTES = 96 * 1024;
static_assert(L_END <= LDS_BYTES, "lds");

__device__ __forceinline__ float bflo(unsigned w) { return __builtin_bit_cast(float, w << 16); }
__device__ __forceinline__ float bfhi(unsigned w) { return __builtin_bit_cast(float, w & 0xffff0000u); }
__device__ __forceinline__ unsigned f2bf(float f) { return (unsigned)__builtin_bit_cast(u16, (__bf16)f); }
__device__ __forceinline__ float rbf(float f) { return bflo(f2bf(f)); }
__device__ __forceinline__ unsigned pack2(float lo, float hi) {
  bf16x2 v = {(__bf16)lo, (__bf16)hi};
  return __builtin_bit_cast(unsigned, v);
}
__device__ __forceinline__ float dot2(unsigned a, unsigned b, float acc) {
  return __builtin_amdgcn_fdot2_f32_bf16(__builtin_bit_cast(bf16x2, a), __builtin_bit_cast(bf16x2, b), acc, false);
}
__device__ __forceinline__ unsigned fb(float f) { return __builtin_bit_cast(unsigned, f); }
__device__ __forceinline__ float bf(unsigned u) { return __builtin_bit_cast(float, u); }
__device__ __forceinline__ unsigned nosent(float f) { return min(fb(f), 0xFFFFFFFEu); }
__device__ __forceinline__ u32x4 nosent4(f32x4 s) { return u32x4{nosent(s[0]), nosent(s[1]), nosent(s[2]), nosent(s[3])}; }
template <int CTRL> __device__ __forceinline__ float dpp(float x) {
  return __builtin_bit_cast(float, __builtin_amdgcn_mov_dpp(__builtin_bit_cast(int, x), CTRL, 0xf, 0xf, true));
}
constexpr int XOR1 = 0xB1, XOR2 = 0x4E, XOR7 = 0x141, XOR8 = 0x128;
__device__ __forceinline__ float xor4(float x) {
  int v = __builtin_bit_cast(int, x);
  int r = __builtin_amdgcn_update_dpp(0, v, 0x114, 0xf, 0xa, false);
  r = __builtin_amdgcn_update_dpp(r, v, 0x104, 0xf, 0x5, false);
  return __builtin_bit_cast(float, r);
}
__device__ __forceinline__ float tr16(float a, float b) {
  auto s = __builtin_amdgcn_permlane16_swap(fb(a), fb(b), false, false);
  return bf(s[0]) + bf(s[1]);
}
__device__ __forceinline__ float tr32(float a, float b) {
  auto s = __builtin_amdgcn_permlane32_swap(fb(a), fb(b), false, false);
  return bf(s[0]) + bf(s[1]);
}
__device__ __forceinline__ float mx16(float a) {
  auto s = __builtin_amdgcn_permlane16_swap(fb(a), fb(a), false, false);
  return fmaxf(bf(s[0]), bf(s[1]));
}
__device__ __forceinline__ float mx32(float a) {
  auto s = __builtin_amdgcn_permlane32_swap(fb(a), fb(a), false, false);
  return fmaxf(bf(s[0]), bf(s[1]));
}
__device__ __forceinline__ float row_sum(float x) {
  x += dpp<XOR1>(x); x += dpp<XOR2>(x); x += dpp<XOR7>(x); x += dpp<XOR8>(x); return x;
}
__device__ __forceinline__ float row_max(float x) {
  x = fmaxf(x, dpp<XOR1>(x)); x = fmaxf(x, dpp<XOR2>(x)); x = fmaxf(x, dpp<XOR7>(x)); x = fmaxf(x, dpp<XOR8>(x)); return x;
}
__device__ __forceinline__ float wave_sum(float x) { x = row_sum(x); x = tr16(x, x); return tr32(x, x); }
__device__ __forceinline__ float wave_max(float x) { x = row_max(x); x = mx16(x); return mx32(x); }

__device__ __forceinline__ unsigned bij32(unsigned x) {
  x += 0x9E3779B9u; x ^= x >> 16; x *= 0x85EBCA6Bu; x ^= x >> 13; x *= 0xC2B2AE35u; x ^= x >> 16; return x;
}
__device__ __forceinline__ rsrc_t mkrs(const void* p, int bytes) { return __builtin_amdgcn_make_buffer_rsrc((void*)p, 0, bytes, 0x00020000); }
__device__ __forceinline__ u32x4 ldnt(const void* p) { return __builtin_nontemporal_load(reinterpret_cast<const u32x4*>(p)); }
__device__ __forceinline__ u32x4 ld_sc1(rsrc_t rs, int off) { return __builtin_amdgcn_raw_buffer_load_b128(rs, off, 0, 16); }
__device__ __forceinline__ u32x2 ld_sc1_64(rsrc_t rs, int off) { return __builtin_amdgcn_raw_buffer_load_b64(rs, off, 0, 16); }
__device__ __forceinline__ void st_sc1(rsrc_t rs, int off, u32x4 v) { __builtin_amdgcn_raw_buffer_store_b128(v, rs, off, 0, 16); }
__device__ __forceinline__ void st_sc1_64(rsrc_t rs, int off, u32x2 v) { __builtin_amdgcn_raw_buffer_store_b64(v, rs, off, 0, 16); }
__device__ __forceinline__ u32x4 ld_f(rsrc_t rs, int off) { return __builtin_amdgcn_raw_buffer_load_b128(rs, off, 0, FAUX); }
__device__ __forceinline__ u32x2 ld_f64(rsrc_t rs, int off) { return __builtin_amdgcn_raw_buffer_load_b64(rs, off, 0, FAUX); }
__device__ __forceinline__ void st_f(rsrc_t rs, int off, u32x4 v) { __builtin_amdgcn_raw_buffer_store_b128(v, rs, off, 0, FAUX); }
__device__ __forceinline__ void st_f64(rsrc_t rs, int off, u32x2 v) { __builtin_amdgcn_raw_buffer_store_b64(v, rs, off, 0, FAUX); }
__device__ __forceinline__ void st_c(rsrc_t rs, int off, u32x4 v) { __builtin_amdgcn_raw_buffer_store_b128(v, rs, off, 0, CAUX); }
__device__ __forceinline__ unsigned mx4(u32x4 v) { return max(max(v[0], v[1]), max(v[2], v[3])); }
#define MEMBAR() asm volatile("" ::: "memory")
#define LGKM0() asm volatile("s_waitcnt lgkmcnt(0)" ::: "memory")
#define SB() __builtin_amdgcn_sched_barrier(0)
#define NOUNROLL _Pragma("nounroll")

__device__ __forceinline__ void lds_put(LAS unsigned char* L, int word, unsigned v) { ((volatile LAS unsigned*)L)[word] = v; }
__device__ __forceinline__ void lds_wait(LAS unsigned char* L, int word, unsigned v) {
  NOUNROLL for (unsigned s = 0; s < SPIN_L; ++s) {
    if (((volatile LAS unsigned*)L)[word] >= v) break;
    __builtin_amdgcn_s_sleep(1);
  }
  MEMBAR();
}
__device__ __forceinline__ void lds_wait4(LAS unsigned char* L, int word, unsigned v) {
  NOUNROLL for (unsigned s = 0; s < SPIN_L; ++s) {
    const u32x4 q = *(volatile LAS u32x4*)(L + word * 4);
    if (min(min(q[0], q[1]), min(q[2], q[3])) >= v) break;
    __builtin_amdgcn_s_sleep(1);
  }
  MEMBAR();
}
__device__ __forceinline__ void lds_spin(LAS unsigned char* L, int word, unsigned v) {
  NOUNROLL for (unsigned s = 0; s < SPIN_L; ++s) {
    if (((volatile LAS unsigned*)L)[word] >= v) break;
  }
  MEMBAR();
}
__device__ __forceinline__ unsigned lds_arrive(int word) {
  unsigned old;
  asm volatile("s_mov_b64 exec, 1\n\tds_add_rtn_u32 %0, %1, %2\n\ts_mov_b64 exec, -1\n\ts_waitcnt lgkmcnt(0)"
               : "=&v"(old) : "v"(word * 4), "v"(1u) : "memory");
  return __builtin_amdgcn_readfirstlane(old);
}
__device__ __forceinline__ void lds_arrive_nr(int word) {
  asm volatile("s_mov_b64 exec, 1\n\tds_add_u32 %0, %1\n\ts_mov_b64 exec, -1" :: "v"(word * 4), "v"(1u) : "memory");
}
__device__ __forceinline__ void lds_wait8(LAS unsigned char* L, unsigned v) {
  NOUNROLL for (unsigned s = 0; s < SPIN_L; ++s) {
    const u32x4 q = *(volatile LAS u32x4*)L, r = *(volatile LAS u32x4*)(L + 16);
    if (min(min(min(q[0], q[1]), min(q[2], q[3])), min(min(r[0], r[1]), min(r[2], r[3]))) >= v) break;
    __builtin_amdgcn_s_sleep(2);
  }
  MEMBAR();
}
#define HBAR() do { LGKM0(); __builtin_amdgcn_s_barrier(); MEMBAR(); } while (0)
__device__ __forceinline__ void wait_until(u64 t) {
  NOUNROLL for (unsigned s = 0; s < 4096u; ++s) {
    if (__builtin_amdgcn_s_memrealtime() >= t) break;
    __builtin_amdgcn_s_sleep(2);
  }
  MEMBAR();
}
template <int N> __device__ __forceinline__ void vmwait() { asm volatile("s_waitcnt vmcnt(%0)" :: "i"(N) : "memory"); }

#define ST_(k) do {} while (0)
#define CNT_(sh) do {} while (0)

__device__ __forceinline__ float red8_b345(const float (&a)[8], int lane) {
  float v[4];
  const bool hi = (lane & 8) != 0;
#pragma unroll
  for (int k = 0; k < 4; ++k) {
    const float t1 = a[k] + dpp<XOR8>(a[k]), t2 = a[k + 4] + dpp<XOR8>(a[k + 4]);
    v[k] = hi ? t2 : t1;
  }
  const float u0 = tr16(v[0], v[2]), u1 = tr16(v[1], v[3]);
  return tr32(u0, u1);
}
__device__ __forceinline__ void prepack(u32x4& wa, u32x4& wb) {
#pragma unroll
  for (int d = 0; d < 4; ++d) {
    const unsigned lo = __builtin_amdgcn_perm(wb[d], wa[d], 0x05040100u);
    const unsigned hi = __builtin_amdgcn_perm(wb[d], wa[d], 0x07060302u);
    wa[d] = lo; wb[d] = hi;
  }
}
__device__ __forceinline__ void pin(u32x4& a) { asm volatile("" : "+v"(a)); }
__device__ __forceinline__ void mac_packed(float (&acc)[8], u32x4 wa, u32x4 wb, unsigned xp) {
#pragma unroll
  for (int d = 0; d < 4; ++d) { acc[2 * d] = dot2(wa[d], xp, acc[2 * d]); acc[2 * d + 1] = dot2(wb[d], xp, acc[2 * d + 1]); }
}
__device__ __forceinline__ void mac_pair(float (&acc)[8], u32x4 wa, u32x4 wb, unsigned xp) { prepack(wa, wb); mac_packed(acc, wa, wb, xp); }
__device__ __forceinline__ float silu_f(float v) { return v * __builtin_amdgcn_rcpf(1.f + __expf(-v)); }
__device__ __forceinline__ f32x4 sum4(const LAS float* p, int stride) {
  return (*(const LAS f32x4*)p + *(const LAS f32x4*)(p + stride)) + (*(const LAS f32x4*)(p + 2 * stride) + *(const LAS f32x4*)(p + 3 * stride));
}

__global__ void __launch_bounds__(768, 3)
mega(const u16* __restrict__ x, const u16* __restrict__ kc, const u16* __restrict__ vc, const u16* __restrict__ wq,
     const u16* __restrict__ wo, const u16* __restrict__ w1, const u16* __restrict__ w2, const u16* __restrict__ cnd,
     const u16* __restrict__ wmod, u16* __restrict__ out, unsigned char* ws) {
  extern __shared__ __attribute__((aligned(16))) unsigned char smem[];
  LAS unsigned char* L = (LAS unsigned char*)smem;
  const int tid = threadIdx.x, lane = tid & 63, wave = __builtin_amdgcn_readfirstlane(tid >> 6);
  const int b = blockIdx.x;
  const int h = (b & 7) * 4 + (b >> 6), j = (b >> 3) & 7, u = h * 8 + j;
  const int r4 = lane >> 4, c16 = lane & 15;
  const rsrc_t rs = mkrs(ws, WS_END);
  u64 pc0;
  asm volatile("s_getpc_b64 %0" : "=s"(pc0));
  ST_(0);

  if (wave < 8) {
    const size_t kvoff = ((size_t)h * SK + (SK - WIN) + j * 512) * DH;
    const rsrc_t rk = mkrs(kc + kvoff, 131072), rv = mkrs(vc + kvoff, 131072);
    const int rot = (b * 8 + (b >> 4) + wave) * 1024;
    u32x4 big[32];
    u32x4 pp0, pp1;
    float mw;
    LAS float* STAT = (LAS float*)(L + L_STAT);
#define LOAD_STEP(i) do { \
      big[i] = __builtin_amdgcn_raw_buffer_load_b128((i) < 16 ? rk : rv, lane * 16, (((i) & 15) * 8192 + rot) & 131071, 2); \
      if ((i) == PREB - 1) __builtin_amdgcn_s_barrier();     \
      if ((i) == ITK || (i) == IT) lds_put(L, C_PROG + wave, (unsigned)(i)); \
      if ((i) >= 16 + DEP + 2 && (((i) - DEP) & 1) == 0 && (i) <= IB) { prepack(big[(i) - DEP - 2], big[(i) - DEP - 1]); pin(big[(i) - DEP - 2]); pin(big[(i) - DEP - 1]); } \
      if ((i) < ISW) { if ((i) >= DEPA) vmwait<DEPA>(); } else if ((i) < IB && (i) != IS) vmwait<DEP>(); \
      SB(); } while (0)
#pragma unroll
    for (int i = 0; i <= IS; ++i) LOAD_STEP(i);
    {
      ST_(2); lds_wait(L, C_QREADY, 1u); ST_(3);
      const u32x4 qp = *(LAS u32x4*)(L + L_Q + c16 * 16);
      float xs[16];
#pragma unroll
      for (int ii = 0; ii < 16; ++ii) {
        float d = 0.f;
#pragma unroll
        for (int k = 0; k < 4; ++k) d = dot2(big[ii][k], qp[k], d);
        xs[ii] = d;
      }
#define TR_STEP(HALF, CTRL, BIT) _Pragma("unroll") for (int q = 0; q < (HALF); ++q) { \
      const float lo_ = xs[q], hi_ = xs[q + (HALF)]; const float t1_ = lo_ + dpp<CTRL>(lo_), t2_ = hi_ + dpp<CTRL>(hi_); \
      xs[q] = (c16 & (BIT)) ? t2_ : t1_; }
      TR_STEP(8, XOR8, 8) TR_STEP(4, XOR7, 4) TR_STEP(2, XOR2, 2) TR_STEP(1, XOR1, 1)
#undef TR_STEP
      const float sc = rbf(rbf(xs[0]) * 0.08838834764831845f);
      mw = wave_max(sc);
      const float e = __expf(sc - mw);
      LAS u16* P = (LAS u16*)(L + L_P) + wave * 64;
      P[lane] = (u16)f2bf(e);
      MEMBAR();
      pp0 = *(volatile LAS u32x4*)(P + r4 * 16); pp1 = *(volatile LAS u32x4*)(P + r4 * 16 + 8);
      STAT[wave] = mw;
      STAT[8 + wave] = wave_sum(e);
      if (lds_arrive(C_M) == 7u) {
        const float m8 = STAT[lane & 7], l8 = STAT[8 + (lane & 7)];
        float mc = fmaxf(m8, dpp<XOR1>(m8)); mc = fmaxf(mc, dpp<XOR2>(mc)); mc = fmaxf(mc, dpp<XOR7>(mc));
        float lc = l8 * __expf(m8 - mc);
        lc += dpp<XOR1>(lc); lc += dpp<XOR2>(lc); lc += dpp<XOR7>(lc);
        if (lane == 0) __builtin_amdgcn_raw_buffer_store_b64(u32x2{nosent(mc), nosent(lc)}, rs, WS_ST + u * 128, 0, MLAUX);
      }
          ST_(4);
      }
#pragma unroll
    for (int i = IS + 1; i < 32; ++i) LOAD_STEP(i);
    ST_(1);
    __builtin_amdgcn_s_setprio(2);
    {
      float acc[8] = {0.f, 0.f, 0.f, 0.f, 0.f, 0.f, 0.f, 0.f};
      constexpr int NPRE = ((IB < 30 ? IB : 30) - 16 - DEP) / 2;
      float scl;
#pragma unroll
      for (int m = 0; m < 8; ++m) {
        if (m == NPRE) {
          lds_spin(L, C_M, 8u);
          const f32x4 ma = *(LAS f32x4*)STAT, mb = *(LAS f32x4*)(STAT + 4);
          const float mc = fmaxf(fmaxf(fmaxf(ma[0], ma[1]), fmaxf(ma[2], ma[3])), fmaxf(fmaxf(mb[0], mb[1]), fmaxf(mb[2], mb[3])));
          scl = __expf(mw - mc);
          asm volatile("" : "+v"(scl));
        }
        if (m >= NPRE) prepack(big[16 + 2 * m], big[16 + 2 * m + 1]);
        mac_packed(acc, big[16 + 2 * m], big[16 + 2 * m + 1], m < 4 ? pp0[m & 3] : pp1[m & 3]);
        SB();
      }
      ST_(5);
      float v[4];
#pragma unroll
      for (int k = 0; k < 4; ++k) v[k] = tr16(acc[k], acc[k + 4]);
      const float y0 = tr32(v[0], v[2]) * scl, y1 = tr32(v[1], v[3]) * scl;
      LAS float* OR = (LAS float*)(L + L_OR);
      *(LAS f32x2*)(OR + wave * 128 + c16 * 8 + (r4 & 1) * 4 + (r4 >> 1) * 2) = f32x2{y0, y1};
      if (lds_arrive(C_PV) == 7u) {
        const f32x4 s = sum4(OR + (lane >> 5) * 512 + 4 * (lane & 31), 128);
        f32x4 t;
#pragma unroll
        for (int k = 0; k < 4; ++k) t[k] = tr32(s[k], s[k]);
        if (lane < 32) st_sc1(rs, WS_OP + u * 512 + lane * 16, nosent4(t));
        MEMBAR();
        lds_put(L, C_PV, 9u);
      }
      ST_(6);
    }
  } else {
    const int w = wave - 8;
    int zero_v;
    asm volatile("v_mov_b32 %0, 0" : "=v"(zero_v));
    const int r8 = lane >> 3, c8 = lane & 7;
    __builtin_amdgcn_s_setprio(3);
    const u64 tstart = __builtin_amdgcn_s_memrealtime();
    unsigned cpf0 = 0;
    const bool m1 = b < 240;
    const int cg = b % 60, rg = b / 60;
    u32x4 wm[5]; unsigned cw0, cw1, cw2;
#define MOD_TILE_LOAD() do {  \
      const int row0 = 160 * rg + 40 * w; \
      const u16* wb = wmod + (size_t)row0 * (6 * D) + cg * 64 + c8 * 8; \
      wm[0] = ldnt(wb + (size_t)(2 * r8) * (6 * D)); wm[1] = ldnt(wb + (size_t)(2 * r8 + 1) * (6 * D)); \
      wm[2] = ldnt(wb + (size_t)(16 + 2 * r8) * (6 * D)); wm[3] = ldnt(wb + (size_t)(17 + 2 * r8) * (6 * D)); \
      wm[4] = ldnt(wb + (size_t)(32 + r8) * (6 * D)); \
      cw0 = __builtin_nontemporal_load((const unsigned*)(cnd + row0 + 2 * r8)); \
      cw1 = __builtin_nontemporal_load((const unsigned*)(cnd + row0 + 16 + 2 * r8)); \
      cw2 = __builtin_nontemporal_load(cnd + row0 + 32 + r8); \
     } while (0)
    if (m1 && cg < 20) MOD_TILE_LOAD();
    const u32x2 Ev = ld_sc1_64(rs, WS_EPOCH + zero_v);
    const int l20 = (lane & 31) < 20 ? (lane & 31) : 19;
    const int hrow = 80 * j + 20 * w + l20;
    const u32x4 xa = ldnt(x + lane * 8);
    const unsigned xb = __builtin_nontemporal_load((const unsigned*)x + 256 + lane);
    const float xr = bflo((unsigned)__builtin_nontemporal_load(x + hrow));
    if (w == 3) {
      if (lane < 32) lds_put(L, lane, 0u);
    }
    SB();
    LGKM0();
    __builtin_amdgcn_s_barrier();
    SB();
    if (CPF && b < CPF && w == 2) {
      const rsrc_t rc = mkrs((const void*)pc0, CLEN);
      unsigned c0 = __builtin_amdgcn_raw_buffer_load_b32(rc, lane * 128, 0, 0), c1 = __builtin_amdgcn_raw_buffer_load_b32(rc, lane * 128 + 8192, 0, 0),
               c2 = __builtin_amdgcn_raw_buffer_load_b32(rc, lane * 128 + 16384, 0, 0);
      cpf0 = c0 ^ c1 ^ c2;
    }
    u32x4 wqv[5];
    if (WQE) {
#pragma unroll
      for (int i = 0; i < 5; ++i) wqv[i] = ldnt(wq + (size_t)(80 * j + 20 * w + 5 * r4 + i) * (HQ * DH) + h * DH + c16 * 8);
      SB();
    }
    const unsigned tag = bij32(Ev[0]), flo = bij32(Ev[0] ^ Ev[1] ^ 0x5bd1e995u);
#define MOD_TILE_COMPUTE() do {  \
      float acc[8] = {0.f, 0.f, 0.f, 0.f, 0.f, 0.f, 0.f, 0.f}; \
      mac_pair(acc, wm[0], wm[1], pack2(silu_f(bflo(cw0)), silu_f(bfhi(cw0)))); \
      mac_pair(acc, wm[2], wm[3], pack2(silu_f(bflo(cw1)), silu_f(bfhi(cw1)))); \
      { \
        const unsigned sl = f2bf(silu_f(bflo(cw2))), sh = sl << 16; \
_Pragma("unroll")         for (int d = 0; d < 4; ++d) { acc[2 * d] = dot2(wm[4][d], sl, acc[2 * d]); acc[2 * d + 1] = dot2(wm[4][d], sh, acc[2 * d + 1]); } \
      } \
      const float y = red8_b345(acc, lane); \
      const int col = c8 * 8 + ((lane >> 3) & 1) * 4 + ((lane >> 4) & 1) * 2 + (lane >> 5); \
      LAS float* MR = (LAS float*)(L + L_MR); \
      MR[w * 64 + col] = y; \
      if (lds_arrive(C_MOD1) == 3u) { \
        const float s = (MR[lane] + MR[64 + lane]) + (MR[128 + lane] + MR[192 + lane]); \
        st_f64(rs, WS_MP + (rg * 3840 + cg * 64 + lane) * 8, u32x2{fb(s), tag}); \
      } \
     } while (0)
    if (m1 && cg < 20) MOD_TILE_COMPUTE();
    ST_(1);
    if (w == 3) {
      u32x4 sv = {SENT, SENT, SENT, SENT};
      st_c(rs, (lane < 32 ? WS_QP : WS_OP) + u * 512 + (lane & 31) * 16, sv);
      if (b >= 128 && b < 128 + NF) {
        st_c(rs, WS_ZP + (b - 128) * 2560 + lane * 16, sv);
        st_c(rs, WS_ZP + (b - 128) * 2560 + 1024 + lane * 16, sv);
      }
      int o2 = 0x7FFFFF00;
      if (lane == 63) o2 = WS_ST + u * 128;
      else if (b < 120) { if (lane < 16) o2 = WS_YP + b * 256 + lane * 16; }
      else if (b >= 128 && b < 128 + NF) { if (lane < 32) o2 = WS_ZP + (b - 128) * 2560 + 2048 + lane * 16; }
      else if (b == 255 && lane < 8) { o2 = WS_TICKET + lane * 128; sv = u32x4{0u, 0u, 0u, 0u}; }
      st_c(rs, o2, sv);
          vmwait<0>();
      if (lane == 0) st_f64(rs, WS_FLAGS + b * 8, u32x2{flo, tag});
    }
    {
      if (!WQE) {
#pragma unroll
        for (int i = 0; i < 5; ++i) wqv[i] = ldnt(wq + (size_t)(80 * j + 20 * w + 5 * r4 + i) * (HQ * DH) + h * DH + c16 * 8);
      }
      SB();
      float nrm;
      {
        float s = bflo(xb) + bfhi(xb);
#pragma unroll
        for (int i = 0; i < 4; ++i) s += bflo(xa[i]) + bfhi(xa[i]);
        const float mean = wave_sum(s) * (1.f / 640.f);
        float q2 = (bflo(xb) - mean) * (bflo(xb) - mean) + (bfhi(xb) - mean) * (bfhi(xb) - mean);
#pragma unroll
        for (int i = 0; i < 4; ++i) { const float d0 = bflo(xa[i]) - mean, d1 = bfhi(xa[i]) - mean; q2 += d0 * d0 + d1 * d1; }
        const float var = wave_sum(q2) * (1.f / 640.f);
        const float den = rbf(sqrtf(rbf(rbf(var) + 1e-5f)));
        nrm = rbf(rbf(xr - rbf(mean)) / den);
      }
      const int mp = WS_MP + ((lane >> 5) * 640 + hrow) * 8;
      u32x2 g[NMP];
      wait_until(tstart + T1);
      NOUNROLL for (unsigned s = 0; s < SPIN_G; ++s) {
        bool ok = true;
        MEMBAR();
#pragma unroll
        for (int i = 0; i < NMP; ++i) g[i] = ld_f64(rs, mp + i * 3840 * 8);
#pragma unroll
        for (int i = 0; i < NMP; ++i) ok = ok & (g[i][1] == tag);
        CNT_(0);
        if (__all(ok)) break;
        __builtin_amdgcn_s_sleep(PSLEEP);
      }
      const float mv = rbf((bf(g[0][0]) + bf(g[1][0])) + (bf(g[2][0]) + bf(g[3][0])));
      auto sw = __builtin_amdgcn_permlane32_swap(fb(mv), fb(mv), false, false);
      const float hval = rbf(rbf(nrm * rbf(1.f + bf(sw[1]))) + bf(sw[0]));
      ST_(2);
      float acc[8] = {0.f, 0.f, 0.f, 0.f, 0.f, 0.f, 0.f, 0.f};
#pragma unroll
      for (int i = 0; i < 5; ++i) {
        const float hk = __shfl(hval, 5 * r4 + i);
#pragma unroll
        for (int d = 0; d < 4; ++d) { acc[2 * d] = fmaf(hk, bflo(wqv[i][d]), acc[2 * d]); acc[2 * d + 1] = fmaf(hk, bfhi(wqv[i][d]), acc[2 * d + 1]); }
      }
      float v[4];
#pragma unroll
      for (int k = 0; k < 4; ++k) v[k] = tr16(acc[k], acc[k + 4]);
      const float y0 = tr32(v[0], v[2]), y1 = tr32(v[1], v[3]);
      LAS float* QR = (LAS float*)(L + L_QR);
      *(LAS f32x2*)(QR + w * 128 + c16 * 8 + (r4 & 1) * 4 + (r4 >> 1) * 2) = f32x2{y0, y1};
      if (lds_arrive(C_QP) == 3u) {
        if (lane < 32) st_f(rs, WS_QP + u * 512 + lane * 16, nosent4(sum4(QR + 4 * lane, 128)));
      }
    }
    asm volatile("" :: "v"(cpf0));
    ST_(3);
    if (m1 && cg >= 20) { MOD_TILE_LOAD(); MOD_TILE_COMPUTE(); }
    if (w == 1) {
      wait_until(tstart + T3);
      NOUNROLL for (unsigned s = 0; s < SPIN_G; ++s) {
        MEMBAR();
        const u32x4 g0 = ld_f(rs, WS_FLAGS + lane * 16), g1 = ld_f(rs, WS_FLAGS + 1024 + lane * 16);
        const unsigned bad = ((g0[0] ^ flo) | (g0[1] ^ tag)) | ((g0[2] ^ flo) | (g0[3] ^ tag)) | ((g1[0] ^ flo) | (g1[1] ^ tag)) | ((g1[2] ^ flo) | (g1[3] ^ tag));
        if (__all(bad == 0u)) break;
        __builtin_amdgcn_s_sleep(PSLEEP);
      }
      lds_put(L, C_FLAGOK, 1u);
      lds_wait(L, C_PROG, ITK);
      if (lane == 0) {
        const unsigned tk = __hip_atomic_fetch_add((GAS unsigned*)(ws + WS_TICKET + j * 128), 1u, RLX_AGENT);
        lds_put(L, C_ROLE, tk * 8u + (unsigned)j + 1u);
      }
    } else if (w == 0) {
      u32x4 a0, a1, b0, b1;
      const int off = WS_QP + (h * 8 + 2 * r4) * 512 + c16 * 32;
      lds_wait(L, C_FLAGOK, 1u);
      wait_until(tstart + T2);
      NOUNROLL for (unsigned s = 0; s < SPIN_G; ++s) {
        MEMBAR();
        a0 = ld_f(rs, off); a1 = ld_f(rs, off + 16); b0 = ld_f(rs, off + 512); b1 = ld_f(rs, off + 528);
        CNT_(16);
        if (__all(max(max(mx4(a0), mx4(a1)), max(mx4(b0), mx4(b1))) != SENT)) break;
        __builtin_amdgcn_s_sleep(PSLEEP);
      }
      float v[4];
#pragma unroll
      for (int k = 0; k < 4; ++k) v[k] = tr16(bf(a0[k]) + bf(b0[k]), bf(a1[k]) + bf(b1[k]));
      const float y0 = tr32(v[0], v[2]), y1 = tr32(v[1], v[3]);
      ((LAS unsigned*)(L + L_Q))[c16 * 4 + (r4 & 1) * 2 + (r4 >> 1)] = pack2(y0, y1);
      MEMBAR();
      lds_put(L, C_QREADY, 1u);
      ST_(4);
    } else if (b >= 240) {
      const int mc = ((b - 240) * 2 + (w - 2)) * 80 + 2 * (lane < 40 ? lane : 39);
      u32x4 g[4];
      lds_wait(L, C_FLAGOK, 1u);
      wait_until(tstart + TM2);
      NOUNROLL for (unsigned s = 0; s < SPIN_G; ++s) {
        MEMBAR();
        unsigned bad = 0u;
#pragma unroll
        for (int i = 0; i < 4; ++i) g[i] = ld_f(rs, WS_MP + (i * 3840 + 1280 + mc) * 8);
#pragma unroll
        for (int i = 0; i < 4; ++i) bad |= (g[i][1] ^ tag) | (g[i][3] ^ tag);
        if (__all(bad == 0u)) break;
        __builtin_amdgcn_s_sleep(PSLEEP);
      }
      const float m0 = rbf((bf(g[0][0]) + bf(g[1][0])) + (bf(g[2][0]) + bf(g[3][0]))), m1v = rbf((bf(g[0][2]) + bf(g[1][2])) + (bf(g[2][2]) + bf(g[3][2])));
      st_f64(rs, WS_M2 + mc * 4, u32x2{nosent(m0), nosent(m1v)});
    }
    lds_wait(L, C_ROLE, 1u);
    const int role = __builtin_amdgcn_readfirstlane(((volatile LAS unsigned*)L)[C_ROLE]) - 1;
    __builtin_amdgcn_s_setprio(0);
    const int r16 = lane >> 2, c4 = lane & 3;
    const int col32 = c4 * 8 + ((lane >> 3) & 1) * 4 + ((lane >> 4) & 1) * 2 + (lane >> 5);
    const int col64 = c8 * 8 + ((lane >> 3) & 1) * 4 + ((lane >> 4) & 1) * 2 + (lane >> 5);
    LAS float* YR = (LAS float*)(L + L_YR);
    if (role < N_M2 + N_WO) {
      const int g = (role - N_M2) & 7, cgp = (role - N_M2) >> 3, hw = 4 * g + w;
      u32x4 wt[16];
      TSTART(TA_WO);
      const u16* wb = wo + (size_t)(hw * 128 + 16 * r8) * D + cgp * 64 + c8 * 8;
#pragma unroll
      for (int i = 0; i < 16; ++i) { wt[i] = ldnt(wb + i * D); if (i >= TDEP && i < 15) vmwait<TDEP>(); SB(); }
      SB();
      ST_(5);
      u32x4 a[4]; u32x2 ml;
      const int off = WS_OP + (hw * 8 + 4 * (lane >> 5)) * 512 + (lane & 31) * 16;
      const int mo = WS_ST + (hw * 8 + c8) * 128;
      ml = ld_sc1_64(rs, mo);
      SB();
#pragma unroll
      for (int m = 0; m < 8; ++m) { prepack(wt[2 * m], wt[2 * m + 1]); pin(wt[2 * m]); pin(wt[2 * m + 1]); }
      float acc[8] = {0.f, 0.f, 0.f, 0.f, 0.f, 0.f, 0.f, 0.f};
      asm volatile("" : "+v"(acc[0]), "+v"(acc[1]), "+v"(acc[2]), "+v"(acc[3]), "+v"(acc[4]), "+v"(acc[5]), "+v"(acc[6]), "+v"(acc[7]));
      float wj[4];
      NOUNROLL for (unsigned s = 0; s < SPIN_G; ++s) {
        MEMBAR();
#pragma unroll
        for (int i = 0; i < 4; ++i) a[i] = ld_sc1(rs, off + i * 512);
        const bool mok = __all(max(ml[0], ml[1]) != SENT);
        {
          float M = bf(ml[0]);
          M = fmaxf(M, dpp<XOR1>(M)); M = fmaxf(M, dpp<XOR2>(M)); M = fmaxf(M, dpp<XOR7>(M));
          const float e = __expf(bf(ml[0]) - M);
          float Ls = bf(ml[1]) * e;
          Ls += dpp<XOR1>(Ls); Ls += dpp<XOR2>(Ls); Ls += dpp<XOR7>(Ls);
          const float wgt = e * __builtin_amdgcn_rcpf(Ls);
#pragma unroll
          for (int i = 0; i < 4; ++i) {
            const float lo = bf(__builtin_amdgcn_readlane(fb(wgt), i)), hi = bf(__builtin_amdgcn_readlane(fb(wgt), 4 + i));
            wj[i] = lane < 32 ? lo : hi;
          }
          asm volatile("" : "+v"(wj[0]), "+v"(wj[1]), "+v"(wj[2]), "+v"(wj[3]));
        }
        if (!mok) ml = ld_sc1_64(rs, mo);
        if (mok && __all(max(max(mx4(a[0]), mx4(a[1])), max(mx4(a[2]), mx4(a[3]))) != SENT)) break;
      }
      ST_(6);
      float t4[4];
#pragma unroll
      for (int k = 0; k < 4; ++k) t4[k] = fmaf(wj[0], bf(a[0][k]), wj[1] * bf(a[1][k])) + fmaf(wj[2], bf(a[2][k]), wj[3] * bf(a[3][k]));
      LAS unsigned* OB = (LAS unsigned*)(L + L_OB) + w * 64;
      OB[2 * (lane & 31) + (lane >> 5)] = pack2(tr32(t4[0], t4[2]), tr32(t4[1], t4[3]));
      MEMBAR();
      const u32x4 oa = *(volatile LAS u32x4*)(OB + 8 * r8), ob = *(volatile LAS u32x4*)(OB + 8 * r8 + 4);
#pragma unroll
      for (int m = 0; m < 4; ++m) mac_packed(acc, wt[2 * m], wt[2 * m + 1], oa[m]);
#pragma unroll
      for (int m = 0; m < 4; ++m) mac_packed(acc, wt[8 + 2 * m], wt[8 + 2 * m + 1], ob[m]);
      const float y = red8_b345(acc, lane);
      YR[w * 64 + col64] = y;
      HBAR();
      if (w == 0 && lane < 16) st_sc1(rs, WS_YP + (g * 640 + cgp * 64 + 4 * lane) * 4, nosent4(sum4(YR + 4 * lane, 64)));
      ST_(7);
    } else if (role < N_M2 + N_WO + NF) {
      const int f = role - (N_M2 + N_WO);
      const int row0 = 160 * w + 10 * r16;
      const int t = w * 64 + lane;
      const int tq = t < 160 ? t : 159;
      const bool fin = f < 20 && w == 3;
      const int oc = (f < 20 ? 32 * f : 0) + 4 * c8 + ((lane >> 4) & 1) * 2 + (lane >> 5);
      const u32x2 xq = __builtin_nontemporal_load((const u32x2*)(x + 4 * tq));
      LAS unsigned* X1 = (LAS unsigned*)(L + L_X1);
      u32x4 wt[18]; unsigned w2t[8];
      TSTART(TA_FFN);
      const u16* wb = w1 + (size_t)row0 * DFF + f * 32 + c4 * 8;
      const u16* w2b = w2 + (size_t)(f * 32 + 8 * w) * D;
#pragma unroll
      for (int i = 0; i < 18; ++i) {
        if (i < 10) wt[i] = ldnt(wb + (size_t)i * DFF);
        else {
          wt[i] = ldnt(w2b + (i - 10) * D + lane * 8);
          w2t[i - 10] = __builtin_nontemporal_load((const unsigned*)(w2b + (i - 10) * D + 512 + lane * 2));
        }
        if (i >= TDEPF && i < 17) vmwait<TDEPF>();
        SB();
      }
      SB();
      ST_(5);
      u32x4 ga; u32x2 sbA, sbB, cbA, cbB; unsigned gbw;
      NOUNROLL for (unsigned s = 0; s < SPIN_G; ++s) {
        MEMBAR();
        ga = ld_sc1(rs, WS_M2 + tq * 16);
        gbw = __builtin_amdgcn_raw_buffer_load_b32(rs, WS_M2 + 7680 + oc * 4, 0, 16);
        sbA = ld_sc1_64(rs, WS_M2 + 2560 + (row0 + 2 * c4) * 4); sbB = ld_sc1_64(rs, WS_M2 + 2560 + (row0 + 8) * 4);
        cbA = ld_sc1_64(rs, WS_M2 + 5120 + (row0 + 2 * c4) * 4); cbB = ld_sc1_64(rs, WS_M2 + 5120 + (row0 + 8) * 4);
        const unsigned mm = max(max(mx4(ga), gbw), max(max(max(sbA[0], sbA[1]), max(sbB[0], sbB[1])), max(max(cbA[0], cbA[1]), max(cbB[0], cbB[1]))));
        if (__all(mm != SENT)) break;
        __builtin_amdgcn_s_sleep(2);
      }
      float c1[4] = {rbf(1.f + bf(cbA[0])), rbf(1.f + bf(cbA[1])), rbf(1.f + bf(cbB[0])), rbf(1.f + bf(cbB[1]))};
      float xv[4] = {bflo(xq[0]), bfhi(xq[0]), bflo(xq[1]), bfhi(xq[1])};
      asm volatile("" : "+v"(c1[0]), "+v"(c1[1]), "+v"(c1[2]), "+v"(c1[3]), "+v"(xv[0]), "+v"(xv[1]), "+v"(xv[2]), "+v"(xv[3]));
      const float s1[4] = {bf(sbA[0]), bf(sbA[1]), bf(sbB[0]), bf(sbB[1])};
#pragma unroll
      for (int m = 0; m < 9; ++m) { prepack(wt[2 * m], wt[2 * m + 1]); pin(wt[2 * m]); pin(wt[2 * m + 1]); }
#pragma unroll
      for (int m = 0; m < 4; ++m) {
        const unsigned lo = __builtin_amdgcn_perm(w2t[2 * m + 1], w2t[2 * m], 0x05040100u), hi = __builtin_amdgcn_perm(w2t[2 * m + 1], w2t[2 * m], 0x07060302u);
        w2t[2 * m] = lo; w2t[2 * m + 1] = hi;
        asm volatile("" : "+v"(w2t[2 * m]), "+v"(w2t[2 * m + 1]));
      }
      float acc[8] = {0.f, 0.f, 0.f, 0.f, 0.f, 0.f, 0.f, 0.f};
      asm volatile("" : "+v"(acc[0]), "+v"(acc[1]), "+v"(acc[2]), "+v"(acc[3]), "+v"(acc[4]), "+v"(acc[5]), "+v"(acc[6]), "+v"(acc[7]));
      if (w < 3) {
        u32x4 a[8];
        NOUNROLL for (unsigned s = 0; s < SPIN_G; ++s) {
          MEMBAR();
#pragma unroll
          for (int i = 0; i < 8; ++i) a[i] = ld_sc1(rs, WS_YP + i * 2560 + tq * 16);
          unsigned mm = mx4(a[0]);
#pragma unroll
          for (int i = 1; i < 8; ++i) mm = max(mm, mx4(a[i]));
          if (__all(mm != SENT)) break;
        }
        ST_(6);
        float x1[4];
#pragma unroll
        for (int k = 0; k < 4; ++k) {
          const float at = ((bf(a[0][k]) + bf(a[1][k])) + (bf(a[2][k]) + bf(a[3][k]))) + ((bf(a[4][k]) + bf(a[5][k])) + (bf(a[6][k]) + bf(a[7][k])));
          x1[k] = xv[k] + rbf(bf(ga[k]) * rbf(at));
        }
        *(LAS u32x2*)(X1 + 2 * tq) = u32x2{pack2(x1[0], x1[1]), pack2(x1[2], x1[3])};
      }
      HBAR();
      unsigned hp5[5];
      {
        const u32x4 ya = *(LAS u32x4*)(X1 + 4 * lane);
        const unsigned yb = X1[256 + lane];
        const unsigned hA = X1[(row0 >> 1) + c4], hB = X1[(row0 >> 1) + 4];
        float sa_ = dot2(yb, 0x3F803F80u, 0.f), sq_ = dot2(yb, yb, 0.f);
#pragma unroll
        for (int k = 0; k < 4; ++k) { sa_ = dot2(ya[k], 0x3F803F80u, sa_); sq_ = dot2(ya[k], ya[k], sq_); }
        float r = tr32(sa_, sq_);
        r = tr16(r, r);
        r = row_sum(r);
        const float mean = bf(__builtin_amdgcn_readlane(fb(r), 0)) * (1.f / 640.f);
        float var = fmaf(-mean, mean, bf(__builtin_amdgcn_readlane(fb(r), 32)) * (1.f / 640.f));
        if (!(var * 1024.f > mean * mean)) {
          float q2 = 0.f;
#pragma unroll
          for (int k = 0; k < 4; ++k) { const float d0 = bflo(ya[k]) - mean, d1 = bfhi(ya[k]) - mean; q2 += d0 * d0 + d1 * d1; }
          const float d0 = bflo(yb) - mean, d1 = bfhi(yb) - mean; q2 += d0 * d0 + d1 * d1;
          var = wave_sum(q2) * (1.f / 640.f);
        }
        const float mr = rbf(mean);
        const float rden = __builtin_amdgcn_rcpf(rbf(__builtin_amdgcn_sqrtf(rbf(rbf(var) + 1e-5f))));
        const float xr2[4] = {bflo(hA), bfhi(hA), bflo(hB), bfhi(hB)};
        float hv[4];
#pragma unroll
        for (int k = 0; k < 4; ++k) hv[k] = rbf(rbf(rbf(xr2[k] - mr) * rden) * c1[k]) + s1[k];
        const unsigned pA = pack2(hv[0], hv[1]);
        hp5[4] = pack2(hv[2], hv[3]);
        hp5[0] = __builtin_amdgcn_mov_dpp((int)pA, 0x00, 0xf, 0xf, true);
        hp5[1] = __builtin_amdgcn_mov_dpp((int)pA, 0x55, 0xf, 0xf, true);
        hp5[2] = __builtin_amdgcn_mov_dpp((int)pA, 0xAA, 0xf, 0xf, true);
        hp5[3] = __builtin_amdgcn_mov_dpp((int)pA, 0xFF, 0xf, 0xf, true);
      }
      ST_(7);
      {
#pragma unroll
        for (int m = 0; m < 5; ++m) mac_packed(acc, wt[2 * m], wt[2 * m + 1], hp5[m]);
        float y = red8_b345(acc, lane);
        y += xor4(y);
        YR[w * 64 + col32] = y;
        float z[8] = {0.f, 0.f, 0.f, 0.f, 0.f, 0.f, 0.f, 0.f};
        float z8 = 0.f, z9 = 0.f;
        asm volatile("" : "+v"(z[0]), "+v"(z[1]), "+v"(z[2]), "+v"(z[3]), "+v"(z[4]), "+v"(z[5]), "+v"(z[6]), "+v"(z[7]), "+v"(z8), "+v"(z9));
        HBAR();
        const LAS float* yp = YR + 8 * w + c8;
        float hd = (yp[0] + yp[64]) + (yp[128] + yp[192]);
        hd = hd < 0.f ? 0.f : hd;
        const unsigned hpk = pack2(hd, dpp<XOR1>(hd));
#pragma unroll
        for (int m = 0; m < 4; ++m) {
          const unsigned hp = __builtin_amdgcn_readlane(hpk, 2 * m);
          mac_packed(z, wt[10 + 2 * m], wt[10 + 2 * m + 1], hp);
          z8 = dot2(w2t[2 * m], hp, z8);
          z9 = dot2(w2t[2 * m + 1], hp, z9);
        }
        LAS float* ZR = (LAS float*)(L + L_ZR);
        *(LAS f32x4*)(ZR + w * 640 + 8 * lane) = f32x4{z[0], z[1], z[2], z[3]};
        *(LAS f32x4*)(ZR + w * 640 + 8 * lane + 4) = f32x4{z[4], z[5], z[6], z[7]};
        *(LAS f32x2*)(ZR + w * 640 + 512 + 2 * lane) = f32x2{z8, z9};
        HBAR();
        if (t < 160) st_sc1(rs, WS_ZP + f * 2560 + t * 16, nosent4(sum4(ZR + 4 * t, 640)));
      }
      ST_(8);
      if (fin) {
        u32x4 zp[7];
        const int zo = WS_ZP + r8 * 2560 + f * 128 + c8 * 16;
        const int z6 = r8 < NF - 48 ? zo + 48 * 2560 : 0x7FFFFF00;
        const float xo = bflo((unsigned)((LAS u16*)X1)[oc]);
        NOUNROLL for (unsigned s = 0; s < SPIN_G; ++s) {
          MEMBAR();
#pragma unroll
          for (int i = 0; i < 6; ++i) zp[i] = ld_sc1(rs, zo + i * 8 * 2560);
          zp[6] = ld_sc1(rs, z6);
          unsigned mm = mx4(zp[0]);
#pragma unroll
          for (int i = 1; i < 7; ++i) mm = max(mm, mx4(zp[i]));
          if (__all(mm != SENT)) break;
        }
        ST_(9);
        float r[4];
#pragma unroll
        for (int k = 0; k < 4; ++k) r[k] = ((bf(zp[0][k]) + bf(zp[1][k])) + (bf(zp[2][k]) + bf(zp[3][k]))) + ((bf(zp[4][k]) + bf(zp[5][k])) + bf(zp[6][k]));
        float v = tr32(tr16(r[0], r[2]), tr16(r[1], r[3]));
        v += dpp<XOR8>(v);
        out[oc] = (u16)f2bf(xo + rbf(bf(gbw) * rbf(v)));
        if (f == 0 && lane == 0) {
          const u64 e1 = (((u64)Ev[1] << 32) | (u64)Ev[0]) + 1ull;
          st_sc1_64(rs, WS_EPOCH, u32x2{(unsigned)e1, (unsigned)(e1 >> 32)});
        }
        ST_(10);
      }
    }
  }
}

extern "C" void kernel_launch(void* const* d_in, const int* in_sizes, int n_in, void* d_out, int out_size, void* d_ws,
                              size_t ws_size, hipStream_t stream) {
  (void)in_sizes; (void)n_in; (void)out_size; (void)ws_size;
  (void)hipFuncSetAttribute((const void*)mega, hipFuncAttributeMaxDynamicSharedMemorySize, LDS_BYTES);
  hipLaunchKernelGGL(mega, dim3(256), dim3(768), LDS_BYTES, stream, (const u16*)d_in[0], (const u16*)d_in[1], (const u16*)d_in[2],
                     (const u16*)d_in[3], (const u16*)d_in[4], (const u16*)d_in[6], (const u16*)d_in[7], (const u16*)d_in[8],
                     (const u16*)d_in[9], (u16*)d_out, (unsigned char*)d_ws);
}
#define STAMP_ROLE 1
#define STAMP_CLS(r) ((r) < N_M2 ? 3 : (r) < N_M2 + N_WO ? 2 : (r) < N_M2 + N_WO + NF ? 4 : 0)
#define NWAVES 12
#define NSTREAM 8
```

```cpp
#include <hip/hip_runtime.h>
#include <hip/hip_bf16.h>
#include <stdint.h>

#define STAMP 0
#define PSLEEP 16
#define I1 12
#define I2 21
#define I3 17
#define IT 31
#define IS 31
#define ITK 22
#define PREB 1
#define IB 25
#define TA_M2 0
#define TA_WO 2
#define TA_FFN 2
#define TDEPF 40
#define TSTART(m) do { if ((m) == 2) lds_wait(L, C_PV, 9u); else if ((m) == 1) lds_wait8(L, IT); } while (0)
#define MP10 1
#define T1 380
#define T3 500
#define T2 680
#define TM2 950
#define DEPA 1
#define ISW 8
#define WQE 1
#define FAUX 16
#define MLAUX 16
#define CAUX 16
#define CPF 16
#define CPO 5120
#define R_WO 40
#define R_M2 0
#define R_FFN 120
#define MAP 0
#define CAL 0
#define WOE 8
#define FFE 0
#define DEP 2
#define TDEP 40

#define GAS __attribute__((address_space(1)))
#define LAS __attribute__((address_space(3)))
#define RLX_AGENT __ATOMIC_RELAXED, __HIP_MEMORY_SCOPE_AGENT
typedef unsigned short u16;
typedef unsigned long long u64;
typedef unsigned u32x4 __attribute__((ext_vector_type(4)));
typedef unsigned u32x2 __attribute__((ext_vector_type(2)));
typedef float f32x4 __attribute__((ext_vector_type(4)));
typedef float f32x2 __attribute__((ext_vector_type(2)));
typedef __bf16 bf16x2 __attribute__((ext_vector_type(2)));
typedef GAS u64 gu64;
typedef __amdgpu_buffer_rsrc_t rsrc_t;

constexpr int D = 640, HQ = 32, SK = 16384, DH = 128, WIN = 4096, DFF = 1728;
constexpr int NF = 54;

constexpr int WS_EPOCH = 0;
constexpr int WS_FLAGS = 4096;
constexpr int WS_TICKET = 2048;
constexpr int NMP = 4;
constexpr int WS_MP = 8192;
constexpr int WS_QP = 131072;
constexpr int WS_OP = WS_QP + 131072;
constexpr int WS_ST = WS_OP + 131072;
constexpr int WS_YP = WS_ST + 32768;
constexpr int WS_M2 = WS_YP + 20480;
constexpr int WS_ZP = WS_M2 + 10240;
constexpr int WS_END = WS_ZP + NF * 2560;
constexpr int N_M2 = 40, N_WO = 80;
constexpr int WS_STAMP = 2 << 20;

constexpr unsigned SENT = 0xFFFFFFFFu;
constexpr unsigned SPIN_G = 1u << 18;
constexpr unsigned SPIN_L = 1u << 22;

constexpr int C_PROG = 0;
constexpr int C_MOD1 = 8, C_QP = 9, C_QREADY = 10, C_M = 11, C_ROLE = 12, C_PV = 13, C_FLAGOK = 14, C_T1 = 15;
constexpr int L_P = 128;
constexpr int L_MR = L_P + 1024;
constexpr int L_QR = L_MR + 1024;
constexpr int L_OR = L_QR + 2048;
constexpr int L_OB = L_OR + 4096;
constexpr int L_YR = L_OB + 1024;
constexpr int L_STAT = L_YR + 1024;
constexpr int L_Q = L_STAT + 64;
constexpr int L_X1 = L_Q + 256;
constexpr int L_ZR = L_X1 + 2560;
constexpr int L_END = L_ZR + 10240;
constexpr int LDS_BYTES = 96 * 1024;
static_assert(L_END <= LDS_BYTES, "lds");

__device__ __forceinline__ float bflo(unsigned w) { return __builtin_bit_cast(float, w << 16); }
__device__ __forceinline__ float bfhi(unsigned w) { return __builtin_bit_cast(float, w & 0xffff0000u); }
__device__ __forceinline__ unsigned f2bf(float f) { return (unsigned)__builtin_bit_cast(u16, (__bf16)f); }
__device__ __forceinline__ float rbf(float f) { return bflo(f2bf(f)); }
__device__ __forceinline__ unsigned pack2(float lo, float hi) {
  bf16x2 v = {(__bf16)lo, (__bf16)hi};
  return __builtin_bit_cast(unsigned, v);
}
__device__ __forceinline__ float dot2(unsigned a, unsigned b, float acc) {
  return __builtin_amdgcn_fdot2_f32_bf16(__builtin_bit_cast(bf16x2, a), __builtin_bit_cast(bf16x2, b), acc, false);
}
__device__ __forceinline__ unsigned fb(float f) { return __builtin_bit_cast(unsigned, f); }
__device__ __forceinline__ float bf(unsigned u) { return __builtin_bit_cast(float, u); }
__device__ __forceinline__ unsigned nosent(float f) { return min(fb(f), 0xFFFFFFFEu); }
__device__ __forceinline__ u32x4 nosent4(f32x4 s) { return u32x4{nosent(s[0]), nosent(s[1]), nosent(s[2]), nosent(s[3])}; }
template <int CTRL> __device__ __forceinline__ float dpp(float x) {
  return __builtin_bit_cast(float, __builtin_amdgcn_mov_dpp(__builtin_bit_cast(int, x), CTRL, 0xf, 0xf, true));
}
constexpr int XOR1 = 0xB1, XOR2 = 0x4E, XOR7 = 0x141, XOR8 = 0x128;
__device__ __forceinline__ float xor4(float x) {
  int v = __builtin_bit_cast(int, x);
  int r = __builtin_amdgcn_update_dpp(0, v, 0x114, 0xf, 0xa, false);
  r = __builtin_amdgcn_update_dpp(r, v, 0x104, 0xf, 0x5, false);
  return __builtin_bit_cast(float, r);
}
__device__ __forceinline__ float tr16(float a, float b) {
  auto s = __builtin_amdgcn_permlane16_swap(fb(a), fb(b), false, false);
  return bf(s[0]) + bf(s[1]);
}
__device__ __forceinline__ float tr32(float a, float b) {
  auto s = __builtin_amdgcn_permlane32_swap(fb(a), fb(b), false, false);
  return bf(s[0]) + bf(s[1]);
}
__device__ __forceinline__ float mx16(float a) {
  auto s = __builtin_amdgcn_permlane16_swap(fb(a), fb(a), false, false);
  return fmaxf(bf(s[0]), bf(s[1]));
}
__device__ __forceinline__ float mx32(float a) {
  auto s = __builtin_amdgcn_permlane32_swap(fb(a), fb(a), false, false);
  return fmaxf(bf(s[0]), bf(s[1]));
}
__device__ __forceinline__ float row_sum(float x) {
  x += dpp<XOR1>(x); x += dpp<XOR2>(x); x += dpp<XOR7>(x); x += dpp<XOR8>(x); return x;
}
__device__ __forceinline__ float row_max(float x) {
  x = fmaxf(x, dpp<XOR1>(x)); x = fmaxf(x, dpp<XOR2>(x)); x = fmaxf(x, dpp<XOR7>(x)); x = fmaxf(x, dpp<XOR8>(x)); return x;
}
__device__ __forceinline__ float wave_sum(float x) { x = row_sum(x); x = tr16(x, x); return tr32(x, x); }
__device__ __forceinline__ float wave_max(float x) { x = row_max(x); x = mx16(x); return mx32(x); }

__device__ __forceinline__ unsigned bij32(unsigned x) {
  x += 0x9E3779B9u; x ^= x >> 16; x *= 0x85EBCA6Bu; x ^= x >> 13; x *= 0xC2B2AE35u; x ^= x >> 16; return x;
}
__device__ __forceinline__ rsrc_t mkrs(const void* p, int bytes) { return __builtin_amdgcn_make_buffer_rsrc((void*)p, 0, bytes, 0x00020000); }
__device__ __forceinline__ u32x4 ldnt(const void* p) { return __builtin_nontemporal_load(reinterpret_cast<const u32x4*>(p)); }
__device__ __forceinline__ u32x4 ld_sc1(rsrc_t rs, int off) { return __builtin_amdgcn_raw_buffer_load_b128(rs, off, 0, 16); }
__device__ __forceinline__ u32x2 ld_sc1_64(rsrc_t rs, int off) { return __builtin_amdgcn_raw_buffer_load_b64(rs, off, 0, 16); }
__device__ __forceinline__ void st_sc1(rsrc_t rs, int off, u32x4 v) { __builtin_amdgcn_raw_buffer_store_b128(v, rs, off, 0, 16); }
__device__ __forceinline__ void st_sc1_64(rsrc_t rs, int off, u32x2 v) { __builtin_amdgcn_raw_buffer_store_b64(v, rs, off, 0, 16); }
__device__ __forceinline__ u32x4 ld_f(rsrc_t rs, int off) { return __builtin_amdgcn_raw_buffer_load_b128(rs, off, 0, FAUX); }
__device__ __forceinline__ u32x2 ld_f64(rsrc_t rs, int off) { return __builtin_amdgcn_raw_buffer_load_b64(rs, off, 0, FAUX); }
__device__ __forceinline__ void st_f(rsrc_t rs, int off, u32x4 v) { __builtin_amdgcn_raw_buffer_store_b128(v, rs, off, 0, FAUX); }
__device__ __forceinline__ void st_f64(rsrc_t rs, int off, u32x2 v) { __builtin_amdgcn_raw_buffer_store_b64(v, rs, off, 0, FAUX); }
__device__ __forceinline__ void st_c(rsrc_t rs, int off, u32x4 v) { __builtin_amdgcn_raw_buffer_store_b128(v, rs, off, 0, CAUX); }
__device__ __forceinline__ unsigned mx4(u32x4 v) { return max(max(v[0], v[1]), max(v[2], v[3])); }
#define MEMBAR() asm volatile("" ::: "memory")
#define LGKM0() asm volatile("s_waitcnt lgkmcnt(0)" ::: "memory")
#define SB() __builtin_amdgcn_sched_barrier(0)
#define NOUNROLL _Pragma("nounroll")

__device__ __forceinline__ void lds_put(LAS unsigned char* L, int word, unsigned v) { ((volatile LAS unsigned*)L)[word] = v; }
__device__ __forceinline__ void lds_wait(LAS unsigned char* L, int word, unsigned v) {
  NOUNROLL for (unsigned s = 0; s < SPIN_L; ++s) {
    if (((volatile LAS unsigned*)L)[word] >= v) break;
    __builtin_amdgcn_s_sleep(1);
  }
  MEMBAR();
}
__device__ __forceinline__ void lds_wait4(LAS unsigned char* L, int word, unsigned v) {
  NOUNROLL for (unsigned s = 0; s < SPIN_L; ++s) {
    const u32x4 q = *(volatile LAS u32x4*)(L + word * 4);
    if (min(min(q[0], q[1]), min(q[2], q[3])) >= v) break;
    __builtin_amdgcn_s_sleep(1);
  }
  MEMBAR();
}
__device__ __forceinline__ void lds_spin(LAS unsigned char* L, int word, unsigned v) {
  NOUNROLL for (unsigned s = 0; s < SPIN_L; ++s) {
    if (((volatile LAS unsigned*)L)[word] >= v) break;
  }
  MEMBAR();
}
__device__ __forceinline__ unsigned lds_arrive(int word) {
  unsigned old;
  asm volatile("s_mov_b64 exec, 1\n\tds_add_rtn_u32 %0, %1, %2\n\ts_mov_b64 exec, -1\n\ts_waitcnt lgkmcnt(0)"
               : "=&v"(old) : "v"(word * 4), "v"(1u) : "memory");
  return __builtin_amdgcn_readfirstlane(old);
}
__device__ __forceinline__ void lds_arrive_nr(int word) {
  asm volatile("s_mov_b64 exec, 1\n\tds_add_u32 %0, %1\n\ts_mov_b64 exec, -1" :: "v"(word * 4), "v"(1u) : "memory");
}
__device__ __forceinline__ void lds_wait8(LAS unsigned char* L, unsigned v) {
  NOUNROLL for (unsigned s = 0; s < SPIN_L; ++s) {
    const u32x4 q = *(volatile LAS u32x4*)L, r = *(volatile LAS u32x4*)(L + 16);
    if (min(min(min(q[0], q[1]), min(q[2], q[3])), min(min(r[0], r[1]), min(r[2], r[3]))) >= v) break;
    __builtin_amdgcn_s_sleep(2);
  }
  MEMBAR();
}
#define HBAR() do { LGKM0(); __builtin_amdgcn_s_barrier(); MEMBAR(); } while (0)
__device__ __forceinline__ void wait_until(u64 t) {
  NOUNROLL for (unsigned s = 0; s < 4096u; ++s) {
    if (__builtin_amdgcn_s_memrealtime() >= t) break;
    __builtin_amdgcn_s_sleep(2);
  }
  MEMBAR();
}
template <int N> __device__ __forceinline__ void vmwait() { asm volatile("s_waitcnt vmcnt(%0)" :: "i"(N) : "memory"); }

#define ST_(k) do {} while (0)
#define CNT_(sh) do {} while (0)

__device__ __forceinline__ float red8_b345(const float (&a)[8], int lane) {
  float v[4];
  const bool hi = (lane & 8) != 0;
#pragma unroll
  for (int k = 0; k < 4; ++k) {
    const float t1 = a[k] + dpp<XOR8>(a[k]), t2 = a[k + 4] + dpp<XOR8>(a[k + 4]);
    v[k] = hi ? t2 : t1;
  }
  const float u0 = tr16(v[0], v[2]), u1 = tr16(v[1], v[3]);
  return tr32(u0, u1);
}
__device__ __forceinline__ void prepack(u32x4& wa, u32x4& wb) {
#pragma unroll
  for (int d = 0; d < 4; ++d) {
    const unsigned lo = __builtin_amdgcn_perm(wb[d], wa[d], 0x05040100u);
    const unsigned hi = __builtin_amdgcn_perm(wb[d], wa[d], 0x07060302u);
    wa[d] = lo; wb[d] = hi;
  }
}
__device__ __forceinline__ void pin(u32x4& a) { asm volatile("" : "+v"(a)); }
__device__ __forceinline__ void mac_packed(float (&acc)[8], u32x4 wa, u32x4 wb, unsigned xp) {
#pragma unroll
  for (int d = 0; d < 4; ++d) { acc[2 * d] = dot2(wa[d], xp, acc[2 * d]); acc[2 * d + 1] = dot2(wb[d], xp, acc[2 * d + 1]); }
}
__device__ __forceinline__ void mac_pair(float (&acc)[8], u32x4 wa, u32x4 wb, unsigned xp) { prepack(wa, wb); mac_packed(acc, wa, wb, xp); }
__device__ __forceinline__ float silu_f(float v) { return v * __builtin_amdgcn_rcpf(1.f + __expf(-v)); }
__device__ __forceinline__ f32x4 sum4(const LAS float* p, int stride) {
  return (*(const LAS f32x4*)p + *(const LAS f32x4*)(p + stride)) + (*(const LAS f32x4*)(p + 2 * stride) + *(const LAS f32x4*)(p + 3 * stride));
}

#define CODE_PREFETCH(NAME) do { if (CPF && w == 3) { \
      u64 pc_; int len_; \
      asm volatile("s_getpc_b64 %0\n.L" #NAME "_beg:\n\ts_mov_b32 %1, .L" #NAME "_end-.L" #NAME "_beg" : "=s"(pc_), "=s"(len_)); \
      len_ = min(max(len_, 0), 16384); \
      const rsrc_t rc_ = mkrs((const void*)pc_, len_); \
      const unsigned c_ = __builtin_amdgcn_raw_buffer_load_b32(rc_, max(len_ - 4096, 0) + lane * 64, 0, 0); \
      const unsigned d_ = __builtin_amdgcn_raw_buffer_load_b32(rc_, lane * 64, 0, 0); \
      asm volatile("" :: "v"(c_), "v"(d_)); } } while (0)
#define CODE_ALIGN(NAME, P) do { if (CAL) asm volatile("s_branch .L" #NAME "_al\n\t.p2align " #P "\n.L" #NAME "_al:"); } while (0)
#define CODE_END(NAME) asm volatile(".L" #NAME "_end:")

__global__ void __launch_bounds__(768, 3)
mega(const u16* __restrict__ x, const u16* __restrict__ kc, const u16* __restrict__ vc, const u16* __restrict__ wq,
     const u16* __restrict__ wo, const u16* __restrict__ w1, const u16* __restrict__ w2, const u16* __restrict__ cnd,
     const u16* __restrict__ wmod, u16* __restrict__ out, unsigned char* ws) {
  extern __shared__ __attribute__((aligned(16))) unsigned char smem[];
  LAS unsigned char* L = (LAS unsigned char*)smem;
  const int tid = threadIdx.x, lane = tid & 63, wave = __builtin_amdgcn_readfirstlane(tid >> 6);
  const int b = blockIdx.x;
  const int h = MAP ? b >> 3 : (b & 7) * 4 + (b >> 6), j = MAP ? b & 7 : (b >> 3) & 7, u = h * 8 + j;
  const int shard = (b >> 3) & 7;
  const int r4 = lane >> 4, c16 = lane & 15;
  const rsrc_t rs = mkrs(ws, WS_END);
  ST_(0);

  if (wave < 8) {
    const size_t kvoff = ((size_t)h * SK + (SK - WIN) + j * 512) * DH;
    const rsrc_t rk = mkrs(kc + kvoff, 131072), rv = mkrs(vc + kvoff, 131072);
    const int rot = (b * 8 + (b >> 4) + wave) * 1024;
    u32x4 big[32];
    u32x4 pp0, pp1;
    float mw;
    LAS float* STAT = (LAS float*)(L + L_STAT);
#define LOAD_STEP(i) do { \
      big[i] = __builtin_amdgcn_raw_buffer_load_b128((i) < 16 ? rk : rv, lane * 16, (((i) & 15) * 8192 + rot) & 131071, 2); \
      if ((i) == PREB - 1) __builtin_amdgcn_s_barrier();     \
      if ((i) == ITK || (i) == IT) lds_put(L, C_PROG + wave, (unsigned)(i)); \
      if ((i) >= 16 + DEP + 2 && (((i) - DEP) & 1) == 0 && (i) <= IB) { prepack(big[(i) - DEP - 2], big[(i) - DEP - 1]); pin(big[(i) - DEP - 2]); pin(big[(i) - DEP - 1]); } \
      if ((i) < ISW) { if ((i) >= DEPA) vmwait<DEPA>(); } else if ((i) < IB && (i) != IS) vmwait<DEP>(); \
      SB(); } while (0)
#pragma unroll
    for (int i = 0; i <= IS; ++i) LOAD_STEP(i);
    {
      ST_(2); lds_wait(L, C_QREADY, 1u); ST_(3);
      const u32x4 qp = *(LAS u32x4*)(L + L_Q + c16 * 16);
      float xs[16];
#pragma unroll
      for (int ii = 0; ii < 16; ++ii) {
        float d = 0.f;
#pragma unroll
        for (int k = 0; k < 4; ++k) d = dot2(big[ii][k], qp[k], d);
        xs[ii] = d;
      }
#define TR_STEP(HALF, CTRL, BIT) _Pragma("unroll") for (int q = 0; q < (HALF); ++q) { \
      const float lo_ = xs[q], hi_ = xs[q + (HALF)]; const float t1_ = lo_ + dpp<CTRL>(lo_), t2_ = hi_ + dpp<CTRL>(hi_); \
      xs[q] = (c16 & (BIT)) ? t2_ : t1_; }
      TR_STEP(8, XOR8, 8) TR_STEP(4, XOR7, 4) TR_STEP(2, XOR2, 2) TR_STEP(1, XOR1, 1)
#undef TR_STEP
      const float sc = rbf(rbf(xs[0]) * 0.08838834764831845f);
      mw = wave_max(sc);
      const float e = __expf(sc - mw);
      LAS u16* P = (LAS u16*)(L + L_P) + wave * 64;
      P[lane] = (u16)f2bf(e);
      MEMBAR();
      pp0 = *(volatile LAS u32x4*)(P + r4 * 16); pp1 = *(volatile LAS u32x4*)(P + r4 * 16 + 8);
      STAT[wave] = mw;
      STAT[8 + wave] = wave_sum(e);
      if (lds_arrive(C_M) == 7u) {
        const float m8 = STAT[lane & 7], l8 = STAT[8 + (lane & 7)];
        float mc = fmaxf(m8, dpp<XOR1>(m8)); mc = fmaxf(mc, dpp<XOR2>(mc)); mc = fmaxf(mc, dpp<XOR7>(mc));
        float lc = l8 * __expf(m8 - mc);
        lc += dpp<XOR1>(lc); lc += dpp<XOR2>(lc); lc += dpp<XOR7>(lc);
        if (lane == 0) __builtin_amdgcn_raw_buffer_store_b64(u32x2{nosent(mc), nosent(lc)}, rs, WS_ST + u * 128, 0, MLAUX);
      }
          ST_(4);
      }
#pragma unroll
    for (int i = IS + 1; i < 32; ++i) LOAD_STEP(i);
    ST_(1);
    __builtin_amdgcn_s_setprio(2);
    {
      float acc[8] = {0.f, 0.f, 0.f, 0.f, 0.f, 0.f, 0.f, 0.f};
      constexpr int NPRE = ((IB < 30 ? IB : 30) - 16 - DEP) / 2;
      float scl;
#pragma unroll
      for (int m = 0; m < 8; ++m) {
        if (m == NPRE) {
          lds_spin(L, C_M, 8u);
          const f32x4 ma = *(LAS f32x4*)STAT, mb = *(LAS f32x4*)(STAT + 4);
          const float mc = fmaxf(fmaxf(fmaxf(ma[0], ma[1]), fmaxf(ma[2], ma[3])), fmaxf(fmaxf(mb[0], mb[1]), fmaxf(mb[2], mb[3])));
          scl = __expf(mw - mc);
          asm volatile("" : "+v"(scl));
        }
        if (m >= NPRE) prepack(big[16 + 2 * m], big[16 + 2 * m + 1]);
        mac_packed(acc, big[16 + 2 * m], big[16 + 2 * m + 1], m < 4 ? pp0[m & 3] : pp1[m & 3]);
        SB();
      }
      ST_(5);
      float v[4];
#pragma unroll
      for (int k = 0; k < 4; ++k) v[k] = tr16(acc[k], acc[k + 4]);
      const float y0 = tr32(v[0], v[2]) * scl, y1 = tr32(v[1], v[3]) * scl;
      LAS float* OR = (LAS float*)(L + L_OR);
      *(LAS f32x2*)(OR + wave * 128 + c16 * 8 + (r4 & 1) * 4 + (r4 >> 1) * 2) = f32x2{y0, y1};
      if (lds_arrive(C_PV) == 7u) {
        const f32x4 s = sum4(OR + (lane >> 5) * 512 + 4 * (lane & 31), 128);
        f32x4 t;
#pragma unroll
        for (int k = 0; k < 4; ++k) t[k] = tr32(s[k], s[k]);
        if (lane < 32) st_sc1(rs, WS_OP + u * 512 + lane * 16, nosent4(t));
        MEMBAR();
        lds_put(L, C_PV, 9u);
      }
      ST_(6);
    }
  } else {
    const int w = wave - 8;
    int zero_v;
    asm volatile("v_mov_b32 %0, 0" : "=v"(zero_v));
    const int r8 = lane >> 3, c8 = lane & 7;
    __builtin_amdgcn_s_setprio(3);
    const u64 tstart = __builtin_amdgcn_s_memrealtime();
    const bool m1 = b < 80;
    const int cg = b % 20, rg = b / 20;
    u32x4 wm[5]; unsigned cw0, cw1, cw2;
#define MOD_TILE_LOAD() do {  \
      const int row0 = 160 * rg + 40 * w; \
      const u16* wb = wmod + (size_t)row0 * (6 * D) + cg * 64 + c8 * 8; \
      wm[0] = ldnt(wb + (size_t)(2 * r8) * (6 * D)); wm[1] = ldnt(wb + (size_t)(2 * r8 + 1) * (6 * D)); \
      wm[2] = ldnt(wb + (size_t)(16 + 2 * r8) * (6 * D)); wm[3] = ldnt(wb + (size_t)(17 + 2 * r8) * (6 * D)); \
      wm[4] = ldnt(wb + (size_t)(32 + r8) * (6 * D)); \
      cw0 = __builtin_nontemporal_load((const unsigned*)(cnd + row0 + 2 * r8)); \
      cw1 = __builtin_nontemporal_load((const unsigned*)(cnd + row0 + 16 + 2 * r8)); \
      cw2 = __builtin_nontemporal_load(cnd + row0 + 32 + r8); \
     } while (0)
    if (m1) MOD_TILE_LOAD();
    const u32x2 Ev = ld_sc1_64(rs, WS_EPOCH + zero_v);
    const int l20 = (lane & 31) < 20 ? (lane & 31) : 19;
    const int hrow = 80 * j + 20 * w + l20;
    const u32x4 xa = ldnt(x + lane * 8);
    const unsigned xb = __builtin_nontemporal_load((const unsigned*)x + 256 + lane);
    const float xr = bflo((unsigned)__builtin_nontemporal_load(x + hrow));
    if (w == 3) {
      if (lane < 32) lds_put(L, lane, 0u);
    }
    SB();
    LGKM0();
    __builtin_amdgcn_s_barrier();
    SB();
    u32x4 wqv[5];
    if (WQE) {
#pragma unroll
      for (int i = 0; i < 5; ++i) wqv[i] = ldnt(wq + (size_t)(80 * j + 20 * w + 5 * r4 + i) * (HQ * DH) + h * DH + c16 * 8);
      SB();
    }
    const unsigned tag = bij32(Ev[0]), flo = bij32(Ev[0] ^ Ev[1] ^ 0x5bd1e995u);
#define MOD_TILE_COMPUTE() do {  \
      float acc[8] = {0.f, 0.f, 0.f, 0.f, 0.f, 0.f, 0.f, 0.f}; \
      mac_pair(acc, wm[0], wm[1], pack2(silu_f(bflo(cw0)), silu_f(bfhi(cw0)))); \
      mac_pair(acc, wm[2], wm[3], pack2(silu_f(bflo(cw1)), silu_f(bfhi(cw1)))); \
      { \
        const unsigned sl = f2bf(silu_f(bflo(cw2))), sh = sl << 16; \
_Pragma("unroll")         for (int d = 0; d < 4; ++d) { acc[2 * d] = dot2(wm[4][d], sl, acc[2 * d]); acc[2 * d + 1] = dot2(wm[4][d], sh, acc[2 * d + 1]); } \
      } \
      const float y = red8_b345(acc, lane); \
      const int col = c8 * 8 + ((lane >> 3) & 1) * 4 + ((lane >> 4) & 1) * 2 + (lane >> 5); \
      LAS float* MR = (LAS float*)(L + L_MR); \
      MR[w * 64 + col] = y; \
      if (lds_arrive(C_MOD1) == 3u) { \
        const float s = (MR[lane] + MR[64 + lane]) + (MR[128 + lane] + MR[192 + lane]); \
        st_f64(rs, WS_MP + (rg * 1280 + cg * 64 + lane) * 8, u32x2{fb(s), tag}); \
      } \
     } while (0)
    if (m1) MOD_TILE_COMPUTE();
    ST_(1);
    if (w == 3) {
      u32x4 sv = {SENT, SENT, SENT, SENT};
      st_c(rs, (lane < 32 ? WS_QP : WS_OP) + u * 512 + (lane & 31) * 16, sv);
      if (b >= 128 && b < 128 + NF) {
        st_c(rs, WS_ZP + (b - 128) * 2560 + lane * 16, sv);
        st_c(rs, WS_ZP + (b - 128) * 2560 + 1024 + lane * 16, sv);
      }
      int o2 = 0x7FFFFF00;
      if (lane == 63) o2 = WS_ST + u * 128;
      else if (b < 120) { if (lane < 16) o2 = WS_YP + b * 256 + lane * 16; }
      else if (b >= 128 && b < 128 + NF) { if (lane < 32) o2 = WS_ZP + (b - 128) * 2560 + 2048 + lane * 16; }
      else if (b == 255 && lane < 8) { o2 = WS_TICKET + lane * 128; sv = u32x4{0u, 0u, 0u, 0u}; }
      st_c(rs, o2, sv);
          vmwait<0>();
      if (lane == 0) st_f64(rs, WS_FLAGS + b * 8, u32x2{flo, tag});
    }
    {
      if (!WQE) {
#pragma unroll
        for (int i = 0; i < 5; ++i) wqv[i] = ldnt(wq + (size_t)(80 * j + 20 * w + 5 * r4 + i) * (HQ * DH) + h * DH + c16 * 8);
      }
      SB();
      float nrm;
      {
        float s = bflo(xb) + bfhi(xb);
#pragma unroll
        for (int i = 0; i < 4; ++i) s += bflo(xa[i]) + bfhi(xa[i]);
        const float mean = wave_sum(s) * (1.f / 640.f);
        float q2 = (bflo(xb) - mean) * (bflo(xb) - mean) + (bfhi(xb) - mean) * (bfhi(xb) - mean);
#pragma unroll
        for (int i = 0; i < 4; ++i) { const float d0 = bflo(xa[i]) - mean, d1 = bfhi(xa[i]) - mean; q2 += d0 * d0 + d1 * d1; }
        const float var = wave_sum(q2) * (1.f / 640.f);
        const float den = rbf(sqrtf(rbf(rbf(var) + 1e-5f)));
        nrm = rbf(rbf(xr - rbf(mean)) / den);
      }
      const int mp = WS_MP + ((lane >> 5) * 640 + hrow) * 8;
      u32x2 g[NMP];
      wait_until(tstart + T1);
      NOUNROLL for (unsigned s = 0; s < SPIN_G; ++s) {
        bool ok = true;
        MEMBAR();
#pragma unroll
        for (int i = 0; i < NMP; ++i) g[i] = ld_f64(rs, mp + i * 1280 * 8);
#pragma unroll
        for (int i = 0; i < NMP; ++i) ok = ok & (g[i][1] == tag);
        CNT_(0);
        if (__all(ok)) break;
        __builtin_amdgcn_s_sleep(PSLEEP);
      }
      const float mv = rbf((bf(g[0][0]) + bf(g[1][0])) + (bf(g[2][0]) + bf(g[3][0])));
      auto sw = __builtin_amdgcn_permlane32_swap(fb(mv), fb(mv), false, false);
      const float hval = rbf(rbf(nrm * rbf(1.f + bf(sw[1]))) + bf(sw[0]));
      ST_(2);
      float acc[8] = {0.f, 0.f, 0.f, 0.f, 0.f, 0.f, 0.f, 0.f};
#pragma unroll
      for (int i = 0; i < 5; ++i) {
        const float hk = __shfl(hval, 5 * r4 + i);
#pragma unroll
        for (int d = 0; d < 4; ++d) { acc[2 * d] = fmaf(hk, bflo(wqv[i][d]), acc[2 * d]); acc[2 * d + 1] = fmaf(hk, bfhi(wqv[i][d]), acc[2 * d + 1]); }
      }
      float v[4];
#pragma unroll
      for (int k = 0; k < 4; ++k) v[k] = tr16(acc[k], acc[k + 4]);
      const float y0 = tr32(v[0], v[2]), y1 = tr32(v[1], v[3]);
      LAS float* QR = (LAS float*)(L + L_QR);
      *(LAS f32x2*)(QR + w * 128 + c16 * 8 + (r4 & 1) * 4 + (r4 >> 1) * 2) = f32x2{y0, y1};
      if (lds_arrive(C_QP) == 3u) {
        if (lane < 32) st_f(rs, WS_QP + u * 512 + lane * 16, nosent4(sum4(QR + 4 * lane, 128)));
      }
    }
    ST_(3);
    if (w == 1) {
      wait_until(tstart + T3);
      NOUNROLL for (unsigned s = 0; s < SPIN_G; ++s) {
        MEMBAR();
        const u32x4 g0 = ld_f(rs, WS_FLAGS + lane * 16), g1 = ld_f(rs, WS_FLAGS + 1024 + lane * 16);
        const unsigned bad = ((g0[0] ^ flo) | (g0[1] ^ tag)) | ((g0[2] ^ flo) | (g0[3] ^ tag)) | ((g1[0] ^ flo) | (g1[1] ^ tag)) | ((g1[2] ^ flo) | (g1[3] ^ tag));
        if (__all(bad == 0u)) break;
        __builtin_amdgcn_s_sleep(PSLEEP);
      }
      lds_put(L, C_FLAGOK, 1u);
      lds_wait(L, C_PROG, ITK);
      if (lane == 0) {
        const unsigned tk = __hip_atomic_fetch_add((GAS unsigned*)(ws + WS_TICKET + shard * 128), 1u, RLX_AGENT);
        lds_put(L, C_ROLE, tk * 8u + (unsigned)shard + 1u);
      }
    } else if (w == 0) {
      u32x4 a0, a1, b0, b1;
      const int off = WS_QP + (h * 8 + 2 * r4) * 512 + c16 * 32;
      lds_wait(L, C_FLAGOK, 1u);
      wait_until(tstart + T2);
      NOUNROLL for (unsigned s = 0; s < SPIN_G; ++s) {
        MEMBAR();
        a0 = ld_f(rs, off); a1 = ld_f(rs, off + 16); b0 = ld_f(rs, off + 512); b1 = ld_f(rs, off + 528);
        CNT_(16);
        if (__all(max(max(mx4(a0), mx4(a1)), max(mx4(b0), mx4(b1))) != SENT)) break;
        __builtin_amdgcn_s_sleep(PSLEEP);
      }
      float v[4];
#pragma unroll
      for (int k = 0; k < 4; ++k) v[k] = tr16(bf(a0[k]) + bf(b0[k]), bf(a1[k]) + bf(b1[k]));
      const float y0 = tr32(v[0], v[2]), y1 = tr32(v[1], v[3]);
      ((LAS unsigned*)(L + L_Q))[c16 * 4 + (r4 & 1) * 2 + (r4 >> 1)] = pack2(y0, y1);
      MEMBAR();
      lds_put(L, C_QREADY, 1u);
      ST_(4);
    }
    lds_wait(L, C_ROLE, 1u);
    const int role = __builtin_amdgcn_readfirstlane(((volatile LAS unsigned*)L)[C_ROLE]) - 1;
    __builtin_amdgcn_s_setprio(0);
    const int r16 = lane >> 2, c4 = lane & 3;
    const int col32 = c4 * 8 + ((lane >> 3) & 1) * 4 + ((lane >> 4) & 1) * 2 + (lane >> 5);
    const int col64 = c8 * 8 + ((lane >> 3) & 1) * 4 + ((lane >> 4) & 1) * 2 + (lane >> 5);
    LAS float* YR = (LAS float*)(L + L_YR);
    if (role >= R_M2 && role < R_M2 + N_M2) {
      const int m2 = role - R_M2;
      const int row0 = 160 * w + 20 * r8;
      unsigned sp[10];
      u32x2 cw2[5];
      u32x4 wt[20];
      TSTART(TA_M2);
#pragma unroll
      for (int i = 0; i < 5; ++i) cw2[i] = __builtin_nontemporal_load((const u32x2*)(cnd + row0 + 4 * i));
      const u16* wb = wmod + (size_t)row0 * (6 * D) + 1280 + m2 * 64 + c8 * 8;
#pragma unroll
      for (int i = 0; i < 20; ++i) wt[i] = ldnt(wb + (size_t)i * (6 * D));
      SB();
      ST_(5);
#pragma unroll
      for (int i = 0; i < 5; ++i) {
        sp[2 * i] = pack2(silu_f(bflo(cw2[i][0])), silu_f(bfhi(cw2[i][0])));
        sp[2 * i + 1] = pack2(silu_f(bflo(cw2[i][1])), silu_f(bfhi(cw2[i][1])));
      }
      float acc[8] = {0.f, 0.f, 0.f, 0.f, 0.f, 0.f, 0.f, 0.f};
#pragma unroll
      for (int m = 0; m < 10; ++m) mac_pair(acc, wt[2 * m], wt[2 * m + 1], sp[m]);
      const float y = red8_b345(acc, lane);
      ST_(6);
      YR[w * 64 + col64] = y;
      if (lds_arrive(C_T1) == 3u && lane < 16) {
        const f32x4 s = sum4(YR + 4 * lane, 64);
        st_sc1(rs, WS_M2 + (m2 * 64 + 4 * lane) * 4, nosent4(f32x4{rbf(s[0]), rbf(s[1]), rbf(s[2]), rbf(s[3])}));
      }
      ST_(7);
    } else if (role >= R_WO && role < R_WO + N_WO) {
      const int g = (role - R_WO) & 7, cgp = (role - R_WO) >> 3, hw = 4 * g + w;
      u32x4 wt[16];
      CODE_PREFETCH(mega_wo);
      const u16* wb = wo + (size_t)(hw * 128 + 16 * r8) * D + cgp * 64 + c8 * 8;
#pragma unroll
      for (int i = 0; i < WOE; ++i) wt[i] = ldnt(wb + i * D);
      SB();
      TSTART(TA_WO);
#pragma unroll
      for (int i = WOE; i < 16; ++i) wt[i] = ldnt(wb + i * D);
      SB();
      ST_(5);
      u32x4 a[4]; u32x2 ml;
      const int off = WS_OP + (hw * 8 + 4 * (lane >> 5)) * 512 + (lane & 31) * 16;
      const int mo = WS_ST + (hw * 8 + c8) * 128;
      ml = ld_sc1_64(rs, mo);
      SB();
#pragma unroll
      for (int m = 0; m < 8; ++m) { prepack(wt[2 * m], wt[2 * m + 1]); pin(wt[2 * m]); pin(wt[2 * m + 1]); }
      float acc[8] = {0.f, 0.f, 0.f, 0.f, 0.f, 0.f, 0.f, 0.f};
      asm volatile("" : "+v"(acc[0]), "+v"(acc[1]), "+v"(acc[2]), "+v"(acc[3]), "+v"(acc[4]), "+v"(acc[5]), "+v"(acc[6]), "+v"(acc[7]));
      float wj[4];
      CODE_ALIGN(mega_wo, 11);
      NOUNROLL for (unsigned s = 0; s < SPIN_G; ++s) {
        MEMBAR();
#pragma unroll
        for (int i = 0; i < 4; ++i) a[i] = ld_sc1(rs, off + i * 512);
        const bool mok = __all(max(ml[0], ml[1]) != SENT);
        {
          float M = bf(ml[0]);
          M = fmaxf(M, dpp<XOR1>(M)); M = fmaxf(M, dpp<XOR2>(M)); M = fmaxf(M, dpp<XOR7>(M));
          const float e = __expf(bf(ml[0]) - M);
          float Ls = bf(ml[1]) * e;
          Ls += dpp<XOR1>(Ls); Ls += dpp<XOR2>(Ls); Ls += dpp<XOR7>(Ls);
          const float wgt = e * __builtin_amdgcn_rcpf(Ls);
#pragma unroll
          for (int i = 0; i < 4; ++i) {
            const float lo = bf(__builtin_amdgcn_readlane(fb(wgt), i)), hi = bf(__builtin_amdgcn_readlane(fb(wgt), 4 + i));
            wj[i] = lane < 32 ? lo : hi;
          }
          asm volatile("" : "+v"(wj[0]), "+v"(wj[1]), "+v"(wj[2]), "+v"(wj[3]));
        }
        if (!mok) ml = ld_sc1_64(rs, mo);
        if (mok && __all(max(max(mx4(a[0]), mx4(a[1])), max(mx4(a[2]), mx4(a[3]))) != SENT)) break;
      }
      ST_(6);
      float t4[4];
#pragma unroll
      for (int k = 0; k < 4; ++k) t4[k] = fmaf(wj[0], bf(a[0][k]), wj[1] * bf(a[1][k])) + fmaf(wj[2], bf(a[2][k]), wj[3] * bf(a[3][k]));
      LAS unsigned* OB = (LAS unsigned*)(L + L_OB) + w * 64;
      OB[2 * (lane & 31) + (lane >> 5)] = pack2(tr32(t4[0], t4[2]), tr32(t4[1], t4[3]));
      MEMBAR();
      const u32x4 oa = *(volatile LAS u32x4*)(OB + 8 * r8), ob = *(volatile LAS u32x4*)(OB + 8 * r8 + 4);
#pragma unroll
      for (int m = 0; m < 4; ++m) mac_packed(acc, wt[2 * m], wt[2 * m + 1], oa[m]);
#pragma unroll
      for (int m = 0; m < 4; ++m) mac_packed(acc, wt[8 + 2 * m], wt[8 + 2 * m + 1], ob[m]);
      const float y = red8_b345(acc, lane);
      YR[w * 64 + col64] = y;
      HBAR();
      if (w == 0 && lane < 16) st_sc1(rs, WS_YP + (g * 640 + cgp * 64 + 4 * lane) * 4, nosent4(sum4(YR + 4 * lane, 64)));
      ST_(7);
      CODE_END(mega_wo);
    } else if (role >= R_FFN && role < R_FFN + NF) {
      const int f = role - R_FFN;
      const int row0 = 160 * w + 10 * r16;
      const int t = w * 64 + lane;
      const int tq = t < 160 ? t : 159;
      const bool fin = f < 20 && w == 3;
      const int oc = (f < 20 ? 32 * f : 0) + 4 * c8 + ((lane >> 4) & 1) * 2 + (lane >> 5);
      const u32x2 xq = __builtin_nontemporal_load((const u32x2*)(x + 4 * tq));
      LAS unsigned* X1 = (LAS unsigned*)(L + L_X1);
      u32x4 wt[18]; unsigned w2t[8];
      CODE_PREFETCH(mega_ffn);
      const u16* wb = w1 + (size_t)row0 * DFF + f * 32 + c4 * 8;
      const u16* w2b = w2 + (size_t)(f * 32 + 8 * w) * D;
#pragma unroll
      for (int i = 0; i < 18; ++i) {
        if (i == FFE) { SB(); TSTART(TA_FFN); }
        if (i < 10) wt[i] = ldnt(wb + (size_t)i * DFF);
        else {
          wt[i] = ldnt(w2b + (i - 10) * D + lane * 8);
          w2t[i - 10] = __builtin_nontemporal_load((const unsigned*)(w2b + (i - 10) * D + 512 + lane * 2));
        }
      }
      SB();
      ST_(5);
      u32x4 ga; u32x2 sbA, sbB, cbA, cbB; unsigned gbw;
      NOUNROLL for (unsigned s = 0; s < SPIN_G; ++s) {
        MEMBAR();
        ga = ld_sc1(rs, WS_M2 + tq * 16);
        gbw = __builtin_amdgcn_raw_buffer_load_b32(rs, WS_M2 + 7680 + oc * 4, 0, 16);
        sbA = ld_sc1_64(rs, WS_M2 + 2560 + (row0 + 2 * c4) * 4); sbB = ld_sc1_64(rs, WS_M2 + 2560 + (row0 + 8) * 4);
        cbA = ld_sc1_64(rs, WS_M2 + 5120 + (row0 + 2 * c4) * 4); cbB = ld_sc1_64(rs, WS_M2 + 5120 + (row0 + 8) * 4);
        const unsigned mm = max(max(mx4(ga), gbw), max(max(max(sbA[0], sbA[1]), max(sbB[0], sbB[1])), max(max(cbA[0], cbA[1]), max(cbB[0], cbB[1]))));
        if (__all(mm != SENT)) break;
        __builtin_amdgcn_s_sleep(2);
      }
      float c1[4] = {rbf(1.f + bf(cbA[0])), rbf(1.f + bf(cbA[1])), rbf(1.f + bf(cbB[0])), rbf(1.f + bf(cbB[1]))};
      float xv[4] = {bflo(xq[0]), bfhi(xq[0]), bflo(xq[1]), bfhi(xq[1])};
      asm volatile("" : "+v"(c1[0]), "+v"(c1[1]), "+v"(c1[2]), "+v"(c1[3]), "+v"(xv[0]), "+v"(xv[1]), "+v"(xv[2]), "+v"(xv[3]));
      const float s1[4] = {bf(sbA[0]), bf(sbA[1]), bf(sbB[0]), bf(sbB[1])};
#pragma unroll
      for (int m = 0; m < 9; ++m) { prepack(wt[2 * m], wt[2 * m + 1]); pin(wt[2 * m]); pin(wt[2 * m + 1]); }
#pragma unroll
      for (int m = 0; m < 4; ++m) {
        const unsigned lo = __builtin_amdgcn_perm(w2t[2 * m + 1], w2t[2 * m], 0x05040100u), hi = __builtin_amdgcn_perm(w2t[2 * m + 1], w2t[2 * m], 0x07060302u);
        w2t[2 * m] = lo; w2t[2 * m + 1] = hi;
        asm volatile("" : "+v"(w2t[2 * m]), "+v"(w2t[2 * m + 1]));
      }
      float acc[8] = {0.f, 0.f, 0.f, 0.f, 0.f, 0.f, 0.f, 0.f};
      asm volatile("" : "+v"(acc[0]), "+v"(acc[1]), "+v"(acc[2]), "+v"(acc[3]), "+v"(acc[4]), "+v"(acc[5]), "+v"(acc[6]), "+v"(acc[7]));
      CODE_ALIGN(mega_ffn, 12);
      if (w < 3) {
        u32x4 a[8];
        NOUNROLL for (unsigned s = 0; s < SPIN_G; ++s) {
          MEMBAR();
#pragma unroll
          for (int i = 0; i < 8; ++i) a[i] = ld_sc1(rs, WS_YP + i * 2560 + tq * 16);
          unsigned mm = mx4(a[0]);
#pragma unroll
          for (int i = 1; i < 8; ++i) mm = max(mm, mx4(a[i]));
          if (__all(mm != SENT)) break;
        }
        ST_(6);
        float x1[4];
#pragma unroll
        for (int k = 0; k < 4; ++k) {
          const float at = ((bf(a[0][k]) + bf(a[1][k])) + (bf(a[2][k]) + bf(a[3][k]))) + ((bf(a[4][k]) + bf(a[5][k])) + (bf(a[6][k]) + bf(a[7][k])));
          x1[k] = xv[k] + rbf(bf(ga[k]) * rbf(at));
        }
        *(LAS u32x2*)(X1 + 2 * tq) = u32x2{pack2(x1[0], x1[1]), pack2(x1[2], x1[3])};
      }
      HBAR();
      unsigned hp5[5];
      {
        const u32x4 ya = *(LAS u32x4*)(X1 + 4 * lane);
        const unsigned yb = X1[256 + lane];
        const unsigned hA = X1[(row0 >> 1) + c4], hB = X1[(row0 >> 1) + 4];
        float sa_ = dot2(yb, 0x3F803F80u, 0.f), sq_ = dot2(yb, yb, 0.f);
#pragma unroll
        for (int k = 0; k < 4; ++k) { sa_ = dot2(ya[k], 0x3F803F80u, sa_); sq_ = dot2(ya[k], ya[k], sq_); }
        float r = tr32(sa_, sq_);
        r = tr16(r, r);
        r = row_sum(r);
        const float mean = bf(__builtin_amdgcn_readlane(fb(r), 0)) * (1.f / 640.f);
        float var = fmaf(-mean, mean, bf(__builtin_amdgcn_readlane(fb(r), 32)) * (1.f / 640.f));
        if (!(var * 1024.f > mean * mean)) {
          float q2 = 0.f;
#pragma unroll
          for (int k = 0; k < 4; ++k) { const float d0 = bflo(ya[k]) - mean, d1 = bfhi(ya[k]) - mean; q2 += d0 * d0 + d1 * d1; }
          const float d0 = bflo(yb) - mean, d1 = bfhi(yb) - mean; q2 += d0 * d0 + d1 * d1;
          var = wave_sum(q2) * (1.f / 640.f);
        }
        const float mr = rbf(mean);
        const float rden = __builtin_amdgcn_rcpf(rbf(__builtin_amdgcn_sqrtf(rbf(rbf(var) + 1e-5f))));
        const float xr2[4] = {bflo(hA), bfhi(hA), bflo(hB), bfhi(hB)};
        float hv[4];
#pragma unroll
        for (int k = 0; k < 4; ++k) hv[k] = rbf(rbf(rbf(xr2[k] - mr) * rden) * c1[k]) + s1[k];
        const unsigned pA = pack2(hv[0], hv[1]);
        hp5[4] = pack2(hv[2], hv[3]);
        hp5[0] = __builtin_amdgcn_mov_dpp((int)pA, 0x00, 0xf, 0xf, true);
        hp5[1] = __builtin_amdgcn_mov_dpp((int)pA, 0x55, 0xf, 0xf, true);
        hp5[2] = __builtin_amdgcn_mov_dpp((int)pA, 0xAA, 0xf, 0xf, true);
        hp5[3] = __builtin_amdgcn_mov_dpp((int)pA, 0xFF, 0xf, 0xf, true);
      }
      ST_(7);
      {
#pragma unroll
        for (int m = 0; m < 5; ++m) mac_packed(acc, wt[2 * m], wt[2 * m + 1], hp5[m]);
        float y = red8_b345(acc, lane);
        y += xor4(y);
        YR[w * 64 + col32] = y;
        float z[8] = {0.f, 0.f, 0.f, 0.f, 0.f, 0.f, 0.f, 0.f};
        float z8 = 0.f, z9 = 0.f;
        asm volatile("" : "+v"(z[0]), "+v"(z[1]), "+v"(z[2]), "+v"(z[3]), "+v"(z[4]), "+v"(z[5]), "+v"(z[6]), "+v"(z[7]), "+v"(z8), "+v"(z9));
        HBAR();
        const LAS float* yp = YR + 8 * w + c8;
        float hd = (yp[0] + yp[64]) + (yp[128] + yp[192]);
        hd = hd < 0.f ? 0.f : hd;
        const unsigned hpk = pack2(hd, dpp<XOR1>(hd));
#pragma unroll
        for (int m = 0; m < 4; ++m) {
          const unsigned hp = __builtin_amdgcn_readlane(hpk, 2 * m);
          mac_packed(z, wt[10 + 2 * m], wt[10 + 2 * m + 1], hp);
          z8 = dot2(w2t[2 * m], hp, z8);
          z9 = dot2(w2t[2 * m + 1], hp, z9);
        }
        LAS float* ZR = (LAS float*)(L + L_ZR);
        *(LAS f32x4*)(ZR + w * 640 + 8 * lane) = f32x4{z[0], z[1], z[2], z[3]};
        *(LAS f32x4*)(ZR + w * 640 + 8 * lane + 4) = f32x4{z[4], z[5], z[6], z[7]};
        *(LAS f32x2*)(ZR + w * 640 + 512 + 2 * lane) = f32x2{z8, z9};
        HBAR();
        if (t < 160) st_sc1(rs, WS_ZP + f * 2560 + t * 16, nosent4(sum4(ZR + 4 * t, 640)));
      }
      ST_(8);
      if (fin) {
        u32x4 zp[7];
        const int zo = WS_ZP + r8 * 2560 + f * 128 + c8 * 16;
        const int z6 = r8 < NF - 48 ? zo + 48 * 2560 : 0x7FFFFF00;
        const float xo = bflo((unsigned)((LAS u16*)X1)[oc]);
        NOUNROLL for (unsigned s = 0; s < SPIN_G; ++s) {
          MEMBAR();
#pragma unroll
          for (int i = 0; i < 6; ++i) zp[i] = ld_sc1(rs, zo + i * 8 * 2560);
          zp[6] = ld_sc1(rs, z6);
          unsigned mm = mx4(zp[0]);
#pragma unroll
          for (int i = 1; i < 7; ++i) mm = max(mm, mx4(zp[i]));
          if (__all(mm != SENT)) break;
        }
        ST_(9);
        float r[4];
#pragma unroll
        for (int k = 0; k < 4; ++k) r[k] = ((bf(zp[0][k]) + bf(zp[1][k])) + (bf(zp[2][k]) + bf(zp[3][k]))) + ((bf(zp[4][k]) + bf(zp[5][k])) + bf(zp[6][k]));
        float v = tr32(tr16(r[0], r[2]), tr16(r[1], r[3]));
        v += dpp<XOR8>(v);
        out[oc] = (u16)f2bf(xo + rbf(bf(gbw) * rbf(v)));
        if (f == 0 && lane == 0) {
          const u64 e1 = (((u64)Ev[1] << 32) | (u64)Ev[0]) + 1ull;
          st_sc1_64(rs, WS_EPOCH, u32x2{(unsigned)e1, (unsigned)(e1 >> 32)});
        }
        ST_(10);
        CODE_END(mega_ffn);
      }
    }
  }
}

extern "C" void kernel_launch(void* const* d_in, const int* in_sizes, int n_in, void* d_out, int out_size, void* d_ws,
                              size_t ws_size, hipStream_t stream) {
  (void)in_sizes; (void)n_in; (void)out_size; (void)ws_size;
  (void)hipFuncSetAttribute((const void*)mega, hipFuncAttributeMaxDynamicSharedMemorySize, LDS_BYTES);
  hipLaunchKernelGGL(mega, dim3(256), dim3(768), LDS_BYTES, stream, (const u16*)d_in[0], (const u16*)d_in[1], (const u16*)d_in[2],
                     (const u16*)d_in[3], (const u16*)d_in[4], (const u16*)d_in[6], (const u16*)d_in[7], (const u16*)d_in[8],
                     (const u16*)d_in[9], (u16*)d_out, (unsigned char*)d_ws);
}
#define STAMP_ROLE 1
#define STAMP_CLS(r) ((r) < N_M2 ? 3 : (r) < N_M2 + N_WO ? 2 : (r) < N_M2 + N_WO + NF ? 4 : 0)
#define NWAVES 12
#define NSTREAM 8
```

```cpp
#include <hip/hip_runtime.h>
#include <hip/hip_bf16.h>
#include <stdint.h>

#define STAMP 0
#define PSLEEP 16
#define I1 12
#define I2 21
#define I3 17
#define IT 31
#define IS 26
#define ITK 22
#define PREB 1
#define IB 24
#define TA_M2 0
#define TA_WO 2
#define TA_FFN 2
#define TDEPF 40
#define TSTART(m) do { if ((m) == 2) lds_wait(L, C_PV, 9u); else if ((m) == 1) lds_wait8(L, IT); } while (0)
#define MP10 1
#define T1 380
#define T3 500
#define T2 680
#define TM2 950
#define DEPA 1
#define ISW 8
#define WQE 1
#define FAUX 16
#define MLAUX 16
#define CAUX 16
#define CPF 16
#define CPO 5120
#define R_WO 96
#define R_M2 0
#define R_FFN 40
#define MAP 0
#define CAL 0
#define WOE 12
#define FFE 5
#define SFIRST 0
#define KARG 1
#define EPF 0
#define IE 22
#define MSL 2
#define DEP 2
#define TDEP 40

#define GAS __attribute__((address_space(1)))
#define LAS __attribute__((address_space(3)))
#define RLX_AGENT __ATOMIC_RELAXED, __HIP_MEMORY_SCOPE_AGENT
typedef unsigned short u16;
typedef unsigned long long u64;
typedef unsigned u32x4 __attribute__((ext_vector_type(4)));
typedef unsigned u32x2 __attribute__((ext_vector_type(2)));
typedef float f32x4 __attribute__((ext_vector_type(4)));
typedef float f32x2 __attribute__((ext_vector_type(2)));
typedef __bf16 bf16x2 __attribute__((ext_vector_type(2)));
typedef GAS u64 gu64;
typedef __amdgpu_buffer_rsrc_t rsrc_t;

constexpr int D = 640, HQ = 32, SK = 16384, DH = 128, WIN = 4096, DFF = 1728;
constexpr int NF = 54;

constexpr int WS_EPOCH = 0;
constexpr int WS_FLAGS = 4096;
constexpr int WS_TICKET = 2048;
constexpr int NMP = 4;
constexpr int WS_MP = 8192;
constexpr int WS_QP = 131072;
constexpr int WS_OP = WS_QP + 131072;
constexpr int WS_ST = WS_OP + 131072;
constexpr int WS_YP = WS_ST + 32768;
constexpr int WS_M2 = WS_YP + 20480;
constexpr int WS_ZP = WS_M2 + 10240;
constexpr int WS_END = WS_ZP + NF * 2560;
constexpr int N_M2 = 40, N_WO = 80;
constexpr int WS_STAMP = 2 << 20;

constexpr unsigned SENT = 0xFFFFFFFFu;
constexpr unsigned SPIN_G = 1u << 18;
constexpr unsigned SPIN_L = 1u << 22;

constexpr int C_PROG = 0;
constexpr int C_MOD1 = 8, C_QP = 9, C_QREADY = 10, C_M = 11, C_ROLE = 12, C_PV = 13, C_FLAGOK = 14, C_T1 = 15, C_WOUT = 16;
constexpr int L_P = 128;
constexpr int L_MR = L_P + 1024;
constexpr int L_QR = L_MR + 1024;
constexpr int L_OR = L_QR + 2048;
constexpr int L_OB = L_OR + 4096;
constexpr int L_YR = L_OB + 1024;
constexpr int L_STAT = L_YR + 1024;
constexpr int L_Q = L_STAT + 64;
constexpr int L_X1 = L_Q + 256;
constexpr int L_ZR = L_X1 + 2560;
constexpr int L_END = L_ZR + 10240;
constexpr int LDS_BYTES = 96 * 1024;
static_assert(L_END <= LDS_BYTES, "lds");

__device__ __forceinline__ float bflo(unsigned w) { return __builtin_bit_cast(float, w << 16); }
__device__ __forceinline__ float bfhi(unsigned w) { return __builtin_bit_cast(float, w & 0xffff0000u); }
__device__ __forceinline__ unsigned f2bf(float f) { return (unsigned)__builtin_bit_cast(u16, (__bf16)f); }
__device__ __forceinline__ float rbf(float f) { return bflo(f2bf(f)); }
__device__ __forceinline__ unsigned pack2(float lo, float hi) {
  bf16x2 v = {(__bf16)lo, (__bf16)hi};
  return __builtin_bit_cast(unsigned, v);
}
__device__ __forceinline__ float dot2(unsigned a, unsigned b, float acc) {
  return __builtin_amdgcn_fdot2_f32_bf16(__builtin_bit_cast(bf16x2, a), __builtin_bit_cast(bf16x2, b), acc, false);
}
__device__ __forceinline__ unsigned fb(float f) { return __builtin_bit_cast(unsigned, f); }
__device__ __forceinline__ float bf(unsigned u) { return __builtin_bit_cast(float, u); }
__device__ __forceinline__ unsigned nosent(float f) { return min(fb(f), 0xFFFFFFFEu); }
__device__ __forceinline__ u32x4 nosent4(f32x4 s) { return u32x4{nosent(s[0]), nosent(s[1]), nosent(s[2]), nosent(s[3])}; }
template <int CTRL> __device__ __forceinline__ float dpp(float x) {
  return __builtin_bit_cast(float, __builtin_amdgcn_mov_dpp(__builtin_bit_cast(int, x), CTRL, 0xf, 0xf, true));
}
constexpr int XOR1 = 0xB1, XOR2 = 0x4E, XOR7 = 0x141, XOR8 = 0x128;
__device__ __forceinline__ float xor4(float x) {
  int v = __builtin_bit_cast(int, x);
  int r = __builtin_amdgcn_update_dpp(0, v, 0x114, 0xf, 0xa, false);
  r = __builtin_amdgcn_update_dpp(r, v, 0x104, 0xf, 0x5, false);
  return __builtin_bit_cast(float, r);
}
__device__ __forceinline__ float tr16(float a, float b) {
  auto s = __builtin_amdgcn_permlane16_swap(fb(a), fb(b), false, false);
  return bf(s[0]) + bf(s[1]);
}
__device__ __forceinline__ float tr32(float a, float b) {
  auto s = __builtin_amdgcn_permlane32_swap(fb(a), fb(b), false, false);
  return bf(s[0]) + bf(s[1]);
}
__device__ __forceinline__ float mx16(float a) {
  auto s = __builtin_amdgcn_permlane16_swap(fb(a), fb(a), false, false);
  return fmaxf(bf(s[0]), bf(s[1]));
}
__device__ __forceinline__ float mx32(float a) {
  auto s = __builtin_amdgcn_permlane32_swap(fb(a), fb(a), false, false);
  return fmaxf(bf(s[0]), bf(s[1]));
}
__device__ __forceinline__ float row_sum(float x) {
  x += dpp<XOR1>(x); x += dpp<XOR2>(x); x += dpp<XOR7>(x); x += dpp<XOR8>(x); return x;
}
__device__ __forceinline__ float row_max(float x) {
  x = fmaxf(x, dpp<XOR1>(x)); x = fmaxf(x, dpp<XOR2>(x)); x = fmaxf(x, dpp<XOR7>(x)); x = fmaxf(x, dpp<XOR8>(x)); return x;
}
__device__ __forceinline__ float wave_sum(float x) { x = row_sum(x); x = tr16(x, x); return tr32(x, x); }
__device__ __forceinline__ float wave_max(float x) { x = row_max(x); x = mx16(x); return mx32(x); }

__device__ __forceinline__ unsigned bij32(unsigned x) {
  x += 0x9E3779B9u; x ^= x >> 16; x *= 0x85EBCA6Bu; x ^= x >> 13; x *= 0xC2B2AE35u; x ^= x >> 16; return x;
}
__device__ __forceinline__ rsrc_t mkrs(const void* p, int bytes) { return __builtin_amdgcn_make_buffer_rsrc((void*)p, 0, bytes, 0x00020000); }
__device__ __forceinline__ u32x4 ldnt(const void* p) { return __builtin_nontemporal_load(reinterpret_cast<const u32x4*>(p)); }
__device__ __forceinline__ u32x4 ld_sc1(rsrc_t rs, int off) { return __builtin_amdgcn_raw_buffer_load_b128(rs, off, 0, 16); }
__device__ __forceinline__ u32x4 ld_sc1s(rsrc_t rs, int voff, int soff) { return __builtin_amdgcn_raw_buffer_load_b128(rs, voff, soff, 16); }
__device__ __forceinline__ u32x2 ld_sc1_64(rsrc_t rs, int off) { return __builtin_amdgcn_raw_buffer_load_b64(rs, off, 0, 16); }
__device__ __forceinline__ void st_sc1(rsrc_t rs, int off, u32x4 v) { __builtin_amdgcn_raw_buffer_store_b128(v, rs, off, 0, 16); }
__device__ __forceinline__ void st_sc1_64(rsrc_t rs, int off, u32x2 v) { __builtin_amdgcn_raw_buffer_store_b64(v, rs, off, 0, 16); }
__device__ __forceinline__ u32x4 ld_f(rsrc_t rs, int off) { return __builtin_amdgcn_raw_buffer_load_b128(rs, off, 0, FAUX); }
__device__ __forceinline__ u32x2 ld_f64(rsrc_t rs, int off) { return __builtin_amdgcn_raw_buffer_load_b64(rs, off, 0, FAUX); }
__device__ __forceinline__ void st_f(rsrc_t rs, int off, u32x4 v) { __builtin_amdgcn_raw_buffer_store_b128(v, rs, off, 0, FAUX); }
__device__ __forceinline__ void st_f64(rsrc_t rs, int off, u32x2 v) { __builtin_amdgcn_raw_buffer_store_b64(v, rs, off, 0, FAUX); }
__device__ __forceinline__ void st_c(rsrc_t rs, int off, u32x4 v) { __builtin_amdgcn_raw_buffer_store_b128(v, rs, off, 0, CAUX); }
__device__ __forceinline__ unsigned mx4(u32x4 v) { return max(max(v[0], v[1]), max(v[2], v[3])); }
#define MEMBAR() asm volatile("" ::: "memory")
#define LGKM0() asm volatile("s_waitcnt lgkmcnt(0)" ::: "memory")
#define SB() __builtin_amdgcn_sched_barrier(0)
#define NOUNROLL _Pragma("nounroll")

__device__ __forceinline__ void lds_put(LAS unsigned char* L, int word, unsigned v) { ((volatile LAS unsigned*)L)[word] = v; }
__device__ __forceinline__ void lds_wait(LAS unsigned char* L, int word, unsigned v) {
  NOUNROLL for (unsigned s = 0; s < SPIN_L; ++s) {
    if (((volatile LAS unsigned*)L)[word] >= v) break;
    __builtin_amdgcn_s_sleep(1);
  }
  MEMBAR();
}
__device__ __forceinline__ void lds_wait4(LAS unsigned char* L, int word, unsigned v) {
  NOUNROLL for (unsigned s = 0; s < SPIN_L; ++s) {
    const u32x4 q = *(volatile LAS u32x4*)(L + word * 4);
    if (min(min(q[0], q[1]), min(q[2], q[3])) >= v) break;
    __builtin_amdgcn_s_sleep(1);
  }
  MEMBAR();
}
__device__ __forceinline__ void lds_spin(LAS unsigned char* L, int word, unsigned v) {
  NOUNROLL for (unsigned s = 0; s < SPIN_L; ++s) {
    if (((volatile LAS unsigned*)L)[word] >= v) break;
  }
  MEMBAR();
}
__device__ __forceinline__ unsigned lds_arrive(int word) {
  unsigned old;
  asm volatile("s_mov_b64 exec, 1\n\tds_add_rtn_u32 %0, %1, %2\n\ts_mov_b64 exec, -1\n\ts_waitcnt lgkmcnt(0)"
               : "=&v"(old) : "v"(word * 4), "v"(1u) : "memory");
  return __builtin_amdgcn_readfirstlane(old);
}
__device__ __forceinline__ void lds_arrive_nr(int word) {
  asm volatile("s_mov_b64 exec, 1\n\tds_add_u32 %0, %1\n\ts_mov_b64 exec, -1" :: "v"(word * 4), "v"(1u) : "memory");
}
__device__ __forceinline__ void lds_wait8(LAS unsigned char* L, unsigned v) {
  NOUNROLL for (unsigned s = 0; s < SPIN_L; ++s) {
    const u32x4 q = *(volatile LAS u32x4*)L, r = *(volatile LAS u32x4*)(L + 16);
    if (min(min(min(q[0], q[1]), min(q[2], q[3])), min(min(r[0], r[1]), min(r[2], r[3]))) >= v) break;
    __builtin_amdgcn_s_sleep(2);
  }
  MEMBAR();
}
#define HBAR() do { LGKM0(); __builtin_amdgcn_s_barrier(); MEMBAR(); } while (0)
__device__ __forceinline__ void wait_until(u64 t) {
  NOUNROLL for (unsigned s = 0; s < 4096u; ++s) {
    if (__builtin_amdgcn_s_memrealtime() >= t) break;
    __builtin_amdgcn_s_sleep(2);
  }
  MEMBAR();
}
template <int N> __device__ __forceinline__ void vmwait() { asm volatile("s_waitcnt vmcnt(%0)" :: "i"(N) : "memory"); }

#define ST_(k) do {} while (0)
#define CNT_(sh) do {} while (0)

__device__ __forceinline__ float red8_b345(const float (&a)[8], int lane) {
  float v[4];
  const bool hi = (lane & 8) != 0;
#pragma unroll
  for (int k = 0; k < 4; ++k) {
    const float t1 = a[k] + dpp<XOR8>(a[k]), t2 = a[k + 4] + dpp<XOR8>(a[k + 4]);
    v[k] = hi ? t2 : t1;
  }
  const float u0 = tr16(v[0], v[2]), u1 = tr16(v[1], v[3]);
  return tr32(u0, u1);
}
__device__ __forceinline__ void prepack(u32x4& wa, u32x4& wb) {
#pragma unroll
  for (int d = 0; d < 4; ++d) {
    const unsigned lo = __builtin_amdgcn_perm(wb[d], wa[d], 0x05040100u);
    const unsigned hi = __builtin_amdgcn_perm(wb[d], wa[d], 0x07060302u);
    wa[d] = lo; wb[d] = hi;
  }
}
__device__ __forceinline__ void pin(u32x4& a) { asm volatile("" : "+v"(a)); }
__device__ __forceinline__ void mac_packed(float (&acc)[8], u32x4 wa, u32x4 wb, unsigned xp) {
#pragma unroll
  for (int d = 0; d < 4; ++d) { acc[2 * d] = dot2(wa[d], xp, acc[2 * d]); acc[2 * d + 1] = dot2(wb[d], xp, acc[2 * d + 1]); }
}
__device__ __forceinline__ void mac_pair(float (&acc)[8], u32x4 wa, u32x4 wb, unsigned xp) { prepack(wa, wb); mac_packed(acc, wa, wb, xp); }
__device__ __forceinline__ float silu_f(float v) { return v * __builtin_amdgcn_rcpf(1.f + __expf(-v)); }
__device__ __forceinline__ f32x4 sum4(const LAS float* p, int stride) {
  return (*(const LAS f32x4*)p + *(const LAS f32x4*)(p + stride)) + (*(const LAS f32x4*)(p + 2 * stride) + *(const LAS f32x4*)(p + 3 * stride));
}

#define CODE_PREFETCH(NAME) do { if (CPF && w == 3) { \
      u64 pc_; int len_; \
      asm volatile("s_getpc_b64 %0\n.L" #NAME "_beg:\n\ts_mov_b32 %1, .L" #NAME "_end-.L" #NAME "_beg" : "=s"(pc_), "=s"(len_)); \
      len_ = min(max(len_, 0), 16384); \
      const rsrc_t rc_ = mkrs((const void*)pc_, len_); \
      const unsigned c_ = __builtin_amdgcn_raw_buffer_load_b32(rc_, max(len_ - 4096, 0) + lane * 64, 0, 0); \
      const unsigned d_ = __builtin_amdgcn_raw_buffer_load_b32(rc_, lane * 64, 0, 0); \
      asm volatile("" :: "v"(c_), "v"(d_)); } } while (0)
#define CODE_ALIGN(NAME, P) do { if (CAL) asm volatile("s_branch .L" #NAME "_al\n\t.p2align " #P "\n.L" #NAME "_al:"); } while (0)
#define CODE_END(NAME) asm volatile(".L" #NAME "_end:")

__global__ void __launch_bounds__(768, 3)
mega(const u16* __restrict__ kc, const u16* __restrict__ vc, unsigned char* ws, const u16* __restrict__ wmod,
     const u16* __restrict__ cnd, const u16* __restrict__ x, const u16* __restrict__ wq, const u16* __restrict__ wo,
     const u16* __restrict__ w1, const u16* __restrict__ w2, u16* __restrict__ out) {
  asm volatile("" :: "s"(kc), "s"(vc), "s"(ws), "s"(wmod), "s"(cnd), "s"(x), "s"(wq), "s"(wo));
  extern __shared__ __attribute__((aligned(16))) unsigned char smem[];
  LAS unsigned char* L = (LAS unsigned char*)smem;
  const int tid = threadIdx.x, lane = tid & 63, wave = __builtin_amdgcn_readfirstlane(tid >> 6);
  const int b = blockIdx.x;
  const int h = MAP ? b >> 3 : (b & 7) * 4 + (b >> 6), j = MAP ? b & 7 : (b >> 3) & 7, u = h * 8 + j;
  const int shard = (b >> 3) & 7;
  const int r4 = lane >> 4, c16 = lane & 15;
  const rsrc_t rs = mkrs(ws, WS_END);
  ST_(0);
  if (EPF && b >= 256 - EPF && wave == 7) {
    u64 pc_; int l0_, l1_, l2_;
    asm volatile("s_getpc_b64 %0\n.Lmega_pc0:\n\ts_mov_b32 %1, .Lmega_ffn_end-.Lmega_pc0\n\ts_mov_b32 %2, .Lmega_wo_end-.Lmega_pc0\n\ts_mov_b32 %3, .Lmega_s_end-.Lmega_pc0"
                 : "=s"(pc_), "=s"(l0_), "=s"(l1_), "=s"(l2_));
    const rsrc_t rc_ = mkrs((const void*)pc_, min(max(max(l0_, l1_), max(l2_, 0)), 24576));
    unsigned c_[6];
#pragma unroll
    for (int i = 0; i < 6; ++i) c_[i] = __builtin_amdgcn_raw_buffer_load_b32(rc_, lane * 64, i * 4096, 0);
    asm volatile("" :: "v"(c_[0]), "v"(c_[1]), "v"(c_[2]), "v"(c_[3]), "v"(c_[4]), "v"(c_[5]));
  }

  if (__builtin_expect(wave < 8, SFIRST)) {
    const size_t kvoff = ((size_t)h * SK + (SK - WIN) + j * 512) * DH;
    const rsrc_t rk = mkrs(kc + kvoff, 131072), rv = mkrs(vc + kvoff, 131072);
    const int rot = (b * 8 + (b >> 4) + wave) * 1024;
    u32x4 big[32];
    u32x4 pp0, pp1;
    float mw;
    LAS float* STAT = (LAS float*)(L + L_STAT);
#define LOAD_STEP(i) do { \
      big[i] = __builtin_amdgcn_raw_buffer_load_b128((i) < 16 ? rk : rv, lane * 16, (((i) & 15) * 8192 + rot) & 131071, 2); \
      if ((i) == 0) ST_(7); \
      if ((i) == 2) ST_(8); \
      if ((i) == PREB - 1) __builtin_amdgcn_s_barrier();     \
      if ((i) == ITK || (i) == IT || (i) == IE) lds_put(L, C_PROG + wave, (unsigned)(i)); \
      if ((i) >= 16 + DEP + 2 && (((i) - DEP) & 1) == 0 && (i) <= IB) { prepack(big[(i) - DEP - 2], big[(i) - DEP - 1]); pin(big[(i) - DEP - 2]); pin(big[(i) - DEP - 1]); } \
      if ((i) < ISW) { if ((i) >= DEPA) vmwait<DEPA>(); } else if ((i) < IB && (i) != IS) vmwait<DEP>(); \
      SB(); } while (0)
#pragma unroll
    for (int i = 0; i <= IS; ++i) LOAD_STEP(i);
    {
      ST_(2); lds_wait(L, C_QREADY, 1u); ST_(3);
      const u32x4 qp = *(LAS u32x4*)(L + L_Q + c16 * 16);
      float xs[16];
#pragma unroll
      for (int ii = 0; ii < 16; ++ii) {
        float d = 0.f;
#pragma unroll
        for (int k = 0; k < 4; ++k) d = dot2(big[ii][k], qp[k], d);
        xs[ii] = d;
      }
#define TR_STEP(HALF, CTRL, BIT) _Pragma("unroll") for (int q = 0; q < (HALF); ++q) { \
      const float lo_ = xs[q], hi_ = xs[q + (HALF)]; const float t1_ = lo_ + dpp<CTRL>(lo_), t2_ = hi_ + dpp<CTRL>(hi_); \
      xs[q] = (c16 & (BIT)) ? t2_ : t1_; }
      TR_STEP(8, XOR8, 8) TR_STEP(4, XOR7, 4) TR_STEP(2, XOR2, 2) TR_STEP(1, XOR1, 1)
#undef TR_STEP
      const float sc = rbf(rbf(xs[0]) * 0.08838834764831845f);
      mw = wave_max(sc);
      const float e = __expf(sc - mw);
      LAS u16* P = (LAS u16*)(L + L_P) + wave * 64;
      P[lane] = (u16)f2bf(e);
      MEMBAR();
      pp0 = *(volatile LAS u32x4*)(P + r4 * 16); pp1 = *(volatile LAS u32x4*)(P + r4 * 16 + 8);
      STAT[wave] = mw;
      STAT[8 + wave] = wave_sum(e);
      if (lds_arrive(C_M) == 7u) {
        const float m8 = STAT[lane & 7], l8 = STAT[8 + (lane & 7)];
        float mc = fmaxf(m8, dpp<XOR1>(m8)); mc = fmaxf(mc, dpp<XOR2>(mc)); mc = fmaxf(mc, dpp<XOR7>(mc));
        float lc = l8 * __expf(m8 - mc);
        lc += dpp<XOR1>(lc); lc += dpp<XOR2>(lc); lc += dpp<XOR7>(lc);
        if (lane == 0) __builtin_amdgcn_raw_buffer_store_b64(u32x2{nosent(mc), nosent(lc)}, rs, WS_ST + u * 128, 0, MLAUX);
      }
          ST_(4);
      }
#pragma unroll
    for (int i = IS + 1; i < 32; ++i) LOAD_STEP(i);
    ST_(1);
    __builtin_amdgcn_s_setprio(2);
    {
      float acc[8] = {0.f, 0.f, 0.f, 0.f, 0.f, 0.f, 0.f, 0.f};
      constexpr int NPRE = ((IB < 30 ? IB : 30) - 16 - DEP) / 2;
      float scl;
#pragma unroll
      for (int m = 0; m < 8; ++m) {
        if (m == NPRE) {
          lds_spin(L, C_M, 8u);
          const f32x4 ma = *(LAS f32x4*)STAT, mb = *(LAS f32x4*)(STAT + 4);
          const float mc = fmaxf(fmaxf(fmaxf(ma[0], ma[1]), fmaxf(ma[2], ma[3])), fmaxf(fmaxf(mb[0], mb[1]), fmaxf(mb[2], mb[3])));
          scl = __expf(mw - mc);
          asm volatile("" : "+v"(scl));
        }
        if (m >= NPRE) prepack(big[16 + 2 * m], big[16 + 2 * m + 1]);
        mac_packed(acc, big[16 + 2 * m], big[16 + 2 * m + 1], m < 4 ? pp0[m & 3] : pp1[m & 3]);
        SB();
      }
      ST_(5);
      float v[4];
#pragma unroll
      for (int k = 0; k < 4; ++k) v[k] = tr16(acc[k], acc[k + 4]);
      const float y0 = tr32(v[0], v[2]) * scl, y1 = tr32(v[1], v[3]) * scl;
      LAS float* OR = (LAS float*)(L + L_OR);
      *(LAS f32x2*)(OR + wave * 128 + c16 * 8 + (r4 & 1) * 4 + (r4 >> 1) * 2) = f32x2{y0, y1};
      if (lds_arrive(C_PV) == 7u) {
        const f32x4 s = sum4(OR + (lane >> 5) * 512 + 4 * (lane & 31), 128);
        f32x4 t;
#pragma unroll
        for (int k = 0; k < 4; ++k) t[k] = tr32(s[k], s[k]);
        if (lane < 32) st_sc1(rs, WS_OP + u * 512 + lane * 16, nosent4(t));
        MEMBAR();
        lds_put(L, C_PV, 9u);
      }
      ST_(6);
    }
    if (wave < 5) {
      lds_wait(L, C_ROLE, 1u);
      const int role = __builtin_amdgcn_readfirstlane(((volatile LAS unsigned*)L)[C_ROLE]) - 1;
      if (role >= R_FFN && role < R_FFN + NF) {
        asm volatile(".Lmega_gat_beg:");
        const int t = wave * 64 + lane, hf = lane & 1;
        lds_wait(L, C_WOUT, 4u);
        const unsigned xq = __builtin_nontemporal_load((const unsigned*)x + t);
        u32x2 g0;
        NOUNROLL for (unsigned s = 0; s < SPIN_G; ++s) {
          MEMBAR();
          g0 = ld_sc1_64(rs, WS_M2 + t * 8);
          if (__all(max(g0[0], g0[1]) != SENT)) break;
          __builtin_amdgcn_s_sleep(MSL);
        }
        float x0 = bflo(xq), x1 = bfhi(xq);
        asm volatile("" : "+v"(x0), "+v"(x1));
        u32x4 a[4];
        const int off = hf * 4 * 2560 + (t >> 1) * 16;
        NOUNROLL for (unsigned s = 0; s < SPIN_G; ++s) {
          MEMBAR();
#pragma unroll
          for (int i = 0; i < 4; ++i) a[i] = ld_sc1s(rs, off, WS_YP + i * 2560);
          if (__all(max(max(mx4(a[0]), mx4(a[1])), max(mx4(a[2]), mx4(a[3]))) != SENT)) break;
        }
        ST_(9);
        float sm[4];
#pragma unroll
        for (int k = 0; k < 4; ++k) sm[k] = (bf(a[0][k]) + bf(a[1][k])) + (bf(a[2][k]) + bf(a[3][k]));
        const float at0 = (hf ? sm[2] : sm[0]) + dpp<XOR1>(hf ? sm[0] : sm[2]), at1 = (hf ? sm[3] : sm[1]) + dpp<XOR1>(hf ? sm[1] : sm[3]);
        ((LAS unsigned*)(L + L_X1))[t] = pack2(x0 + rbf(bf(g0[0]) * rbf(at0)), x1 + rbf(bf(g0[1]) * rbf(at1)));
        HBAR();
        ST_(10);
        asm volatile(".Lmega_gat_end:");
      }
    }
    CODE_END(mega_s);
  } else {
    const int w = wave - 8;
    int zero_v;
    asm volatile("v_mov_b32 %0, 0" : "=v"(zero_v));
    const int r8 = lane >> 3, c8 = lane & 7;
    __builtin_amdgcn_s_setprio(3);
    const u64 tstart = __builtin_amdgcn_s_memrealtime();
    const bool m1 = b < 80;
    const int cg = b % 20, rg = b / 20;
    u32x4 wm[5]; unsigned cw0, cw1, cw2;
#define MOD_TILE_LOAD() do {  \
      const int row0 = 160 * rg + 40 * w; \
      const u16* wb = wmod + (size_t)row0 * (6 * D) + cg * 64 + c8 * 8; \
      wm[0] = ldnt(wb + (size_t)(2 * r8) * (6 * D)); wm[1] = ldnt(wb + (size_t)(2 * r8 + 1) * (6 * D)); \
      wm[2] = ldnt(wb + (size_t)(16 + 2 * r8) * (6 * D)); wm[3] = ldnt(wb + (size_t)(17 + 2 * r8) * (6 * D)); \
      wm[4] = ldnt(wb + (size_t)(32 + r8) * (6 * D)); \
      cw0 = __builtin_nontemporal_load((const unsigned*)(cnd + row0 + 2 * r8)); \
      cw1 = __builtin_nontemporal_load((const unsigned*)(cnd + row0 + 16 + 2 * r8)); \
      cw2 = __builtin_nontemporal_load(cnd + row0 + 32 + r8); \
     } while (0)
    if (m1) MOD_TILE_LOAD();
    const u32x2 Ev = ld_sc1_64(rs, WS_EPOCH + zero_v);
    const int l20 = (lane & 31) < 20 ? (lane & 31) : 19;
    const int hrow = 80 * j + 20 * w + l20;
    const u32x4 xa = ldnt(x + lane * 8);
    const unsigned xb = __builtin_nontemporal_load((const unsigned*)x + 256 + lane);
    const float xr = bflo((unsigned)__builtin_nontemporal_load(x + hrow));
    if (w == 3) {
      if (lane < 32) lds_put(L, lane, 0u);
    }
    SB();
    LGKM0();
    __builtin_amdgcn_s_barrier();
    SB();
    u32x4 wqv[5];
    if (WQE) {
#pragma unroll
      for (int i = 0; i < 5; ++i) wqv[i] = ldnt(wq + (size_t)(80 * j + 20 * w + 5 * r4 + i) * (HQ * DH) + h * DH + c16 * 8);
      SB();
    }
    const unsigned tag = bij32(Ev[0]), flo = bij32(Ev[0] ^ Ev[1] ^ 0x5bd1e995u);
#define MOD_TILE_COMPUTE() do {  \
      float acc[8] = {0.f, 0.f, 0.f, 0.f, 0.f, 0.f, 0.f, 0.f}; \
      mac_pair(acc, wm[0], wm[1], pack2(silu_f(bflo(cw0)), silu_f(bfhi(cw0)))); \
      mac_pair(acc, wm[2], wm[3], pack2(silu_f(bflo(cw1)), silu_f(bfhi(cw1)))); \
      { \
        const unsigned sl = f2bf(silu_f(bflo(cw2))), sh = sl << 16; \
_Pragma("unroll")         for (int d = 0; d < 4; ++d) { acc[2 * d] = dot2(wm[4][d], sl, acc[2 * d]); acc[2 * d + 1] = dot2(wm[4][d], sh, acc[2 * d + 1]); } \
      } \
      const float y = red8_b345(acc, lane); \
      const int col = c8 * 8 + ((lane >> 3) & 1) * 4 + ((lane >> 4) & 1) * 2 + (lane >> 5); \
      LAS float* MR = (LAS float*)(L + L_MR); \
      MR[w * 64 + col] = y; \
      if (lds_arrive(C_MOD1) == 3u) { \
        const float s = (MR[lane] + MR[64 + lane]) + (MR[128 + lane] + MR[192 + lane]); \
        st_f64(rs, WS_MP + (rg * 1280 + cg * 64 + lane) * 8, u32x2{fb(s), tag}); \
      } \
     } while (0)
    if (m1) MOD_TILE_COMPUTE();
    ST_(1);
    if (w == 3) {
      u32x4 sv = {SENT, SENT, SENT, SENT};
      st_c(rs, (lane < 32 ? WS_QP : WS_OP) + u * 512 + (lane & 31) * 16, sv);
      if (b >= 128 && b < 128 + NF) {
        st_c(rs, WS_ZP + (b - 128) * 2560 + lane * 16, sv);
        st_c(rs, WS_ZP + (b - 128) * 2560 + 1024 + lane * 16, sv);
      }
      int o2 = 0x7FFFFF00;
      if (lane == 63) o2 = WS_ST + u * 128;
      else if (b < 120) { if (lane < 16) o2 = WS_YP + b * 256 + lane * 16; }
      else if (b >= 128 && b < 128 + NF) { if (lane < 32) o2 = WS_ZP + (b - 128) * 2560 + 2048 + lane * 16; }
      else if (b == 255 && lane < 8) { o2 = WS_TICKET + lane * 128; sv = u32x4{0u, 0u, 0u, 0u}; }
      st_c(rs, o2, sv);
          vmwait<0>();
      if (lane == 0) st_f64(rs, WS_FLAGS + b * 8, u32x2{flo, tag});
    }
    {
      if (!WQE) {
#pragma unroll
        for (int i = 0; i < 5; ++i) wqv[i] = ldnt(wq + (size_t)(80 * j + 20 * w + 5 * r4 + i) * (HQ * DH) + h * DH + c16 * 8);
      }
      SB();
      float nrm;
      {
        float s = bflo(xb) + bfhi(xb);
#pragma unroll
        for (int i = 0; i < 4; ++i) s += bflo(xa[i]) + bfhi(xa[i]);
        const float mean = wave_sum(s) * (1.f / 640.f);
        float q2 = (bflo(xb) - mean) * (bflo(xb) - mean) + (bfhi(xb) - mean) * (bfhi(xb) - mean);
#pragma unroll
        for (int i = 0; i < 4; ++i) { const float d0 = bflo(xa[i]) - mean, d1 = bfhi(xa[i]) - mean; q2 += d0 * d0 + d1 * d1; }
        const float var = wave_sum(q2) * (1.f / 640.f);
        const float den = rbf(sqrtf(rbf(rbf(var) + 1e-5f)));
        nrm = rbf(rbf(xr - rbf(mean)) / den);
      }
      const int mp = WS_MP + ((lane >> 5) * 640 + hrow) * 8;
      u32x2 g[NMP];
      wait_until(tstart + T1);
      NOUNROLL for (unsigned s = 0; s < SPIN_G; ++s) {
        bool ok = true;
        MEMBAR();
#pragma unroll
        for (int i = 0; i < NMP; ++i) g[i] = ld_f64(rs, mp + i * 1280 * 8);
#pragma unroll
        for (int i = 0; i < NMP; ++i) ok = ok & (g[i][1] == tag);
        CNT_(0);
        if (__all(ok)) break;
        __builtin_amdgcn_s_sleep(PSLEEP);
      }
      const float mv = rbf((bf(g[0][0]) + bf(g[1][0])) + (bf(g[2][0]) + bf(g[3][0])));
      auto sw = __builtin_amdgcn_permlane32_swap(fb(mv), fb(mv), false, false);
      const float hval = rbf(rbf(nrm * rbf(1.f + bf(sw[1]))) + bf(sw[0]));
      ST_(2);
      float acc[8] = {0.f, 0.f, 0.f, 0.f, 0.f, 0.f, 0.f, 0.f};
#pragma unroll
      for (int i = 0; i < 5; ++i) {
        const float hk = __shfl(hval, 5 * r4 + i);
#pragma unroll
        for (int d = 0; d < 4; ++d) { acc[2 * d] = fmaf(hk, bflo(wqv[i][d]), acc[2 * d]); acc[2 * d + 1] = fmaf(hk, bfhi(wqv[i][d]), acc[2 * d + 1]); }
      }
      float v[4];
#pragma unroll
      for (int k = 0; k < 4; ++k) v[k] = tr16(acc[k], acc[k + 4]);
      const float y0 = tr32(v[0], v[2]), y1 = tr32(v[1], v[3]);
      LAS float* QR = (LAS float*)(L + L_QR);
      *(LAS f32x2*)(QR + w * 128 + c16 * 8 + (r4 & 1) * 4 + (r4 >> 1) * 2) = f32x2{y0, y1};
      if (lds_arrive(C_QP) == 3u) {
        if (lane < 32) st_f(rs, WS_QP + u * 512 + lane * 16, nosent4(sum4(QR + 4 * lane, 128)));
      }
    }
    ST_(3);
    if (w == 1) {
      wait_until(tstart + T3);
      NOUNROLL for (unsigned s = 0; s < SPIN_G; ++s) {
        MEMBAR();
        const u32x4 g0 = ld_f(rs, WS_FLAGS + lane * 16), g1 = ld_f(rs, WS_FLAGS + 1024 + lane * 16);
        const unsigned bad = ((g0[0] ^ flo) | (g0[1] ^ tag)) | ((g0[2] ^ flo) | (g0[3] ^ tag)) | ((g1[0] ^ flo) | (g1[1] ^ tag)) | ((g1[2] ^ flo) | (g1[3] ^ tag));
        if (__all(bad == 0u)) break;
        __builtin_amdgcn_s_sleep(PSLEEP);
      }
      lds_put(L, C_FLAGOK, 1u);
      lds_wait(L, C_PROG, ITK);
      if (lane == 0) {
        const unsigned tk = __hip_atomic_fetch_add((GAS unsigned*)(ws + WS_TICKET + shard * 128), 1u, RLX_AGENT);
        lds_put(L, C_ROLE, tk * 8u + (unsigned)shard + 1u);
      }
    } else if (w == 0) {
      u32x4 a0, a1, b0, b1;
      const int off = WS_QP + (h * 8 + 2 * r4) * 512 + c16 * 32;
      lds_wait(L, C_FLAGOK, 1u);
      wait_until(tstart + T2);
      NOUNROLL for (unsigned s = 0; s < SPIN_G; ++s) {
        MEMBAR();
        a0 = ld_f(rs, off); a1 = ld_f(rs, off + 16); b0 = ld_f(rs, off + 512); b1 = ld_f(rs, off + 528);
        CNT_(16);
        if (__all(max(max(mx4(a0), mx4(a1)), max(mx4(b0), mx4(b1))) != SENT)) break;
        __builtin_amdgcn_s_sleep(PSLEEP);
      }
      float v[4];
#pragma unroll
      for (int k = 0; k < 4; ++k) v[k] = tr16(bf(a0[k]) + bf(b0[k]), bf(a1[k]) + bf(b1[k]));
      const float y0 = tr32(v[0], v[2]), y1 = tr32(v[1], v[3]);
      ((LAS unsigned*)(L + L_Q))[c16 * 4 + (r4 & 1) * 2 + (r4 >> 1)] = pack2(y0, y1);
      MEMBAR();
      lds_put(L, C_QREADY, 1u);
      ST_(4);
    }
    lds_wait(L, C_ROLE, 1u);
    const int role = __builtin_amdgcn_readfirstlane(((volatile LAS unsigned*)L)[C_ROLE]) - 1;
    __builtin_amdgcn_s_setprio(0);
    const int r16 = lane >> 2, c4 = lane & 3;
    const int col32 = c4 * 8 + ((lane >> 3) & 1) * 4 + ((lane >> 4) & 1) * 2 + (lane >> 5);
    const int col64 = c8 * 8 + ((lane >> 3) & 1) * 4 + ((lane >> 4) & 1) * 2 + (lane >> 5);
    LAS float* YR = (LAS float*)(L + L_YR);
    if (role >= R_M2 && role < R_M2 + N_M2) {
      const int m2 = role - R_M2;
      const int row0 = 160 * w + 20 * r8;
      unsigned sp[10];
      u32x2 cw2[5];
      u32x4 wt[20];
      TSTART(TA_M2);
#pragma unroll
      for (int i = 0; i < 5; ++i) cw2[i] = __builtin_nontemporal_load((const u32x2*)(cnd + row0 + 4 * i));
      const u16* wb = wmod + (size_t)row0 * (6 * D) + 1280 + m2 * 64 + c8 * 8;
#pragma unroll
      for (int i = 0; i < 20; ++i) wt[i] = ldnt(wb + (size_t)i * (6 * D));
      SB();
      ST_(5);
#pragma unroll
      for (int i = 0; i < 5; ++i) {
        sp[2 * i] = pack2(silu_f(bflo(cw2[i][0])), silu_f(bfhi(cw2[i][0])));
        sp[2 * i + 1] = pack2(silu_f(bflo(cw2[i][1])), silu_f(bfhi(cw2[i][1])));
      }
      float acc[8] = {0.f, 0.f, 0.f, 0.f, 0.f, 0.f, 0.f, 0.f};
#pragma unroll
      for (int m = 0; m < 10; ++m) mac_pair(acc, wt[2 * m], wt[2 * m + 1], sp[m]);
      const float y = red8_b345(acc, lane);
      ST_(6);
      YR[w * 64 + col64] = y;
      if (lds_arrive(C_T1) == 3u && lane < 16) {
        const f32x4 s = sum4(YR + 4 * lane, 64);
        st_sc1(rs, WS_M2 + (m2 * 64 + 4 * lane) * 4, nosent4(f32x4{rbf(s[0]), rbf(s[1]), rbf(s[2]), rbf(s[3])}));
      }
      ST_(7);
    } else if (role >= R_WO && role < R_WO + N_WO) {
      const int g = (role - R_WO) & 7, cgp = (role - R_WO) >> 3, hw = 4 * g + w;
      u32x4 wt[16];
      CODE_PREFETCH(mega_wo);
      lds_wait(L, C_PROG + w, IE);
      const u16* wb = wo + (size_t)(hw * 128 + 16 * r8) * D + cgp * 64 + c8 * 8;
#pragma unroll
      for (int i = 0; i < WOE; ++i) wt[i] = ldnt(wb + i * D);
      SB();
      TSTART(TA_WO);
#pragma unroll
      for (int i = WOE; i < 16; ++i) wt[i] = ldnt(wb + i * D);
      SB();
      ST_(5);
      u32x4 a[4]; u32x2 ml;
      const int off = WS_OP + (hw * 8 + 4 * (lane >> 5)) * 512 + (lane & 31) * 16;
      const int mo = WS_ST + (hw * 8 + c8) * 128;
      ml = ld_sc1_64(rs, mo);
      SB();
#pragma unroll
      for (int m = 0; m < 8; ++m) { prepack(wt[2 * m], wt[2 * m + 1]); pin(wt[2 * m]); pin(wt[2 * m + 1]); }
      float acc[8] = {0.f, 0.f, 0.f, 0.f, 0.f, 0.f, 0.f, 0.f};
      asm volatile("" : "+v"(acc[0]), "+v"(acc[1]), "+v"(acc[2]), "+v"(acc[3]), "+v"(acc[4]), "+v"(acc[5]), "+v"(acc[6]), "+v"(acc[7]));
      float wj[4];
      CODE_ALIGN(mega_wo, 11);
      NOUNROLL for (unsigned s = 0; s < SPIN_G; ++s) {
        MEMBAR();
#pragma unroll
        for (int i = 0; i < 4; ++i) a[i] = ld_sc1(rs, off + i * 512);
        const bool mok = __all(max(ml[0], ml[1]) != SENT);
        {
          float M = bf(ml[0]);
          M = fmaxf(M, dpp<XOR1>(M)); M = fmaxf(M, dpp<XOR2>(M)); M = fmaxf(M, dpp<XOR7>(M));
          const float e = __expf(bf(ml[0]) - M);
          float Ls = bf(ml[1]) * e;
          Ls += dpp<XOR1>(Ls); Ls += dpp<XOR2>(Ls); Ls += dpp<XOR7>(Ls);
          const float wgt = e * __builtin_amdgcn_rcpf(Ls);
#pragma unroll
          for (int i = 0; i < 4; ++i) {
            const float lo = bf(__builtin_amdgcn_readlane(fb(wgt), i)), hi = bf(__builtin_amdgcn_readlane(fb(wgt), 4 + i));
            wj[i] = lane < 32 ? lo : hi;
          }
          asm volatile("" : "+v"(wj[0]), "+v"(wj[1]), "+v"(wj[2]), "+v"(wj[3]));
        }
        if (!mok) ml = ld_sc1_64(rs, mo);
        if (mok && __all(max(max(mx4(a[0]), mx4(a[1])), max(mx4(a[2]), mx4(a[3]))) != SENT)) break;
      }
      ST_(6);
      float t4[4];
#pragma unroll
      for (int k = 0; k < 4; ++k) t4[k] = fmaf(wj[0], bf(a[0][k]), wj[1] * bf(a[1][k])) + fmaf(wj[2], bf(a[2][k]), wj[3] * bf(a[3][k]));
      LAS unsigned* OB = (LAS unsigned*)(L + L_OB) + w * 64;
      OB[2 * (lane & 31) + (lane >> 5)] = pack2(tr32(t4[0], t4[2]), tr32(t4[1], t4[3]));
      MEMBAR();
      const u32x4 oa = *(volatile LAS u32x4*)(OB + 8 * r8), ob = *(volatile LAS u32x4*)(OB + 8 * r8 + 4);
#pragma unroll
      for (int m = 0; m < 4; ++m) mac_packed(acc, wt[2 * m], wt[2 * m + 1], oa[m]);
#pragma unroll
      for (int m = 0; m < 4; ++m) mac_packed(acc, wt[8 + 2 * m], wt[8 + 2 * m + 1], ob[m]);
      const float y = red8_b345(acc, lane);
      YR[w * 64 + col64] = y;
      HBAR();
      if (w == 0 && lane < 16) st_sc1(rs, WS_YP + (g * 640 + cgp * 64 + 4 * lane) * 4, nosent4(sum4(YR + 4 * lane, 64)));
      ST_(7);
      CODE_END(mega_wo);
    } else if (role >= R_FFN && role < R_FFN + NF) {
      const int f = role - R_FFN;
      const int row0 = 160 * w + 10 * r16;
      const bool fin = f < 20 && w == 3;
      const int oc = (f < 20 ? 32 * f : 0) + 4 * c8 + ((lane >> 4) & 1) * 2 + (lane >> 5);
      LAS unsigned* X1 = (LAS unsigned*)(L + L_X1);
      u32x4 wt[18]; unsigned w2t[8];
      CODE_PREFETCH(mega_ffn);
      if (CPF && w == 2) {
        u64 pc_; int d_, len_;
        asm volatile("s_getpc_b64 %0\n.Lmega_gat_pc:\n\ts_mov_b32 %1, .Lmega_gat_beg-.Lmega_gat_pc\n\ts_mov_b32 %2, .Lmega_gat_end-.Lmega_gat_beg" : "=s"(pc_), "=s"(d_), "=s"(len_));
        const rsrc_t rc_ = mkrs((const void*)(pc_ + (long long)d_), min(max(len_, 0), 4096));
        const unsigned c_ = __builtin_amdgcn_raw_buffer_load_b32(rc_, lane * 64, 0, 0);
        asm volatile("" :: "v"(c_));
      }
      lds_wait(L, C_PROG + w, IE);
      const u16* wb = w1 + (size_t)row0 * DFF + f * 32 + c4 * 8;
      const u16* w2b = w2 + (size_t)(f * 32 + 8 * r4) * D + 160 * w;
#pragma unroll
      for (int i = 0; i < 18; ++i) {
        if (i == FFE) { SB(); TSTART(TA_FFN); }
        if (i < 10) wt[i] = ldnt(wb + (size_t)i * DFF);
        else {
          wt[i] = ldnt(w2b + (i - 10) * D + c16 * 8);
          w2t[i - 10] = __builtin_nontemporal_load((const unsigned*)(w2b + (i - 10) * D + 128 + c16 * 2));
        }
      }
      SB();
      lds_arrive_nr(C_WOUT);
      ST_(5);
      u32x2 sbA, sbB, cbA, cbB; unsigned gbw;
      NOUNROLL for (unsigned s = 0; s < SPIN_G; ++s) {
        MEMBAR();
        gbw = __builtin_amdgcn_raw_buffer_load_b32(rs, WS_M2 + 7680 + oc * 4, 0, 16);
        sbA = ld_sc1_64(rs, WS_M2 + 2560 + (row0 + 2 * c4) * 4); sbB = ld_sc1_64(rs, WS_M2 + 2560 + (row0 + 8) * 4);
        cbA = ld_sc1_64(rs, WS_M2 + 5120 + (row0 + 2 * c4) * 4); cbB = ld_sc1_64(rs, WS_M2 + 5120 + (row0 + 8) * 4);
        const unsigned mm = max(gbw, max(max(max(sbA[0], sbA[1]), max(sbB[0], sbB[1])), max(max(cbA[0], cbA[1]), max(cbB[0], cbB[1]))));
        if (__all(mm != SENT)) break;
        __builtin_amdgcn_s_sleep(MSL);
      }
      float c1[4] = {rbf(1.f + bf(cbA[0])), rbf(1.f + bf(cbA[1])), rbf(1.f + bf(cbB[0])), rbf(1.f + bf(cbB[1]))};
      asm volatile("" : "+v"(c1[0]), "+v"(c1[1]), "+v"(c1[2]), "+v"(c1[3]));
      const float s1[4] = {bf(sbA[0]), bf(sbA[1]), bf(sbB[0]), bf(sbB[1])};
#pragma unroll
      for (int m = 0; m < 9; ++m) { prepack(wt[2 * m], wt[2 * m + 1]); pin(wt[2 * m]); pin(wt[2 * m + 1]); }
#pragma unroll
      for (int m = 0; m < 4; ++m) {
        const unsigned lo = __builtin_amdgcn_perm(w2t[2 * m + 1], w2t[2 * m], 0x05040100u), hi = __builtin_amdgcn_perm(w2t[2 * m + 1], w2t[2 * m], 0x07060302u);
        w2t[2 * m] = lo; w2t[2 * m + 1] = hi;
        asm volatile("" : "+v"(w2t[2 * m]), "+v"(w2t[2 * m + 1]));
      }
      float acc[8] = {0.f, 0.f, 0.f, 0.f, 0.f, 0.f, 0.f, 0.f};
      asm volatile("" : "+v"(acc[0]), "+v"(acc[1]), "+v"(acc[2]), "+v"(acc[3]), "+v"(acc[4]), "+v"(acc[5]), "+v"(acc[6]), "+v"(acc[7]));
      float z[8] = {0.f, 0.f, 0.f, 0.f, 0.f, 0.f, 0.f, 0.f};
      float z8 = 0.f, z9 = 0.f;
      asm volatile("" : "+v"(z[0]), "+v"(z[1]), "+v"(z[2]), "+v"(z[3]), "+v"(z[4]), "+v"(z[5]), "+v"(z[6]), "+v"(z[7]), "+v"(z8), "+v"(z9));
      ST_(6);
      HBAR();
      unsigned hp5[5];
      {
        const u32x4 ya = *(LAS u32x4*)(X1 + 4 * lane);
        const unsigned yb = X1[256 + lane];
        const unsigned hA = X1[(row0 >> 1) + c4], hB = X1[(row0 >> 1) + 4];
        float sa_ = dot2(yb, 0x3F803F80u, 0.f), sq_ = dot2(yb, yb, 0.f);
#pragma unroll
        for (int k = 0; k < 4; ++k) { sa_ = dot2(ya[k], 0x3F803F80u, sa_); sq_ = dot2(ya[k], ya[k], sq_); }
        float r = tr32(sa_, sq_);
        r = tr16(r, r);
        r = row_sum(r);
        const float mean = bf(__builtin_amdgcn_readlane(fb(r), 0)) * (1.f / 640.f);
        float var = fmaf(-mean, mean, bf(__builtin_amdgcn_readlane(fb(r), 32)) * (1.f / 640.f));
        if (!(var * 1024.f > mean * mean)) {
          float q2 = 0.f;
#pragma unroll
          for (int k = 0; k < 4; ++k) { const float d0 = bflo(ya[k]) - mean, d1 = bfhi(ya[k]) - mean; q2 += d0 * d0 + d1 * d1; }
          const float d0 = bflo(yb) - mean, d1 = bfhi(yb) - mean; q2 += d0 * d0 + d1 * d1;
          var = wave_sum(q2) * (1.f / 640.f);
        }
        const float mr = rbf(mean);
        const float rden = __builtin_amdgcn_rcpf(rbf(__builtin_amdgcn_sqrtf(rbf(rbf(var) + 1e-5f))));
        const float xr2[4] = {bflo(hA), bfhi(hA), bflo(hB), bfhi(hB)};
        float hv[4];
#pragma unroll
        for (int k = 0; k < 4; ++k) hv[k] = rbf(rbf(rbf(xr2[k] - mr) * rden) * c1[k]) + s1[k];
        const unsigned pA = pack2(hv[0], hv[1]);
        hp5[4] = pack2(hv[2], hv[3]);
        hp5[0] = __builtin_amdgcn_mov_dpp((int)pA, 0x00, 0xf, 0xf, true);
        hp5[1] = __builtin_amdgcn_mov_dpp((int)pA, 0x55, 0xf, 0xf, true);
        hp5[2] = __builtin_amdgcn_mov_dpp((int)pA, 0xAA, 0xf, 0xf, true);
        hp5[3] = __builtin_amdgcn_mov_dpp((int)pA, 0xFF, 0xf, 0xf, true);
      }
      ST_(7);
      {
#pragma unroll
        for (int m = 0; m < 5; ++m) mac_packed(acc, wt[2 * m], wt[2 * m + 1], hp5[m]);
        float y = red8_b345(acc, lane);
        y += xor4(y);
        YR[w * 64 + col32] = y;
        HBAR();
        const LAS float* yp = YR + 8 * r4 + c8;
        float hd = (yp[0] + yp[64]) + (yp[128] + yp[192]);
        hd = hd < 0.f ? 0.f : hd;
        const unsigned hpk = pack2(hd, dpp<XOR1>(hd));
        const unsigned hp[4] = {(unsigned)__builtin_amdgcn_mov_dpp((int)hpk, 0x150, 0xf, 0xf, true), (unsigned)__builtin_amdgcn_mov_dpp((int)hpk, 0x152, 0xf, 0xf, true),
                                (unsigned)__builtin_amdgcn_mov_dpp((int)hpk, 0x154, 0xf, 0xf, true), (unsigned)__builtin_amdgcn_mov_dpp((int)hpk, 0x156, 0xf, 0xf, true)};
#pragma unroll
        for (int m = 0; m < 4; ++m) {
          mac_packed(z, wt[10 + 2 * m], wt[10 + 2 * m + 1], hp[m]);
          z8 = dot2(w2t[2 * m], hp[m], z8);
          z9 = dot2(w2t[2 * m + 1], hp[m], z9);
        }
        float v[4];
#pragma unroll
        for (int k = 0; k < 4; ++k) v[k] = tr16(z[k], z[k + 4]);
        float e = tr16(z8, z9);
        const int zo = WS_ZP + f * 2560 + w * 640;
        st_sc1_64(rs, zo + (c16 * 8 + (r4 & 1) * 4 + (r4 >> 1) * 2) * 4, u32x2{nosent(tr32(v[0], v[2])), nosent(tr32(v[1], v[3]))});
        e = tr32(e, e);
        __builtin_amdgcn_raw_buffer_store_b32(nosent(e), rs, zo + 512 + (c16 * 2 + (r4 & 1)) * 4, 0, 16);
      }
      ST_(8);
      if (fin) {
        u32x4 zp[7];
        const int zo = WS_ZP + r8 * 2560 + f * 128 + c8 * 16;
        const int z6 = r8 < NF - 48 ? zo + 48 * 2560 : 0x7FFFFF00;
        const float xo = bflo((unsigned)((LAS u16*)X1)[oc]);
        NOUNROLL for (unsigned s = 0; s < SPIN_G; ++s) {
          MEMBAR();
#pragma unroll
          for (int i = 0; i < 6; ++i) zp[i] = ld_sc1s(rs, zo, i * 8 * 2560);
          zp[6] = ld_sc1(rs, z6);
          unsigned mm = mx4(zp[0]);
#pragma unroll
          for (int i = 1; i < 7; ++i) mm = max(mm, mx4(zp[i]));
          if (__all(mm != SENT)) break;
        }
        ST_(9);
        float r[4];
#pragma unroll
        for (int k = 0; k < 4; ++k) r[k] = ((bf(zp[0][k]) + bf(zp[1][k])) + (bf(zp[2][k]) + bf(zp[3][k]))) + ((bf(zp[4][k]) + bf(zp[5][k])) + bf(zp[6][k]));
        float v = tr32(tr16(r[0], r[2]), tr16(r[1], r[3]));
        v += dpp<XOR8>(v);
        out[oc] = (u16)f2bf(xo + rbf(bf(gbw) * rbf(v)));
        if (f == 0 && lane == 0) {
          const u64 e1 = (((u64)Ev[1] << 32) | (u64)Ev[0]) + 1ull;
          st_sc1_64(rs, WS_EPOCH, u32x2{(unsigned)e1, (unsigned)(e1 >> 32)});
        }
        ST_(10);
        CODE_END(mega_ffn);
      }
    }
  }
}

extern "C" void kernel_launch(void* const* d_in, const int* in_sizes, int n_in, void* d_out, int out_size, void* d_ws,
                              size_t ws_size, hipStream_t stream) {
  (void)in_sizes; (void)n_in; (void)out_size; (void)ws_size;
  (void)hipFuncSetAttribute((const void*)mega, hipFuncAttributeMaxDynamicSharedMemorySize, LDS_BYTES);
  hipLaunchKernelGGL(mega, dim3(256), dim3(768), LDS_BYTES, stream, (const u16*)d_in[1], (const u16*)d_in[2], (unsigned char*)d_ws,
                     (const u16*)d_in[9], (const u16*)d_in[8], (const u16*)d_in[0], (const u16*)d_in[3], (const u16*)d_in[4],
                     (const u16*)d_in[6], (const u16*)d_in[7], (u16*)d_out);
}
#define STAMP_ROLE 1
#define STAMP_CLS(r) ((r) < N_M2 ? 3 : (r) < N_M2 + N_WO ? 2 : (r) < N_M2 + N_WO + NF ? 4 : 0)
#define NWAVES 12
#define NSTREAM 8
```

```cpp
#define DBGSEL 1
#define DBGTHR 1.6f
#include <hip/hip_runtime.h>
#include <hip/hip_bf16.h>
#include <stdint.h>

#define STAMP 0
#define PSLEEP 16
#define I1 12
#define I2 21
#define I3 17
#define IT 31
#define IS 26
#define ITK 22
#define PREB 1
#define IB 24
#define TA_M2 0
#define TA_WO 2
#define TA_FFN 2
#define TDEPF 40
#define TSTART(m) do { if ((m) == 2) lds_wait(L, C_PV, 9u); else if ((m) == 1) lds_wait8(L, IT); } while (0)
#define MP10 1
#define T1 380
#define T3 500
#define T2 680
#define TM2 950
#define DEPA 1
#define ISW 8
#define WQE 1
#define FAUX 16
#define MLAUX 16
#define CAUX 16
#define CPF 16
#define CPO 5120
#define R_WO 96
#define R_M2 0
#define R_FFN 40
#define MAP 0
#define CAL 0
#define WOE 12
#define FFE 5
#define SFIRST 0
#define KARG 1
#define EPF 0
#define IE 22
#define MSL 2
#define DEP 2
#define TDEP 40

#define GAS __attribute__((address_space(1)))
#define LAS __attribute__((address_space(3)))
#define RLX_AGENT __ATOMIC_RELAXED, __HIP_MEMORY_SCOPE_AGENT
typedef unsigned short u16;
typedef unsigned long long u64;
typedef unsigned u32x4 __attribute__((ext_vector_type(4)));
typedef unsigned u32x2 __attribute__((ext_vector_type(2)));
typedef float f32x4 __attribute__((ext_vector_type(4)));
typedef float f32x2 __attribute__((ext_vector_type(2)));
typedef __bf16 bf16x2 __attribute__((ext_vector_type(2)));
typedef GAS u64 gu64;
typedef __amdgpu_buffer_rsrc_t rsrc_t;

constexpr int D = 640, HQ = 32, SK = 16384, DH = 128, WIN = 4096, DFF = 1728;
constexpr int NF = 54;

constexpr int WS_EPOCH = 0;
constexpr int WS_FLAGS = 4096;
constexpr int WS_TICKET = 2048;
constexpr int NMP = 4;
constexpr int WS_MP = 8192;
constexpr int WS_QP = 131072;
constexpr int WS_OP = WS_QP + 131072;
constexpr int WS_ST = WS_OP + 131072;
constexpr int WS_YP = WS_ST + 32768;
constexpr int WS_M2 = WS_YP + 20480;
constexpr int WS_ZP = WS_M2 + 10240;
constexpr int WS_END = WS_ZP + NF * 2560;
constexpr int N_M2 = 40, N_WO = 80;
constexpr int WS_STAMP = 2 << 20;

constexpr unsigned SENT = 0xFFFFFFFFu;
constexpr unsigned SPIN_G = 1u << 18;
constexpr unsigned SPIN_L = 1u << 22;

constexpr int C_PROG = 0;
constexpr int C_MOD1 = 8, C_QP = 9, C_QREADY = 10, C_M = 11, C_ROLE = 12, C_PV = 13, C_FLAGOK = 14, C_T1 = 15, C_WOUT = 16;
constexpr int L_P = 128;
constexpr int L_MR = L_P + 1024;
constexpr int L_QR = L_MR + 1024;
constexpr int L_OR = L_QR + 2048;
constexpr int L_OB = L_OR + 4096;
constexpr int L_YR = L_OB + 1024;
constexpr int L_STAT = L_YR + 1024;
constexpr int L_Q = L_STAT + 64;
constexpr int L_X1 = L_Q + 256;
constexpr int L_ZR = L_X1 + 2560;
constexpr int L_END = L_ZR + 10240;
constexpr int LDS_BYTES = 96 * 1024;
static_assert(L_END <= LDS_BYTES, "lds");

__device__ __forceinline__ float bflo(unsigned w) { return __builtin_bit_cast(float, w << 16); }
__device__ __forceinline__ float bfhi(unsigned w) { return __builtin_bit_cast(float, w & 0xffff0000u); }
__device__ __forceinline__ unsigned f2bf(float f) { return (unsigned)__builtin_bit_cast(u16, (__bf16)f); }
__device__ __forceinline__ float rbf(float f) { return bflo(f2bf(f)); }
__device__ __forceinline__ unsigned pack2(float lo, float hi) {
  bf16x2 v = {(__bf16)lo, (__bf16)hi};
  return __builtin_bit_cast(unsigned, v);
}
__device__ __forceinline__ float dot2(unsigned a, unsigned b, float acc) {
  return __builtin_amdgcn_fdot2_f32_bf16(__builtin_bit_cast(bf16x2, a), __builtin_bit_cast(bf16x2, b), acc, false);
}
__device__ __forceinline__ unsigned fb(float f) { return __builtin_bit_cast(unsigned, f); }
__device__ __forceinline__ float bf(unsigned u) { return __builtin_bit_cast(float, u); }
__device__ __forceinline__ unsigned nosent(float f) { return min(fb(f), 0xFFFFFFFEu); }
__device__ __forceinline__ u32x4 nosent4(f32x4 s) { return u32x4{nosent(s[0]), nosent(s[1]), nosent(s[2]), nosent(s[3])}; }
template <int CTRL> __device__ __forceinline__ float dpp(float x) {
  return __builtin_bit_cast(float, __builtin_amdgcn_mov_dpp(__builtin_bit_cast(int, x), CTRL, 0xf, 0xf, true));
}
constexpr int XOR1 = 0xB1, XOR2 = 0x4E, XOR7 = 0x141, XOR8 = 0x128;
__device__ __forceinline__ float xor4(float x) {
  int v = __builtin_bit_cast(int, x);
  int r = __builtin_amdgcn_update_dpp(0, v, 0x114, 0xf, 0xa, false);
  r = __builtin_amdgcn_update_dpp(r, v, 0x104, 0xf, 0x5, false);
  return __builtin_bit_cast(float, r);
}
__device__ __forceinline__ float tr16(float a, float b) {
  auto s = __builtin_amdgcn_permlane16_swap(fb(a), fb(b), false, false);
  return bf(s[0]) + bf(s[1]);
}
__device__ __forceinline__ float tr32(float a, float b) {
  auto s = __builtin_amdgcn_permlane32_swap(fb(a), fb(b), false, false);
  return bf(s[0]) + bf(s[1]);
}
__device__ __forceinline__ float mx16(float a) {
  auto s = __builtin_amdgcn_permlane16_swap(fb(a), fb(a), false, false);
  return fmaxf(bf(s[0]), bf(s[1]));
}
__device__ __forceinline__ float mx32(float a) {
  auto s = __builtin_amdgcn_permlane32_swap(fb(a), fb(a), false, false);
  return fmaxf(bf(s[0]), bf(s[1]));
}
__device__ __forceinline__ float row_sum(float x) {
  x += dpp<XOR1>(x); x += dpp<XOR2>(x); x += dpp<XOR7>(x); x += dpp<XOR8>(x); return x;
}
__device__ __forceinline__ float row_max(float x) {
  x = fmaxf(x, dpp<XOR1>(x)); x = fmaxf(x, dpp<XOR2>(x)); x = fmaxf(x, dpp<XOR7>(x)); x = fmaxf(x, dpp<XOR8>(x)); return x;
}
__device__ __forceinline__ float wave_sum(float x) { x = row_sum(x); x = tr16(x, x); return tr32(x, x); }
__device__ __forceinline__ float wave_max(float x) { x = row_max(x); x = mx16(x); return mx32(x); }

__device__ __forceinline__ unsigned bij32(unsigned x) {
  x += 0x9E3779B9u; x ^= x >> 16; x *= 0x85EBCA6Bu; x ^= x >> 13; x *= 0xC2B2AE35u; x ^= x >> 16; return x;
}
__device__ __forceinline__ rsrc_t mkrs(const void* p, int bytes) { return __builtin_amdgcn_make_buffer_rsrc((void*)p, 0, bytes, 0x00020000); }
__device__ __forceinline__ u32x4 ldnt(const void* p) { return __builtin_nontemporal_load(reinterpret_cast<const u32x4*>(p)); }
__device__ __forceinline__ u32x4 ld_sc1(rsrc_t rs, int off) { return __builtin_amdgcn_raw_buffer_load_b128(rs, off, 0, 16); }
__device__ __forceinline__ u32x4 ld_sc1s(rsrc_t rs, int voff, int soff) { return __builtin_amdgcn_raw_buffer_load_b128(rs, voff, soff, 16); }
__device__ __forceinline__ u32x2 ld_sc1_64(rsrc_t rs, int off) { return __builtin_amdgcn_raw_buffer_load_b64(rs, off, 0, 16); }
__device__ __forceinline__ void st_sc1(rsrc_t rs, int off, u32x4 v) { __builtin_amdgcn_raw_buffer_store_b128(v, rs, off, 0, 16); }
__device__ __forceinline__ void st_sc1_64(rsrc_t rs, int off, u32x2 v) { __builtin_amdgcn_raw_buffer_store_b64(v, rs, off, 0, 16); }
__device__ __forceinline__ u32x4 ld_f(rsrc_t rs, int off) { return __builtin_amdgcn_raw_buffer_load_b128(rs, off, 0, FAUX); }
__device__ __forceinline__ u32x2 ld_f64(rsrc_t rs, int off) { return __builtin_amdgcn_raw_buffer_load_b64(rs, off, 0, FAUX); }
__device__ __forceinline__ void st_f(rsrc_t rs, int off, u32x4 v) { __builtin_amdgcn_raw_buffer_store_b128(v, rs, off, 0, FAUX); }
__device__ __forceinline__ void st_f64(rsrc_t rs, int off, u32x2 v) { __builtin_amdgcn_raw_buffer_store_b64(v, rs, off, 0, FAUX); }
__device__ __forceinline__ void st_c(rsrc_t rs, int off, u32x4 v) { __builtin_amdgcn_raw_buffer_store_b128(v, rs, off, 0, CAUX); }
__device__ __forceinline__ unsigned mx4(u32x4 v) { return max(max(v[0], v[1]), max(v[2], v[3])); }
#define MEMBAR() asm volatile("" ::: "memory")
#define LGKM0() asm volatile("s_waitcnt lgkmcnt(0)" ::: "memory")
#define SB() __builtin_amdgcn_sched_barrier(0)
#define NOUNROLL _Pragma("nounroll")

__device__ __forceinline__ void lds_put(LAS unsigned char* L, int word, unsigned v) { ((volatile LAS unsigned*)L)[word] = v; }
__device__ __forceinline__ void lds_wait(LAS unsigned char* L, int word, unsigned v) {
  NOUNROLL for (unsigned s = 0; s < SPIN_L; ++s) {
    if (((volatile LAS unsigned*)L)[word] >= v) break;
    __builtin_amdgcn_s_sleep(1);
  }
  MEMBAR();
}
__device__ __forceinline__ void lds_wait4(LAS unsigned char* L, int word, unsigned v) {
  NOUNROLL for (unsigned s = 0; s < SPIN_L; ++s) {
    const u32x4 q = *(volatile LAS u32x4*)(L + word * 4);
    if (min(min(q[0], q[1]), min(q[2], q[3])) >= v) break;
    __builtin_amdgcn_s_sleep(1);
  }
  MEMBAR();
}
__device__ __forceinline__ void lds_spin(LAS unsigned char* L, int word, unsigned v) {
  NOUNROLL for (unsigned s = 0; s < SPIN_L; ++s) {
    if (((volatile LAS unsigned*)L)[word] >= v) break;
  }
  MEMBAR();
}
__device__ __forceinline__ unsigned lds_arrive(int word) {
  unsigned old;
  asm volatile("s_mov_b64 exec, 1\n\tds_add_rtn_u32 %0, %1, %2\n\ts_mov_b64 exec, -1\n\ts_waitcnt lgkmcnt(0)"
               : "=&v"(old) : "v"(word * 4), "v"(1u) : "memory");
  return __builtin_amdgcn_readfirstlane(old);
}
__device__ __forceinline__ void lds_arrive_nr(int word) {
  asm volatile("s_mov_b64 exec, 1\n\tds_add_u32 %0, %1\n\ts_mov_b64 exec, -1" :: "v"(word * 4), "v"(1u) : "memory");
}
__device__ __forceinline__ void lds_wait8(LAS unsigned char* L, unsigned v) {
  NOUNROLL for (unsigned s = 0; s < SPIN_L; ++s) {
    const u32x4 q = *(volatile LAS u32x4*)L, r = *(volatile LAS u32x4*)(L + 16);
    if (min(min(min(q[0], q[1]), min(q[2], q[3])), min(min(r[0], r[1]), min(r[2], r[3]))) >= v) break;
    __builtin_amdgcn_s_sleep(2);
  }
  MEMBAR();
}
#define HBAR() do { LGKM0(); __builtin_amdgcn_s_barrier(); MEMBAR(); } while (0)
__device__ __forceinline__ void wait_until(u64 t) {
  NOUNROLL for (unsigned s = 0; s < 4096u; ++s) {
    if (__builtin_amdgcn_s_memrealtime() >= t) break;
    __builtin_amdgcn_s_sleep(2);
  }
  MEMBAR();
}
template <int N> __device__ __forceinline__ void vmwait() { asm volatile("s_waitcnt vmcnt(%0)" :: "i"(N) : "memory"); }

#define ST_(k) do {} while (0)
#define CNT_(sh) do {} while (0)

__device__ __forceinline__ float red8_b345(const float (&a)[8], int lane) {
  float v[4];
  const bool hi = (lane & 8) != 0;
#pragma unroll
  for (int k = 0; k < 4; ++k) {
    const float t1 = a[k] + dpp<XOR8>(a[k]), t2 = a[k + 4] + dpp<XOR8>(a[k + 4]);
    v[k] = hi ? t2 : t1;
  }
  const float u0 = tr16(v[0], v[2]), u1 = tr16(v[1], v[3]);
  return tr32(u0, u1);
}
__device__ __forceinline__ void prepack(u32x4& wa, u32x4& wb) {
#pragma unroll
  for (int d = 0; d < 4; ++d) {
    const unsigned lo = __builtin_amdgcn_perm(wb[d], wa[d], 0x05040100u);
    const unsigned hi = __builtin_amdgcn_perm(wb[d], wa[d], 0x07060302u);
    wa[d] = lo; wb[d] = hi;
  }
}
__device__ __forceinline__ void pin(u32x4& a) { asm volatile("" : "+v"(a)); }
__device__ __forceinline__ void mac_packed(float (&acc)[8], u32x4 wa, u32x4 wb, unsigned xp) {
#pragma unroll
  for (int d = 0; d < 4; ++d) { acc[2 * d] = dot2(wa[d], xp, acc[2 * d]); acc[2 * d + 1] = dot2(wb[d], xp, acc[2 * d + 1]); }
}
__device__ __forceinline__ void mac_pair(float (&acc)[8], u32x4 wa, u32x4 wb, unsigned xp) { prepack(wa, wb); mac_packed(acc, wa, wb, xp); }
__device__ __forceinline__ float silu_f(float v) { return v * __builtin_amdgcn_rcpf(1.f + __expf(-v)); }
__device__ __forceinline__ f32x4 sum4(const LAS float* p, int stride) {
  return (*(const LAS f32x4*)p + *(const LAS f32x4*)(p + stride)) + (*(const LAS f32x4*)(p + 2 * stride) + *(const LAS f32x4*)(p + 3 * stride));
}

#define CODE_PREFETCH(NAME) do { if (CPF && w == 3) { \
      u64 pc_; int len_; \
      asm volatile("s_getpc_b64 %0\n.L" #NAME "_beg:\n\ts_mov_b32 %1, .L" #NAME "_end-.L" #NAME "_beg" : "=s"(pc_), "=s"(len_)); \
      len_ = min(max(len_, 0), 16384); \
      const rsrc_t rc_ = mkrs((const void*)pc_, len_); \
      const unsigned c_ = __builtin_amdgcn_raw_buffer_load_b32(rc_, max(len_ - 4096, 0) + lane * 64, 0, 0); \
      const unsigned d_ = __builtin_amdgcn_raw_buffer_load_b32(rc_, lane * 64, 0, 0); \
      asm volatile("" :: "v"(c_), "v"(d_)); } } while (0)
#define CODE_ALIGN(NAME, P) do { if (CAL) asm volatile("s_branch .L" #NAME "_al\n\t.p2align " #P "\n.L" #NAME "_al:"); } while (0)
#define CODE_END(NAME) asm volatile(".L" #NAME "_end:")

__global__ void __launch_bounds__(768, 3)
mega(const u16* __restrict__ kc, const u16* __restrict__ vc, unsigned char* ws, const u16* __restrict__ wmod,
     const u16* __restrict__ cnd, const u16* __restrict__ x, const u16* __restrict__ wq, const u16* __restrict__ wo,
     const u16* __restrict__ w1, const u16* __restrict__ w2, u16* __restrict__ out) {
  asm volatile("" :: "s"(kc), "s"(vc), "s"(ws), "s"(wmod), "s"(cnd), "s"(x), "s"(wq), "s"(wo));
  extern __shared__ __attribute__((aligned(16))) unsigned char smem[];
  LAS unsigned char* L = (LAS unsigned char*)smem;
  const int tid = threadIdx.x, lane = tid & 63, wave = __builtin_amdgcn_readfirstlane(tid >> 6);
  const int b = blockIdx.x;
  const int h = MAP ? b >> 3 : (b & 7) * 4 + (b >> 6), j = MAP ? b & 7 : (b >> 3) & 7, u = h * 8 + j;
  const int shard = (b >> 3) & 7;
  const int r4 = lane >> 4, c16 = lane & 15;
  const rsrc_t rs = mkrs(ws, WS_END);
  ST_(0);
  if (EPF && b >= 256 - EPF && wave == 7) {
    u64 pc_; int l0_, l1_, l2_;
    asm volatile("s_getpc_b64 %0\n.Lmega_pc0:\n\ts_mov_b32 %1, .Lmega_ffn_end-.Lmega_pc0\n\ts_mov_b32 %2, .Lmega_wo_end-.Lmega_pc0\n\ts_mov_b32 %3, .Lmega_s_end-.Lmega_pc0"
                 : "=s"(pc_), "=s"(l0_), "=s"(l1_), "=s"(l2_));
    const rsrc_t rc_ = mkrs((const void*)pc_, min(max(max(l0_, l1_), max(l2_, 0)), 24576));
    unsigned c_[6];
#pragma unroll
    for (int i = 0; i < 6; ++i) c_[i] = __builtin_amdgcn_raw_buffer_load_b32(rc_, lane * 64, i * 4096, 0);
    asm volatile("" :: "v"(c_[0]), "v"(c_[1]), "v"(c_[2]), "v"(c_[3]), "v"(c_[4]), "v"(c_[5]));
  }

  if (__builtin_expect(wave < 8, SFIRST)) {
    const size_t kvoff = ((size_t)h * SK + (SK - WIN) + j * 512) * DH;
    const rsrc_t rk = mkrs(kc + kvoff, 131072), rv = mkrs(vc + kvoff, 131072);
    const int rot = (b * 8 + (b >> 4) + wave) * 1024;
    u32x4 big[32];
    u32x4 pp0, pp1;
    float mw;
    LAS float* STAT = (LAS float*)(L + L_STAT);
#define LOAD_STEP(i) do { \
      big[i] = __builtin_amdgcn_raw_buffer_load_b128((i) < 16 ? rk : rv, lane * 16, (((i) & 15) * 8192 + rot) & 131071, 2); \
      if ((i) == 0) ST_(7); \
      if ((i) == 2) ST_(8); \
      if ((i) == PREB - 1) __builtin_amdgcn_s_barrier();     \
      if ((i) == ITK || (i) == IT || (i) == IE) lds_put(L, C_PROG + wave, (unsigned)(i)); \
      if ((i) >= 16 + DEP + 2 && (((i) - DEP) & 1) == 0 && (i) <= IB) { prepack(big[(i) - DEP - 2], big[(i) - DEP - 1]); pin(big[(i) - DEP - 2]); pin(big[(i) - DEP - 1]); } \
      if ((i) < ISW) { if ((i) >= DEPA) vmwait<DEPA>(); } else if ((i) < IB && (i) != IS) vmwait<DEP>(); \
      SB(); } while (0)
#pragma unroll
    for (int i = 0; i <= IS; ++i) LOAD_STEP(i);
    {
      ST_(2); lds_wait(L, C_QREADY, 1u); ST_(3);
      const u32x4 qp = *(LAS u32x4*)(L + L_Q + c16 * 16);
      float xs[16];
#pragma unroll
      for (int ii = 0; ii < 16; ++ii) {
        float d = 0.f;
#pragma unroll
        for (int k = 0; k < 4; ++k) d = dot2(big[ii][k], qp[k], d);
        xs[ii] = d;
      }
#define TR_STEP(HALF, CTRL, BIT) _Pragma("unroll") for (int q = 0; q < (HALF); ++q) { \
      const float lo_ = xs[q], hi_ = xs[q + (HALF)]; const float t1_ = lo_ + dpp<CTRL>(lo_), t2_ = hi_ + dpp<CTRL>(hi_); \
      xs[q] = (c16 & (BIT)) ? t2_ : t1_; }
      TR_STEP(8, XOR8, 8) TR_STEP(4, XOR7, 4) TR_STEP(2, XOR2, 2) TR_STEP(1, XOR1, 1)
#undef TR_STEP
      const float sc = rbf(rbf(xs[0]) * 0.08838834764831845f);
      mw = wave_max(sc);
      const float e = __expf(sc - mw);
      LAS u16* P = (LAS u16*)(L + L_P) + wave * 64;
      P[lane] = (u16)f2bf(e);
      MEMBAR();
      pp0 = *(volatile LAS u32x4*)(P + r4 * 16); pp1 = *(volatile LAS u32x4*)(P + r4 * 16 + 8);
      STAT[wave] = mw;
      STAT[8 + wave] = wave_sum(e);
      if (lds_arrive(C_M) == 7u) {
        const float m8 = STAT[lane & 7], l8 = STAT[8 + (lane & 7)];
        float mc = fmaxf(m8, dpp<XOR1>(m8)); mc = fmaxf(mc, dpp<XOR2>(mc)); mc = fmaxf(mc, dpp<XOR7>(mc));
        float lc = l8 * __expf(m8 - mc);
        lc += dpp<XOR1>(lc); lc += dpp<XOR2>(lc); lc += dpp<XOR7>(lc);
        if (lane == 0) __builtin_amdgcn_raw_buffer_store_b64(u32x2{nosent(mc), nosent(lc)}, rs, WS_ST + u * 128, 0, MLAUX);
      }
          ST_(4);
      }
#pragma unroll
    for (int i = IS + 1; i < 32; ++i) LOAD_STEP(i);
    ST_(1);
    __builtin_amdgcn_s_setprio(2);
    {
      float acc[8] = {0.f, 0.f, 0.f, 0.f, 0.f, 0.f, 0.f, 0.f};
      constexpr int NPRE = ((IB < 30 ? IB : 30) - 16 - DEP) / 2;
      float scl;
#pragma unroll
      for (int m = 0; m < 8; ++m) {
        if (m == NPRE) {
          lds_spin(L, C_M, 8u);
          const f32x4 ma = *(LAS f32x4*)STAT, mb = *(LAS f32x4*)(STAT + 4);
          const float mc = fmaxf(fmaxf(fmaxf(ma[0], ma[1]), fmaxf(ma[2], ma[3])), fmaxf(fmaxf(mb[0], mb[1]), fmaxf(mb[2], mb[3])));
          scl = __expf(mw - mc);
          asm volatile("" : "+v"(scl));
        }
        if (m >= NPRE) prepack(big[16 + 2 * m], big[16 + 2 * m + 1]);
        mac_packed(acc, big[16 + 2 * m], big[16 + 2 * m + 1], m < 4 ? pp0[m & 3] : pp1[m & 3]);
        SB();
      }
      ST_(5);
      float v[4];
#pragma unroll
      for (int k = 0; k < 4; ++k) v[k] = tr16(acc[k], acc[k + 4]);
      const float y0 = tr32(v[0], v[2]) * scl, y1 = tr32(v[1], v[3]) * scl;
      LAS float* OR = (LAS float*)(L + L_OR);
      *(LAS f32x2*)(OR + wave * 128 + c16 * 8 + (r4 & 1) * 4 + (r4 >> 1) * 2) = f32x2{y0, y1};
      if (lds_arrive(C_PV) == 7u) {
        const f32x4 s = sum4(OR + (lane >> 5) * 512 + 4 * (lane & 31), 128);
        f32x4 t;
#pragma unroll
        for (int k = 0; k < 4; ++k) t[k] = tr32(s[k], s[k]);
        if (lane < 32) st_sc1(rs, WS_OP + u * 512 + lane * 16, nosent4(t));
        MEMBAR();
        lds_put(L, C_PV, 9u);
      }
      ST_(6);
    }
    if (wave < 5) {
      lds_wait(L, C_ROLE, 1u);
      const int role = __builtin_amdgcn_readfirstlane(((volatile LAS unsigned*)L)[C_ROLE]) - 1;
      if (role >= R_FFN && role < R_FFN + NF) {
        asm volatile(".Lmega_gat_beg:");
        const int t = wave * 64 + lane, hf = lane & 1;
        lds_wait(L, C_WOUT, 4u);
        const unsigned xq = __builtin_nontemporal_load((const unsigned*)x + t);
        u32x2 g0;
        NOUNROLL for (unsigned s = 0; s < SPIN_G; ++s) {
          MEMBAR();
          g0 = ld_sc1_64(rs, WS_M2 + t * 8);
          if (__all(max(g0[0], g0[1]) != SENT)) break;
          __builtin_amdgcn_s_sleep(MSL);
        }
        float x0 = bflo(xq), x1 = bfhi(xq);
        asm volatile("" : "+v"(x0), "+v"(x1));
        u32x4 a[4];
        const int off = hf * 4 * 2560 + (t >> 1) * 16;
        NOUNROLL for (unsigned s = 0; s < SPIN_G; ++s) {
          MEMBAR();
#pragma unroll
          for (int i = 0; i < 4; ++i) a[i] = ld_sc1s(rs, off, WS_YP + i * 2560);
          if (__all(max(max(mx4(a[0]), mx4(a[1])), max(mx4(a[2]), mx4(a[3]))) != SENT)) break;
        }
        ST_(9);
        float sm[4];
#pragma unroll
        for (int k = 0; k < 4; ++k) sm[k] = (bf(a[0][k]) + bf(a[1][k])) + (bf(a[2][k]) + bf(a[3][k]));
        const float at0 = (hf ? sm[2] : sm[0]) + dpp<XOR1>(hf ? sm[0] : sm[2]), at1 = (hf ? sm[3] : sm[1]) + dpp<XOR1>(hf ? sm[1] : sm[3]);
        ((LAS unsigned*)(L + L_X1))[t] = pack2(x0 + rbf(bf(g0[0]) * rbf(at0)), x1 + rbf(bf(g0[1]) * rbf(at1)));
        HBAR();
        ST_(10);
        asm volatile(".Lmega_gat_end:");
      }
    }
    CODE_END(mega_s);
  } else {
    const int w = wave - 8;
    int zero_v;
    asm volatile("v_mov_b32 %0, 0" : "=v"(zero_v));
    const int r8 = lane >> 3, c8 = lane & 7;
    __builtin_amdgcn_s_setprio(3);
    const u64 tstart = __builtin_amdgcn_s_memrealtime();
    const bool m1 = b < 80;
    const int cg = b % 20, rg = b / 20;
    u32x4 wm[5]; unsigned cw0, cw1, cw2;
#define MOD_TILE_LOAD() do {  \
      const int row0 = 160 * rg + 40 * w; \
      const u16* wb = wmod + (size_t)row0 * (6 * D) + cg * 64 + c8 * 8; \
      wm[0] = ldnt(wb + (size_t)(2 * r8) * (6 * D)); wm[1] = ldnt(wb + (size_t)(2 * r8 + 1) * (6 * D)); \
      wm[2] = ldnt(wb + (size_t)(16 + 2 * r8) * (6 * D)); wm[3] = ldnt(wb + (size_t)(17 + 2 * r8) * (6 * D)); \
      wm[4] = ldnt(wb + (size_t)(32 + r8) * (6 * D)); \
      cw0 = __builtin_nontemporal_load((const unsigned*)(cnd + row0 + 2 * r8)); \
      cw1 = __builtin_nontemporal_load((const unsigned*)(cnd + row0 + 16 + 2 * r8)); \
      cw2 = __builtin_nontemporal_load(cnd + row0 + 32 + r8); \
     } while (0)
    if (m1) MOD_TILE_LOAD();
    const u32x2 Ev = ld_sc1_64(rs, WS_EPOCH + zero_v);
    const int l20 = (lane & 31) < 20 ? (lane & 31) : 19;
    const int hrow = 80 * j + 20 * w + l20;
    const u32x4 xa = ldnt(x + lane * 8);
    const unsigned xb = __builtin_nontemporal_load((const unsigned*)x + 256 + lane);
    const float xr = bflo((unsigned)__builtin_nontemporal_load(x + hrow));
    if (w == 3) {
      if (lane < 32) lds_put(L, lane, 0u);
    }
    SB();
    LGKM0();
    __builtin_amdgcn_s_barrier();
    SB();
    u32x4 wqv[5];
    if (WQE) {
#pragma unroll
      for (int i = 0; i < 5; ++i) wqv[i] = ldnt(wq + (size_t)(80 * j + 20 * w + 5 * r4 + i) * (HQ * DH) + h * DH + c16 * 8);
      SB();
    }
    const unsigned tag = bij32(Ev[0]), flo = bij32(Ev[0] ^ Ev[1] ^ 0x5bd1e995u);
#define MOD_TILE_COMPUTE() do {  \
      float acc[8] = {0.f, 0.f, 0.f, 0.f, 0.f, 0.f, 0.f, 0.f}; \
      mac_pair(acc, wm[0], wm[1], pack2(silu_f(bflo(cw0)), silu_f(bfhi(cw0)))); \
      mac_pair(acc, wm[2], wm[3], pack2(silu_f(bflo(cw1)), silu_f(bfhi(cw1)))); \
      { \
        const unsigned sl = f2bf(silu_f(bflo(cw2))), sh = sl << 16; \
_Pragma("unroll")         for (int d = 0; d < 4; ++d) { acc[2 * d] = dot2(wm[4][d], sl, acc[2 * d]); acc[2 * d + 1] = dot2(wm[4][d], sh, acc[2 * d + 1]); } \
      } \
      const float y = red8_b345(acc, lane); \
      const int col = c8 * 8 + ((lane >> 3) & 1) * 4 + ((lane >> 4) & 1) * 2 + (lane >> 5); \
      LAS float* MR = (LAS float*)(L + L_MR); \
      MR[w * 64 + col] = y; \
      if (lds_arrive(C_MOD1) == 3u) { \
        const float s = (MR[lane] + MR[64 + lane]) + (MR[128 + lane] + MR[192 + lane]); \
        st_f64(rs, WS_MP + (rg * 1280 + cg * 64 + lane) * 8, u32x2{fb(s), tag}); \
      } \
     } while (0)
    if (m1) MOD_TILE_COMPUTE();
    ST_(1);
    if (w == 3) {
      u32x4 sv = {SENT, SENT, SENT, SENT};
      st_c(rs, (lane < 32 ? WS_QP : WS_OP) + u * 512 + (lane & 31) * 16, sv);
      if (b >= 128 && b < 128 + NF) {
        st_c(rs, WS_ZP + (b - 128) * 2560 + lane * 16, sv);
        st_c(rs, WS_ZP + (b - 128) * 2560 + 1024 + lane * 16, sv);
      }
      int o2 = 0x7FFFFF00;
      if (lane == 63) o2 = WS_ST + u * 128;
      else if (b < 120) { if (lane < 16) o2 = WS_YP + b * 256 + lane * 16; }
      else if (b >= 128 && b < 128 + NF) { if (lane < 32) o2 = WS_ZP + (b - 128) * 2560 + 2048 + lane * 16; }
      else if (b == 255 && lane < 8) { o2 = WS_TICKET + lane * 128; sv = u32x4{0u, 0u, 0u, 0u}; }
      st_c(rs, o2, sv);
          vmwait<0>();
      if (lane == 0) { __hip_atomic_store((gu64*)(ws + (3 << 20)) + b, tstart, RLX_AGENT); vmwait<0>(); st_f64(rs, WS_FLAGS + b * 8, u32x2{flo, tag}); }
    }
    {
      if (!WQE) {
#pragma unroll
        for (int i = 0; i < 5; ++i) wqv[i] = ldnt(wq + (size_t)(80 * j + 20 * w + 5 * r4 + i) * (HQ * DH) + h * DH + c16 * 8);
      }
      SB();
      float nrm;
      {
        float s = bflo(xb) + bfhi(xb);
#pragma unroll
        for (int i = 0; i < 4; ++i) s += bflo(xa[i]) + bfhi(xa[i]);
        const float mean = wave_sum(s) * (1.f / 640.f);
        float q2 = (bflo(xb) - mean) * (bflo(xb) - mean) + (bfhi(xb) - mean) * (bfhi(xb) - mean);
#pragma unroll
        for (int i = 0; i < 4; ++i) { const float d0 = bflo(xa[i]) - mean, d1 = bfhi(xa[i]) - mean; q2 += d0 * d0 + d1 * d1; }
        const float var = wave_sum(q2) * (1.f / 640.f);
        const float den = rbf(sqrtf(rbf(rbf(var) + 1e-5f)));
        nrm = rbf(rbf(xr - rbf(mean)) / den);
      }
      const int mp = WS_MP + ((lane >> 5) * 640 + hrow) * 8;
      u32x2 g[NMP];
      wait_until(tstart + T1);
      NOUNROLL for (unsigned s = 0; s < SPIN_G; ++s) {
        bool ok = true;
        MEMBAR();
#pragma unroll
        for (int i = 0; i < NMP; ++i) g[i] = ld_f64(rs, mp + i * 1280 * 8);
#pragma unroll
        for (int i = 0; i < NMP; ++i) ok = ok & (g[i][1] == tag);
        CNT_(0);
        if (__all(ok)) break;
        __builtin_amdgcn_s_sleep(PSLEEP);
      }
      const float mv = rbf((bf(g[0][0]) + bf(g[1][0])) + (bf(g[2][0]) + bf(g[3][0])));
      auto sw = __builtin_amdgcn_permlane32_swap(fb(mv), fb(mv), false, false);
      const float hval = rbf(rbf(nrm * rbf(1.f + bf(sw[1]))) + bf(sw[0]));
      ST_(2);
      float acc[8] = {0.f, 0.f, 0.f, 0.f, 0.f, 0.f, 0.f, 0.f};
#pragma unroll
      for (int i = 0; i < 5; ++i) {
        const float hk = __shfl(hval, 5 * r4 + i);
#pragma unroll
        for (int d = 0; d < 4; ++d) { acc[2 * d] = fmaf(hk, bflo(wqv[i][d]), acc[2 * d]); acc[2 * d + 1] = fmaf(hk, bfhi(wqv[i][d]), acc[2 * d + 1]); }
      }
      float v[4];
#pragma unroll
      for (int k = 0; k < 4; ++k) v[k] = tr16(acc[k], acc[k + 4]);
      const float y0 = tr32(v[0], v[2]), y1 = tr32(v[1], v[3]);
      LAS float* QR = (LAS float*)(L + L_QR);
      *(LAS f32x2*)(QR + w * 128 + c16 * 8 + (r4 & 1) * 4 + (r4 >> 1) * 2) = f32x2{y0, y1};
      if (lds_arrive(C_QP) == 3u) {
        if (lane < 32) st_f(rs, WS_QP + u * 512 + lane * 16, nosent4(sum4(QR + 4 * lane, 128)));
      }
    }
    ST_(3);
    if (w == 1) {
      wait_until(tstart + T3);
      NOUNROLL for (unsigned s = 0; s < SPIN_G; ++s) {
        MEMBAR();
        const u32x4 g0 = ld_f(rs, WS_FLAGS + lane * 16), g1 = ld_f(rs, WS_FLAGS + 1024 + lane * 16);
        const unsigned bad = ((g0[0] ^ flo) | (g0[1] ^ tag)) | ((g0[2] ^ flo) | (g0[3] ^ tag)) | ((g1[0] ^ flo) | (g1[1] ^ tag)) | ((g1[2] ^ flo) | (g1[3] ^ tag));
        if (__all(bad == 0u)) break;
        __builtin_amdgcn_s_sleep(PSLEEP);
      }
      lds_put(L, C_FLAGOK, 1u);
      lds_wait(L, C_PROG, ITK);
      if (lane == 0) {
        const unsigned tk = __hip_atomic_fetch_add((GAS unsigned*)(ws + WS_TICKET + shard * 128), 1u, RLX_AGENT);
        lds_put(L, C_ROLE, tk * 8u + (unsigned)shard + 1u);
      }
    } else if (w == 0) {
      u32x4 a0, a1, b0, b1;
      const int off = WS_QP + (h * 8 + 2 * r4) * 512 + c16 * 32;
      lds_wait(L, C_FLAGOK, 1u);
      wait_until(tstart + T2);
      NOUNROLL for (unsigned s = 0; s < SPIN_G; ++s) {
        MEMBAR();
        a0 = ld_f(rs, off); a1 = ld_f(rs, off + 16); b0 = ld_f(rs, off + 512); b1 = ld_f(rs, off + 528);
        CNT_(16);
        if (__all(max(max(mx4(a0), mx4(a1)), max(mx4(b0), mx4(b1))) != SENT)) break;
        __builtin_amdgcn_s_sleep(PSLEEP);
      }
      float v[4];
#pragma unroll
      for (int k = 0; k < 4; ++k) v[k] = tr16(bf(a0[k]) + bf(b0[k]), bf(a1[k]) + bf(b1[k]));
      const float y0 = tr32(v[0], v[2]), y1 = tr32(v[1], v[3]);
      ((LAS unsigned*)(L + L_Q))[c16 * 4 + (r4 & 1) * 2 + (r4 >> 1)] = pack2(y0, y1);
      MEMBAR();
      lds_put(L, C_QREADY, 1u);
      ST_(4);
    }
    lds_wait(L, C_ROLE, 1u);
    const int role = __builtin_amdgcn_readfirstlane(((volatile LAS unsigned*)L)[C_ROLE]) - 1;
    __builtin_amdgcn_s_setprio(0);
    const int r16 = lane >> 2, c4 = lane & 3;
    const int col32 = c4 * 8 + ((lane >> 3) & 1) * 4 + ((lane >> 4) & 1) * 2 + (lane >> 5);
    const int col64 = c8 * 8 + ((lane >> 3) & 1) * 4 + ((lane >> 4) & 1) * 2 + (lane >> 5);
    LAS float* YR = (LAS float*)(L + L_YR);
    if (role >= R_M2 && role < R_M2 + N_M2) {
      const int m2 = role - R_M2;
      const int row0 = 160 * w + 20 * r8;
      unsigned sp[10];
      u32x2 cw2[5];
      u32x4 wt[20];
      TSTART(TA_M2);
#pragma unroll
      for (int i = 0; i < 5; ++i) cw2[i] = __builtin_nontemporal_load((const u32x2*)(cnd + row0 + 4 * i));
      const u16* wb = wmod + (size_t)row0 * (6 * D) + 1280 + m2 * 64 + c8 * 8;
#pragma unroll
      for (int i = 0; i < 20; ++i) wt[i] = ldnt(wb + (size_t)i * (6 * D));
      SB();
      ST_(5);
#pragma unroll
      for (int i = 0; i < 5; ++i) {
        sp[2 * i] = pack2(silu_f(bflo(cw2[i][0])), silu_f(bfhi(cw2[i][0])));
        sp[2 * i + 1] = pack2(silu_f(bflo(cw2[i][1])), silu_f(bfhi(cw2[i][1])));
      }
      float acc[8] = {0.f, 0.f, 0.f, 0.f, 0.f, 0.f, 0.f, 0.f};
#pragma unroll
      for (int m = 0; m < 10; ++m) mac_pair(acc, wt[2 * m], wt[2 * m + 1], sp[m]);
      const float y = red8_b345(acc, lane);
      ST_(6);
      YR[w * 64 + col64] = y;
      if (lds_arrive(C_T1) == 3u && lane < 16) {
        const f32x4 s = sum4(YR + 4 * lane, 64);
        st_sc1(rs, WS_M2 + (m2 * 64 + 4 * lane) * 4, nosent4(f32x4{rbf(s[0]), rbf(s[1]), rbf(s[2]), rbf(s[3])}));
      }
      ST_(7);
    } else if (role >= R_WO && role < R_WO + N_WO) {
      const int g = (role - R_WO) & 7, cgp = (role - R_WO) >> 3, hw = 4 * g + w;
      u32x4 wt[16];
      CODE_PREFETCH(mega_wo);
      lds_wait(L, C_PROG + w, IE);
      const u16* wb = wo + (size_t)(hw * 128 + 16 * r8) * D + cgp * 64 + c8 * 8;
#pragma unroll
      for (int i = 0; i < WOE; ++i) wt[i] = ldnt(wb + i * D);
      SB();
      TSTART(TA_WO);
#pragma unroll
      for (int i = WOE; i < 16; ++i) wt[i] = ldnt(wb + i * D);
      SB();
      ST_(5);
      u32x4 a[4]; u32x2 ml;
      const int off = WS_OP + (hw * 8 + 4 * (lane >> 5)) * 512 + (lane & 31) * 16;
      const int mo = WS_ST + (hw * 8 + c8) * 128;
      ml = ld_sc1_64(rs, mo);
      SB();
#pragma unroll
      for (int m = 0; m < 8; ++m) { prepack(wt[2 * m], wt[2 * m + 1]); pin(wt[2 * m]); pin(wt[2 * m + 1]); }
      float acc[8] = {0.f, 0.f, 0.f, 0.f, 0.f, 0.f, 0.f, 0.f};
      asm volatile("" : "+v"(acc[0]), "+v"(acc[1]), "+v"(acc[2]), "+v"(acc[3]), "+v"(acc[4]), "+v"(acc[5]), "+v"(acc[6]), "+v"(acc[7]));
      float wj[4];
      CODE_ALIGN(mega_wo, 11);
      NOUNROLL for (unsigned s = 0; s < SPIN_G; ++s) {
        MEMBAR();
#pragma unroll
        for (int i = 0; i < 4; ++i) a[i] = ld_sc1(rs, off + i * 512);
        const bool mok = __all(max(ml[0], ml[1]) != SENT);
        {
          float M = bf(ml[0]);
          M = fmaxf(M, dpp<XOR1>(M)); M = fmaxf(M, dpp<XOR2>(M)); M = fmaxf(M, dpp<XOR7>(M));
          const float e = __expf(bf(ml[0]) - M);
          float Ls = bf(ml[1]) * e;
          Ls += dpp<XOR1>(Ls); Ls += dpp<XOR2>(Ls); Ls += dpp<XOR7>(Ls);
          const float wgt = e * __builtin_amdgcn_rcpf(Ls);
#pragma unroll
          for (int i = 0; i < 4; ++i) {
            const float lo = bf(__builtin_amdgcn_readlane(fb(wgt), i)), hi = bf(__builtin_amdgcn_readlane(fb(wgt), 4 + i));
            wj[i] = lane < 32 ? lo : hi;
          }
          asm volatile("" : "+v"(wj[0]), "+v"(wj[1]), "+v"(wj[2]), "+v"(wj[3]));
        }
        if (!mok) ml = ld_sc1_64(rs, mo);
        if (mok && __all(max(max(mx4(a[0]), mx4(a[1])), max(mx4(a[2]), mx4(a[3]))) != SENT)) break;
      }
      ST_(6);
      float t4[4];
#pragma unroll
      for (int k = 0; k < 4; ++k) t4[k] = fmaf(wj[0], bf(a[0][k]), wj[1] * bf(a[1][k])) + fmaf(wj[2], bf(a[2][k]), wj[3] * bf(a[3][k]));
      LAS unsigned* OB = (LAS unsigned*)(L + L_OB) + w * 64;
      OB[2 * (lane & 31) + (lane >> 5)] = pack2(tr32(t4[0], t4[2]), tr32(t4[1], t4[3]));
      MEMBAR();
      const u32x4 oa = *(volatile LAS u32x4*)(OB + 8 * r8), ob = *(volatile LAS u32x4*)(OB + 8 * r8 + 4);
#pragma unroll
      for (int m = 0; m < 4; ++m) mac_packed(acc, wt[2 * m], wt[2 * m + 1], oa[m]);
#pragma unroll
      for (int m = 0; m < 4; ++m) mac_packed(acc, wt[8 + 2 * m], wt[8 + 2 * m + 1], ob[m]);
      const float y = red8_b345(acc, lane);
      YR[w * 64 + col64] = y;
      HBAR();
      if (w == 0 && lane < 16) st_sc1(rs, WS_YP + (g * 640 + cgp * 64 + 4 * lane) * 4, nosent4(sum4(YR + 4 * lane, 64)));
      ST_(7);
      CODE_END(mega_wo);
    } else if (role >= R_FFN && role < R_FFN + NF) {
      const int f = role - R_FFN;
      const int row0 = 160 * w + 10 * r16;
      const bool fin = f < 20 && w == 3;
      const int oc = (f < 20 ? 32 * f : 0) + 4 * c8 + ((lane >> 4) & 1) * 2 + (lane >> 5);
      LAS unsigned* X1 = (LAS unsigned*)(L + L_X1);
      u32x4 wt[18]; unsigned w2t[8];
      CODE_PREFETCH(mega_ffn);
      if (CPF && w == 2) {
        u64 pc_; int d_, len_;
        asm volatile("s_getpc_b64 %0\n.Lmega_gat_pc:\n\ts_mov_b32 %1, .Lmega_gat_beg-.Lmega_gat_pc\n\ts_mov_b32 %2, .Lmega_gat_end-.Lmega_gat_beg" : "=s"(pc_), "=s"(d_), "=s"(len_));
        const rsrc_t rc_ = mkrs((const void*)(pc_ + (long long)d_), min(max(len_, 0), 4096));
        const unsigned c_ = __builtin_amdgcn_raw_buffer_load_b32(rc_, lane * 64, 0, 0);
        asm volatile("" :: "v"(c_));
      }
      lds_wait(L, C_PROG + w, IE);
      const u16* wb = w1 + (size_t)row0 * DFF + f * 32 + c4 * 8;
      const u16* w2b = w2 + (size_t)(f * 32 + 8 * r4) * D + 160 * w;
#pragma unroll
      for (int i = 0; i < 18; ++i) {
        if (i == FFE) { SB(); TSTART(TA_FFN); }
        if (i < 10) wt[i] = ldnt(wb + (size_t)i * DFF);
        else {
          wt[i] = ldnt(w2b + (i - 10) * D + c16 * 8);
          w2t[i - 10] = __builtin_nontemporal_load((const unsigned*)(w2b + (i - 10) * D + 128 + c16 * 2));
        }
      }
      SB();
      lds_arrive_nr(C_WOUT);
      ST_(5);
      u32x2 sbA, sbB, cbA, cbB; unsigned gbw;
      NOUNROLL for (unsigned s = 0; s < SPIN_G; ++s) {
        MEMBAR();
        gbw = __builtin_amdgcn_raw_buffer_load_b32(rs, WS_M2 + 7680 + oc * 4, 0, 16);
        sbA = ld_sc1_64(rs, WS_M2 + 2560 + (row0 + 2 * c4) * 4); sbB = ld_sc1_64(rs, WS_M2 + 2560 + (row0 + 8) * 4);
        cbA = ld_sc1_64(rs, WS_M2 + 5120 + (row0 + 2 * c4) * 4); cbB = ld_sc1_64(rs, WS_M2 + 5120 + (row0 + 8) * 4);
        const unsigned mm = max(gbw, max(max(max(sbA[0], sbA[1]), max(sbB[0], sbB[1])), max(max(cbA[0], cbA[1]), max(cbB[0], cbB[1]))));
        if (__all(mm != SENT)) break;
        __builtin_amdgcn_s_sleep(MSL);
      }
      float c1[4] = {rbf(1.f + bf(cbA[0])), rbf(1.f + bf(cbA[1])), rbf(1.f + bf(cbB[0])), rbf(1.f + bf(cbB[1]))};
      asm volatile("" : "+v"(c1[0]), "+v"(c1[1]), "+v"(c1[2]), "+v"(c1[3]));
      const float s1[4] = {bf(sbA[0]), bf(sbA[1]), bf(sbB[0]), bf(sbB[1])};
#pragma unroll
      for (int m = 0; m < 9; ++m) { prepack(wt[2 * m], wt[2 * m + 1]); pin(wt[2 * m]); pin(wt[2 * m + 1]); }
#pragma unroll
      for (int m = 0; m < 4; ++m) {
        const unsigned lo = __builtin_amdgcn_perm(w2t[2 * m + 1], w2t[2 * m], 0x05040100u), hi = __builtin_amdgcn_perm(w2t[2 * m + 1], w2t[2 * m], 0x07060302u);
        w2t[2 * m] = lo; w2t[2 * m + 1] = hi;
        asm volatile("" : "+v"(w2t[2 * m]), "+v"(w2t[2 * m + 1]));
      }
      float acc[8] = {0.f, 0.f, 0.f, 0.f, 0.f, 0.f, 0.f, 0.f};
      asm volatile("" : "+v"(acc[0]), "+v"(acc[1]), "+v"(acc[2]), "+v"(acc[3]), "+v"(acc[4]), "+v"(acc[5]), "+v"(acc[6]), "+v"(acc[7]));
      float z[8] = {0.f, 0.f, 0.f, 0.f, 0.f, 0.f, 0.f, 0.f};
      float z8 = 0.f, z9 = 0.f;
      asm volatile("" : "+v"(z[0]), "+v"(z[1]), "+v"(z[2]), "+v"(z[3]), "+v"(z[4]), "+v"(z[5]), "+v"(z[6]), "+v"(z[7]), "+v"(z8), "+v"(z9));
      ST_(6);
      HBAR();
      unsigned hp5[5];
      {
        const u32x4 ya = *(LAS u32x4*)(X1 + 4 * lane);
        const unsigned yb = X1[256 + lane];
        const unsigned hA = X1[(row0 >> 1) + c4], hB = X1[(row0 >> 1) + 4];
        float sa_ = dot2(yb, 0x3F803F80u, 0.f), sq_ = dot2(yb, yb, 0.f);
#pragma unroll
        for (int k = 0; k < 4; ++k) { sa_ = dot2(ya[k], 0x3F803F80u, sa_); sq_ = dot2(ya[k], ya[k], sq_); }
        float r = tr32(sa_, sq_);
        r = tr16(r, r);
        r = row_sum(r);
        const float mean = bf(__builtin_amdgcn_readlane(fb(r), 0)) * (1.f / 640.f);
        float var = fmaf(-mean, mean, bf(__builtin_amdgcn_readlane(fb(r), 32)) * (1.f / 640.f));
        if (!(var * 1024.f > mean * mean)) {
          float q2 = 0.f;
#pragma unroll
          for (int k = 0; k < 4; ++k) { const float d0 = bflo(ya[k]) - mean, d1 = bfhi(ya[k]) - mean; q2 += d0 * d0 + d1 * d1; }
          const float d0 = bflo(yb) - mean, d1 = bfhi(yb) - mean; q2 += d0 * d0 + d1 * d1;
          var = wave_sum(q2) * (1.f / 640.f);
        }
        const float mr = rbf(mean);
        const float rden = __builtin_amdgcn_rcpf(rbf(__builtin_amdgcn_sqrtf(rbf(rbf(var) + 1e-5f))));
        const float xr2[4] = {bflo(hA), bfhi(hA), bflo(hB), bfhi(hB)};
        float hv[4];
#pragma unroll
        for (int k = 0; k < 4; ++k) hv[k] = rbf(rbf(rbf(xr2[k] - mr) * rden) * c1[k]) + s1[k];
        const unsigned pA = pack2(hv[0], hv[1]);
        hp5[4] = pack2(hv[2], hv[3]);
        hp5[0] = __builtin_amdgcn_mov_dpp((int)pA, 0x00, 0xf, 0xf, true);
        hp5[1] = __builtin_amdgcn_mov_dpp((int)pA, 0x55, 0xf, 0xf, true);
        hp5[2] = __builtin_amdgcn_mov_dpp((int)pA, 0xAA, 0xf, 0xf, true);
        hp5[3] = __builtin_amdgcn_mov_dpp((int)pA, 0xFF, 0xf, 0xf, true);
      }
      ST_(7);
      {
#pragma unroll
        for (int m = 0; m < 5; ++m) mac_packed(acc, wt[2 * m], wt[2 * m + 1], hp5[m]);
        float y = red8_b345(acc, lane);
        y += xor4(y);
        YR[w * 64 + col32] = y;
        HBAR();
        const LAS float* yp = YR + 8 * r4 + c8;
        float hd = (yp[0] + yp[64]) + (yp[128] + yp[192]);
        hd = hd < 0.f ? 0.f : hd;
        const unsigned hpk = pack2(hd, dpp<XOR1>(hd));
        const unsigned hp[4] = {(unsigned)__builtin_amdgcn_mov_dpp((int)hpk, 0x150, 0xf, 0xf, true), (unsigned)__builtin_amdgcn_mov_dpp((int)hpk, 0x152, 0xf, 0xf, true),
                                (unsigned)__builtin_amdgcn_mov_dpp((int)hpk, 0x154, 0xf, 0xf, true), (unsigned)__builtin_amdgcn_mov_dpp((int)hpk, 0x156, 0xf, 0xf, true)};
#pragma unroll
        for (int m = 0; m < 4; ++m) {
          mac_packed(z, wt[10 + 2 * m], wt[10 + 2 * m + 1], hp[m]);
          z8 = dot2(w2t[2 * m], hp[m], z8);
          z9 = dot2(w2t[2 * m + 1], hp[m], z9);
        }
        float v[4];
#pragma unroll
        for (int k = 0; k < 4; ++k) v[k] = tr16(z[k], z[k + 4]);
        float e = tr16(z8, z9);
        const int zo = WS_ZP + f * 2560 + w * 640;
        st_sc1_64(rs, zo + (c16 * 8 + (r4 & 1) * 4 + (r4 >> 1) * 2) * 4, u32x2{nosent(tr32(v[0], v[2])), nosent(tr32(v[1], v[3]))});
        e = tr32(e, e);
        __builtin_amdgcn_raw_buffer_store_b32(nosent(e), rs, zo + 512 + (c16 * 2 + (r4 & 1)) * 4, 0, 16);
      }
      ST_(8);
      if (fin) {
        u32x4 zp[7];
        const int zo = WS_ZP + r8 * 2560 + f * 128 + c8 * 16;
        const int z6 = r8 < NF - 48 ? zo + 48 * 2560 : 0x7FFFFF00;
        const float xo = bflo((unsigned)((LAS u16*)X1)[oc]);
        NOUNROLL for (unsigned s = 0; s < SPIN_G; ++s) {
          MEMBAR();
#pragma unroll
          for (int i = 0; i < 6; ++i) zp[i] = ld_sc1s(rs, zo, i * 8 * 2560);
          zp[6] = ld_sc1(rs, z6);
          unsigned mm = mx4(zp[0]);
#pragma unroll
          for (int i = 1; i < 7; ++i) mm = max(mm, mx4(zp[i]));
          if (__all(mm != SENT)) break;
        }
        ST_(9);
        float r[4];
#pragma unroll
        for (int k = 0; k < 4; ++k) r[k] = ((bf(zp[0][k]) + bf(zp[1][k])) + (bf(zp[2][k]) + bf(zp[3][k]))) + ((bf(zp[4][k]) + bf(zp[5][k])) + bf(zp[6][k]));
        float v = tr32(tr16(r[0], r[2]), tr16(r[1], r[3]));
        v += dpp<XOR8>(v);
        float dbgv = 0.f;
        if (f == 0) {
          const u64 now = __builtin_amdgcn_s_memrealtime();
          float mn = 1e30f, mx = -1e30f;
          for (int i = 0; i < 4; ++i) { const u64 t = __hip_atomic_load((gu64*)(ws + (3 << 20)) + lane + 64 * i, RLX_AGENT); const float d = (float)(long long)(now - t) * 0.01f; mn = fminf(mn, d); mx = fmaxf(mx, d); }
          mx = wave_max(mx); mn = -wave_max(-mn);
          const float val = DBGSEL == 0 ? mx : (mx - mn);
          if (val > DBGTHR && oc == 0) dbgv = val;
        }
        out[oc] = (u16)f2bf(xo + rbf(bf(gbw) * rbf(v)) + dbgv);
        if (f == 0 && lane == 0) {
          const u64 e1 = (((u64)Ev[1] << 32) | (u64)Ev[0]) + 1ull;
          st_sc1_64(rs, WS_EPOCH, u32x2{(unsigned)e1, (unsigned)(e1 >> 32)});
        }
        ST_(10);
        CODE_END(mega_ffn);
      }
    }
  }
}

extern "C" void kernel_launch(void* const* d_in, const int* in_sizes, int n_in, void* d_out, int out_size, void* d_ws,
                              size_t ws_size, hipStream_t stream) {
  (void)in_sizes; (void)n_in; (void)out_size; (void)ws_size;
  (void)hipFuncSetAttribute((const void*)mega, hipFuncAttributeMaxDynamicSharedMemorySize, LDS_BYTES);
  hipLaunchKernelGGL(mega, dim3(256), dim3(768), LDS_BYTES, stream, (const u16*)d_in[1], (const u16*)d_in[2], (unsigned char*)d_ws,
                     (const u16*)d_in[9], (const u16*)d_in[8], (const u16*)d_in[0], (const u16*)d_in[3], (const u16*)d_in[4],
                     (const u16*)d_in[6], (const u16*)d_in[7], (u16*)d_out);
}
#define STAMP_ROLE 1
#define STAMP_CLS(r) ((r) < N_M2 ? 3 : (r) < N_M2 + N_WO ? 2 : (r) < N_M2 + N_WO + NF ? 4 : 0)
#define NWAVES 12
#define NSTREAM 8
```

```cpp
#include <hip/hip_runtime.h>
#include <hip/hip_bf16.h>
#include <stdint.h>

#define GAS __attribute__((address_space(1)))
#define LAS __attribute__((address_space(3)))
typedef unsigned short u16;
typedef unsigned long long u64;
typedef unsigned u32x4 __attribute__((ext_vector_type(4)));
typedef unsigned u32x2 __attribute__((ext_vector_type(2)));
typedef float f32x4 __attribute__((ext_vector_type(4)));
typedef float f32x2 __attribute__((ext_vector_type(2)));
typedef __bf16 bf16x2 __attribute__((ext_vector_type(2)));
typedef __amdgpu_buffer_rsrc_t rsrc_t;

constexpr int D = 640, HQ = 32, SK = 16384, DH = 128, WIN = 4096, DFF = 1728;

constexpr int DEPA = 1, ISW = 8;
constexpr int DEP = 2, IB = 24;
constexpr int IS = 26;
constexpr int ITK = 22;
constexpr int T1 = 420, T3 = 540, T2 = 720;
constexpr int WOE = 12, FFE = 5;

constexpr int R_M2 = 0, N_M2 = 40;
constexpr int R_FFN = 40, NF = 54;
constexpr int R_WO = 96, N_WO = 80;

constexpr int WS_EPOCH = 0;
constexpr int WS_TICKET = 2048;
constexpr int WS_FLAGS = 4096;
constexpr int WS_MP = 8192;
constexpr int WS_QP = 65536;
constexpr int WS_OP = WS_QP + 131072;
constexpr int WS_ST = WS_OP + 131072;
constexpr int WS_YP = WS_ST + 32768;
constexpr int WS_M2 = WS_YP + 20480;
constexpr int WS_ZP = WS_M2 + 10240;
constexpr int WS_END = WS_ZP + NF * 2560;

constexpr unsigned SENT = 0xFFFFFFFFu;
constexpr int OOB = 0x7FFFFF00;
constexpr unsigned SPIN_G = 1u << 18;
constexpr unsigned SPIN_L = 1u << 22;

constexpr int C_PROG = 0;
constexpr int C_MOD1 = 8, C_QP = 9, C_QREADY = 10, C_M = 11, C_ROLE = 12, C_PV = 13, C_FLAGOK = 14, C_T1 = 15, C_WOUT = 16;
constexpr int L_P = 128;
constexpr int L_MR = L_P + 1024;
constexpr int L_QR = L_MR + 1024;
constexpr int L_OR = L_QR + 2048;
constexpr int L_OB = L_OR + 4096;
constexpr int L_YR = L_OB + 1024;
constexpr int L_STAT = L_YR + 1024;
constexpr int L_Q = L_STAT + 64;
constexpr int L_X1 = L_Q + 256;
constexpr int LDS_BYTES = 96 * 1024;

__device__ __forceinline__ float bflo(unsigned w) { return __builtin_bit_cast(float, w << 16); }
__device__ __forceinline__ float bfhi(unsigned w) { return __builtin_bit_cast(float, w & 0xffff0000u); }
__device__ __forceinline__ unsigned f2bf(float f) { return (unsigned)__builtin_bit_cast(u16, (__bf16)f); }
__device__ __forceinline__ float rbf(float f) { return bflo(f2bf(f)); }
__device__ __forceinline__ unsigned pack2(float lo, float hi) {
  bf16x2 v = {(__bf16)lo, (__bf16)hi};
  return __builtin_bit_cast(unsigned, v);
}
__device__ __forceinline__ float dot2(unsigned a, unsigned b, float acc) {
  return __builtin_amdgcn_fdot2_f32_bf16(__builtin_bit_cast(bf16x2, a), __builtin_bit_cast(bf16x2, b), acc, false);
}
__device__ __forceinline__ unsigned fb(float f) { return __builtin_bit_cast(unsigned, f); }
__device__ __forceinline__ float bf(unsigned u) { return __builtin_bit_cast(float, u); }
__device__ __forceinline__ unsigned nosent(float f) { return min(fb(f), 0xFFFFFFFEu); }
__device__ __forceinline__ u32x4 nosent4(f32x4 s) { return u32x4{nosent(s[0]), nosent(s[1]), nosent(s[2]), nosent(s[3])}; }
template <int CTRL> __device__ __forceinline__ float dpp(float x) {
  return __builtin_bit_cast(float, __builtin_amdgcn_mov_dpp(__builtin_bit_cast(int, x), CTRL, 0xf, 0xf, true));
}
template <int CTRL> __device__ __forceinline__ unsigned dppu(unsigned x) { return (unsigned)__builtin_amdgcn_mov_dpp((int)x, CTRL, 0xf, 0xf, true); }
constexpr int XOR1 = 0xB1, XOR2 = 0x4E, XOR7 = 0x141, XOR8 = 0x128;
__device__ __forceinline__ float xor4(float x) {
  int v = __builtin_bit_cast(int, x);
  int r = __builtin_amdgcn_update_dpp(0, v, 0x114, 0xf, 0xa, false);
  r = __builtin_amdgcn_update_dpp(r, v, 0x104, 0xf, 0x5, false);
  return __builtin_bit_cast(float, r);
}
__device__ __forceinline__ float tr16(float a, float b) {
  auto s = __builtin_amdgcn_permlane16_swap(fb(a), fb(b), false, false);
  return bf(s[0]) + bf(s[1]);
}
__device__ __forceinline__ float tr32(float a, float b) {
  auto s = __builtin_amdgcn_permlane32_swap(fb(a), fb(b), false, false);
  return bf(s[0]) + bf(s[1]);
}
__device__ __forceinline__ float row_sum(float x) {
  x += dpp<XOR1>(x); x += dpp<XOR2>(x); x += dpp<XOR7>(x); x += dpp<XOR8>(x); return x;
}
__device__ __forceinline__ float wave_sum(float x) { x = row_sum(x); x = tr16(x, x); return tr32(x, x); }
__device__ __forceinline__ float wave_max(float x) {
  x = fmaxf(x, dpp<XOR1>(x)); x = fmaxf(x, dpp<XOR2>(x)); x = fmaxf(x, dpp<XOR7>(x)); x = fmaxf(x, dpp<XOR8>(x));
  auto s = __builtin_amdgcn_permlane16_swap(fb(x), fb(x), false, false);
  x = fmaxf(bf(s[0]), bf(s[1]));
  auto t = __builtin_amdgcn_permlane32_swap(fb(x), fb(x), false, false);
  return fmaxf(bf(t[0]), bf(t[1]));
}
__device__ __forceinline__ unsigned bij32(unsigned x) {
  x += 0x9E3779B9u; x ^= x >> 16; x *= 0x85EBCA6Bu; x ^= x >> 13; x *= 0xC2B2AE35u; x ^= x >> 16; return x;
}
__device__ __forceinline__ rsrc_t mkrs(const void* p, int bytes) { return __builtin_amdgcn_make_buffer_rsrc((void*)p, 0, bytes, 0x00020000); }
__device__ __forceinline__ u32x4 ldnt(const void* p) { return __builtin_nontemporal_load(reinterpret_cast<const u32x4*>(p)); }
__device__ __forceinline__ u32x4 ld_sc1(rsrc_t rs, int voff, int soff = 0) { return __builtin_amdgcn_raw_buffer_load_b128(rs, voff, soff, 16); }
__device__ __forceinline__ u32x2 ld_sc1_64(rsrc_t rs, int off) { return __builtin_amdgcn_raw_buffer_load_b64(rs, off, 0, 16); }
__device__ __forceinline__ unsigned ld_sc1_32(rsrc_t rs, int off) { return __builtin_amdgcn_raw_buffer_load_b32(rs, off, 0, 16); }
__device__ __forceinline__ void st_sc1(rsrc_t rs, int off, u32x4 v) { __builtin_amdgcn_raw_buffer_store_b128(v, rs, off, 0, 16); }
__device__ __forceinline__ void st_sc1_64(rsrc_t rs, int off, u32x2 v) { __builtin_amdgcn_raw_buffer_store_b64(v, rs, off, 0, 16); }
__device__ __forceinline__ void st_sc1_32(rsrc_t rs, int off, unsigned v) { __builtin_amdgcn_raw_buffer_store_b32(v, rs, off, 0, 16); }
__device__ __forceinline__ unsigned mx4(u32x4 v) { return max(max(v[0], v[1]), max(v[2], v[3])); }
#define MEMBAR() asm volatile("" ::: "memory")
#define LGKM0() asm volatile("s_waitcnt lgkmcnt(0)" ::: "memory")
#define SB() __builtin_amdgcn_sched_barrier(0)
#define NOUNROLL _Pragma("nounroll")
#define HBAR() do { LGKM0(); __builtin_amdgcn_s_barrier(); MEMBAR(); } while (0)
#define PIN8(a) asm volatile("" : "+v"(a[0]), "+v"(a[1]), "+v"(a[2]), "+v"(a[3]), "+v"(a[4]), "+v"(a[5]), "+v"(a[6]), "+v"(a[7]))

__device__ __forceinline__ void lds_put(LAS unsigned char* L, int word, unsigned v) { ((volatile LAS unsigned*)L)[word] = v; }
template <int SLEEP> __device__ __forceinline__ void lds_wait_t(LAS unsigned char* L, int word, unsigned v) {
  NOUNROLL for (unsigned s = 0; s < SPIN_L; ++s) {
    if (((volatile LAS unsigned*)L)[word] >= v) break;
    if (SLEEP) __builtin_amdgcn_s_sleep(SLEEP);
  }
  MEMBAR();
}
#define lds_wait lds_wait_t<1>
#define lds_spin lds_wait_t<0>
__device__ __forceinline__ unsigned lds_arrive(int word) {
  unsigned old;
  asm volatile("s_mov_b64 exec, 1\n\tds_add_rtn_u32 %0, %1, %2\n\ts_mov_b64 exec, -1\n\ts_waitcnt lgkmcnt(0)"
               : "=&v"(old) : "v"(word * 4), "v"(1u) : "memory");
  return __builtin_amdgcn_readfirstlane(old);
}
__device__ __forceinline__ void lds_arrive_nr(int word) {
  asm volatile("s_mov_b64 exec, 1\n\tds_add_u32 %0, %1\n\ts_mov_b64 exec, -1" :: "v"(word * 4), "v"(1u) : "memory");
}
__device__ __forceinline__ void wait_until(u64 t) {
  NOUNROLL for (unsigned s = 0; s < 4096u; ++s) {
    if (__builtin_amdgcn_s_memrealtime() >= t) break;
    __builtin_amdgcn_s_sleep(2);
  }
  MEMBAR();
}
template <int N> __device__ __forceinline__ void vmwait() { asm volatile("s_waitcnt vmcnt(%0)" :: "i"(N) : "memory"); }

__device__ __forceinline__ float red8_b345(const float (&a)[8], int lane) {
  float v[4];
  const bool hi = (lane & 8) != 0;
#pragma unroll
  for (int k = 0; k < 4; ++k) {
    const float t1 = a[k] + dpp<XOR8>(a[k]), t2 = a[k + 4] + dpp<XOR8>(a[k + 4]);
    v[k] = hi ? t2 : t1;
  }
  const float u0 = tr16(v[0], v[2]), u1 = tr16(v[1], v[3]);
  return tr32(u0, u1);
}
__device__ __forceinline__ void prepack(u32x4& wa, u32x4& wb) {
#pragma unroll
  for (int d = 0; d < 4; ++d) {
    const unsigned lo = __builtin_amdgcn_perm(wb[d], wa[d], 0x05040100u);
    const unsigned hi = __builtin_amdgcn_perm(wb[d], wa[d], 0x07060302u);
    wa[d] = lo; wb[d] = hi;
  }
}
__device__ __forceinline__ void prepack_now(u32x4& wa, u32x4& wb) { prepack(wa, wb); asm volatile("" : "+v"(wa), "+v"(wb)); }
__device__ __forceinline__ void mac_packed(float (&acc)[8], u32x4 wa, u32x4 wb, unsigned xp) {
#pragma unroll
  for (int d = 0; d < 4; ++d) { acc[2 * d] = dot2(wa[d], xp, acc[2 * d]); acc[2 * d + 1] = dot2(wb[d], xp, acc[2 * d + 1]); }
}
__device__ __forceinline__ void mac_pair(float (&acc)[8], u32x4 wa, u32x4 wb, unsigned xp) { prepack(wa, wb); mac_packed(acc, wa, wb, xp); }
__device__ __forceinline__ float silu_f(float v) { return v * __builtin_amdgcn_rcpf(1.f + __expf(-v)); }
__device__ __forceinline__ unsigned silu2(unsigned w) { return pack2(silu_f(bflo(w)), silu_f(bfhi(w))); }
__device__ __forceinline__ f32x4 sum4(const LAS float* p, int stride) {
  return (*(const LAS f32x4*)p + *(const LAS f32x4*)(p + stride)) + (*(const LAS f32x4*)(p + 2 * stride) + *(const LAS f32x4*)(p + 3 * stride));
}

#define CODE_PREFETCH(NAME, DST) do { \
      u64 pc_; int len_; \
      asm volatile("s_getpc_b64 %0\n.L" #NAME "_beg:\n\ts_mov_b32 %1, .L" #NAME "_end-.L" #NAME "_beg" : "=s"(pc_), "=s"(len_)); \
      DST = __builtin_amdgcn_raw_buffer_load_b32(mkrs((const void*)pc_, min(max(len_, 0), 16384)), lane * 64, w * 4096, 0); } while (0)
#define CODE_LABEL(NAME) asm volatile(".L" #NAME ":")

__global__ void __launch_bounds__(768, 3)
mega(const u16* __restrict__ kc, const u16* __restrict__ vc, unsigned char* ws, const u16* __restrict__ wmod,
     const u16* __restrict__ cnd, const u16* __restrict__ x, const u16* __restrict__ wq, const u16* __restrict__ wo,
     const u16* __restrict__ w1, const u16* __restrict__ w2, u16* __restrict__ out) {
  asm volatile("" :: "s"(kc), "s"(vc), "s"(ws), "s"(wmod), "s"(cnd), "s"(x), "s"(wq), "s"(wo));
  extern __shared__ __attribute__((aligned(16))) unsigned char smem[];
  LAS unsigned char* L = (LAS unsigned char*)smem;
  const int tid = threadIdx.x, lane = tid & 63, wave = __builtin_amdgcn_readfirstlane(tid >> 6);
  const int b = blockIdx.x;
  const int h = (b & 7) * 4 + (b >> 6), j = (b >> 3) & 7, u = h * 8 + j;
  const int r4 = lane >> 4, c16 = lane & 15;
  const rsrc_t rs = mkrs(ws, WS_END);

  if (__builtin_expect(wave < 8, 0)) {
    const size_t kvoff = ((size_t)h * SK + (SK - WIN) + j * 512) * DH;
    const rsrc_t rk = mkrs(kc + kvoff, 131072), rv = mkrs(vc + kvoff, 131072);
    const int rot = (b * 8 + (b >> 4) + wave) * 1024;
    u32x4 big[32];
    u32x4 pp0, pp1;
    float mw;
    LAS float* STAT = (LAS float*)(L + L_STAT);
#define LOAD_STEP(i) do { \
      big[i] = __builtin_amdgcn_raw_buffer_load_b128((i) < 16 ? rk : rv, lane * 16, (((i) & 15) * 8192 + rot) & 131071, 2); \
      if ((i) == 0) __builtin_amdgcn_s_barrier();     \
      if ((i) == ITK) lds_put(L, C_PROG + wave, 1u); \
      if ((i) >= 16 + DEP + 2 && (((i) - DEP) & 1) == 0 && (i) <= IB) prepack_now(big[(i) - DEP - 2], big[(i) - DEP - 1]); \
      if ((i) < ISW) { if ((i) >= DEPA) vmwait<DEPA>(); } else if ((i) < IB) vmwait<DEP>(); \
      SB(); } while (0)
#pragma unroll
    for (int i = 0; i <= IS; ++i) LOAD_STEP(i);
    {
      lds_wait(L, C_QREADY, 1u);
      const u32x4 qp = *(LAS u32x4*)(L + L_Q + c16 * 16);
      float xs[16];
#pragma unroll
      for (int ii = 0; ii < 16; ++ii) {
        float d = 0.f;
#pragma unroll
        for (int k = 0; k < 4; ++k) d = dot2(big[ii][k], qp[k], d);
        xs[ii] = d;
      }
#define TR_STEP(HALF, CTRL, BIT) _Pragma("unroll") for (int q = 0; q < (HALF); ++q) { \
      const float lo_ = xs[q], hi_ = xs[q + (HALF)]; const float t1_ = lo_ + dpp<CTRL>(lo_), t2_ = hi_ + dpp<CTRL>(hi_); \
      xs[q] = (c16 & (BIT)) ? t2_ : t1_; }
      TR_STEP(8, XOR8, 8) TR_STEP(4, XOR7, 4) TR_STEP(2, XOR2, 2) TR_STEP(1, XOR1, 1)
#undef TR_STEP
      const float sc = rbf(rbf(xs[0]) * 0.08838834764831845f);
      mw = wave_max(sc);
      const float e = __expf(sc - mw);
      LAS u16* P = (LAS u16*)(L + L_P) + wave * 64;
      P[lane] = (u16)f2bf(e);
      MEMBAR();
      pp0 = *(volatile LAS u32x4*)(P + r4 * 16); pp1 = *(volatile LAS u32x4*)(P + r4 * 16 + 8);
      STAT[wave] = mw;
      STAT[8 + wave] = wave_sum(e);
      if (lds_arrive(C_M) == 7u) {
        const float m8 = STAT[lane & 7], l8 = STAT[8 + (lane & 7)];
        float mc = fmaxf(m8, dpp<XOR1>(m8)); mc = fmaxf(mc, dpp<XOR2>(mc)); mc = fmaxf(mc, dpp<XOR7>(mc));
        float lc = l8 * __expf(m8 - mc);
        lc += dpp<XOR1>(lc); lc += dpp<XOR2>(lc); lc += dpp<XOR7>(lc);
        if (lane == 0) st_sc1_64(rs, WS_ST + u * 128, u32x2{nosent(mc), nosent(lc)});
      }
    }
#pragma unroll
    for (int i = IS + 1; i < 32; ++i) LOAD_STEP(i);
#undef LOAD_STEP
    __builtin_amdgcn_s_setprio(2);
    {
      float acc[8] = {0.f, 0.f, 0.f, 0.f, 0.f, 0.f, 0.f, 0.f};
      constexpr int NPRE = (IB - 16 - DEP) / 2;
      float scl;
#pragma unroll
      for (int m = 0; m < 8; ++m) {
        if (m == NPRE) {
          lds_spin(L, C_M, 8u);
          const f32x4 ma = *(LAS f32x4*)STAT, mb = *(LAS f32x4*)(STAT + 4);
          const float mc = fmaxf(fmaxf(fmaxf(ma[0], ma[1]), fmaxf(ma[2], ma[3])), fmaxf(fmaxf(mb[0], mb[1]), fmaxf(mb[2], mb[3])));
          scl = __expf(mw - mc);
          asm volatile("" : "+v"(scl));
        }
        if (m >= NPRE) prepack(big[16 + 2 * m], big[16 + 2 * m + 1]);
        mac_packed(acc, big[16 + 2 * m], big[16 + 2 * m + 1], m < 4 ? pp0[m & 3] : pp1[m & 3]);
        SB();
      }
      float v[4];
#pragma unroll
      for (int k = 0; k < 4; ++k) v[k] = tr16(acc[k], acc[k + 4]);
      const float y0 = tr32(v[0], v[2]) * scl, y1 = tr32(v[1], v[3]) * scl;
      LAS float* OR = (LAS float*)(L + L_OR);
      *(LAS f32x2*)(OR + wave * 128 + c16 * 8 + (r4 & 1) * 4 + (r4 >> 1) * 2) = f32x2{y0, y1};
      if (lds_arrive(C_PV) == 7u) {
        const f32x4 s = sum4(OR + (lane >> 5) * 512 + 4 * (lane & 31), 128);
        f32x4 t;
#pragma unroll
        for (int k = 0; k < 4; ++k) t[k] = tr32(s[k], s[k]);
        if (lane < 32) st_sc1(rs, WS_OP + u * 512 + lane * 16, nosent4(t));
        MEMBAR();
        lds_put(L, C_PV, 9u);
      }
    }
    if (wave < 5) {
      lds_wait(L, C_ROLE, 1u);
      const int role = __builtin_amdgcn_readfirstlane(((volatile LAS unsigned*)L)[C_ROLE]) - 1;
      if (role >= R_FFN && role < R_FFN + NF) {
        CODE_LABEL(mega_gat_beg);
        const int t = wave * 64 + lane, hf = lane & 1;
        lds_wait(L, C_WOUT, 4u);
        const unsigned xq = __builtin_nontemporal_load((const unsigned*)x + t);
        u32x2 ga;
        u32x4 a[4];
        const int off = hf * 4 * 2560 + (t >> 1) * 16;
        NOUNROLL for (unsigned s = 0; s < SPIN_G; ++s) {
          MEMBAR();
          ga = ld_sc1_64(rs, WS_M2 + t * 8);
#pragma unroll
          for (int i = 0; i < 4; ++i) a[i] = ld_sc1(rs, off, WS_YP + i * 2560);
          if (__all(max(max(max(mx4(a[0]), mx4(a[1])), max(mx4(a[2]), mx4(a[3]))), max(ga[0], ga[1])) != SENT)) break;
        }
        float sm[4];
#pragma unroll
        for (int k = 0; k < 4; ++k) sm[k] = (bf(a[0][k]) + bf(a[1][k])) + (bf(a[2][k]) + bf(a[3][k]));
        const float at0 = (hf ? sm[2] : sm[0]) + dpp<XOR1>(hf ? sm[0] : sm[2]), at1 = (hf ? sm[3] : sm[1]) + dpp<XOR1>(hf ? sm[1] : sm[3]);
        ((LAS unsigned*)(L + L_X1))[t] = pack2(bflo(xq) + rbf(bf(ga[0]) * rbf(at0)), bfhi(xq) + rbf(bf(ga[1]) * rbf(at1)));
        HBAR();
        CODE_LABEL(mega_gat_end);
      }
    }
  } else {
    const int w = wave - 8;
    int zero_v;
    asm volatile("v_mov_b32 %0, 0" : "=v"(zero_v));
    const int r8 = lane >> 3, c8 = lane & 7;
    const int col64 = c8 * 8 + ((lane >> 3) & 1) * 4 + ((lane >> 4) & 1) * 2 + (lane >> 5);
    __builtin_amdgcn_s_setprio(3);
    const u64 tstart = __builtin_amdgcn_s_memrealtime();
    const u32x2 Ev = ld_sc1_64(rs, WS_EPOCH + zero_v);
    const bool m1 = b < 80;
    const int cg = b % 20, rg = b / 20;
    u32x4 wm[5]; unsigned cw0, cw1, cw2;
    {
      const rsrc_t rm = mkrs(wmod, m1 ? D * 6 * D * 2 : 0), rcn = mkrs(cnd, m1 ? D * 2 : 0);
      const int row0 = 160 * rg + 40 * w;
      const int so = row0 * (6 * D * 2) + cg * 128, vo = r8 * (2 * 6 * D * 2) + c8 * 16;
      wm[0] = __builtin_amdgcn_raw_buffer_load_b128(rm, vo, so, 2);
      wm[1] = __builtin_amdgcn_raw_buffer_load_b128(rm, vo, so + 6 * D * 2, 2);
      wm[2] = __builtin_amdgcn_raw_buffer_load_b128(rm, vo, so + 16 * 6 * D * 2, 2);
      wm[3] = __builtin_amdgcn_raw_buffer_load_b128(rm, vo, so + 17 * 6 * D * 2, 2);
      wm[4] = __builtin_amdgcn_raw_buffer_load_b128(rm, r8 * (6 * D * 2) + c8 * 16, so + 32 * 6 * D * 2, 2);
      cw0 = __builtin_amdgcn_raw_buffer_load_b32(rcn, r8 * 4, row0 * 2, 2);
      cw1 = __builtin_amdgcn_raw_buffer_load_b32(rcn, r8 * 4, row0 * 2 + 32, 2);
      cw2 = (unsigned)(u16)__builtin_amdgcn_raw_buffer_load_b16(rcn, r8 * 2, row0 * 2 + 64, 2);
    }
    const int l20 = (lane & 31) < 20 ? (lane & 31) : 19;
    const int hrow = 80 * j + 20 * w + l20;
    const u32x4 xa = ldnt(x + lane * 8);
    const unsigned xb = __builtin_nontemporal_load((const unsigned*)x + 256 + lane);
    const float xr = bflo((unsigned)__builtin_nontemporal_load(x + hrow));
    if (w == 3 && lane < 32) lds_put(L, lane, 0u);
    SB();
    LGKM0();
    __builtin_amdgcn_s_barrier();
    SB();
    u32x4 wqv[5];
    {
      const rsrc_t rq = mkrs(wq, D * HQ * DH * 2);
#pragma unroll
      for (int i = 0; i < 5; ++i)
        wqv[i] = __builtin_amdgcn_raw_buffer_load_b128(rq, r4 * (5 * HQ * DH * 2) + c16 * 16, (80 * j + 20 * w + i) * (HQ * DH * 2) + h * (DH * 2), 2);
      SB();
    }
    const unsigned tag = bij32(Ev[0]), flo = bij32(Ev[0] ^ Ev[1] ^ 0x5bd1e995u);
    if (m1) {
      float acc[8] = {0.f, 0.f, 0.f, 0.f, 0.f, 0.f, 0.f, 0.f};
      mac_pair(acc, wm[0], wm[1], silu2(cw0));
      mac_pair(acc, wm[2], wm[3], silu2(cw1));
      const unsigned sl = f2bf(silu_f(bflo(cw2))), sh = sl << 16;
#pragma unroll
      for (int d = 0; d < 4; ++d) { acc[2 * d] = dot2(wm[4][d], sl, acc[2 * d]); acc[2 * d + 1] = dot2(wm[4][d], sh, acc[2 * d + 1]); }
      LAS float* MR = (LAS float*)(L + L_MR);
      MR[w * 64 + col64] = red8_b345(acc, lane);
      if (lds_arrive(C_MOD1) == 3u) {
        const float s = (MR[lane] + MR[64 + lane]) + (MR[128 + lane] + MR[192 + lane]);
        st_sc1_64(rs, WS_MP + (rg * 1280 + cg * 64 + lane) * 8, u32x2{fb(s), tag});
      }
    }
    if (w == 3) {
      u32x4 sv = {SENT, SENT, SENT, SENT};
      st_sc1(rs, (lane < 32 ? WS_QP : WS_OP) + u * 512 + (lane & 31) * 16, sv);
      if (b >= 128 && b < 128 + NF) {
        st_sc1(rs, WS_ZP + (b - 128) * 2560 + lane * 16, sv);
        st_sc1(rs, WS_ZP + (b - 128) * 2560 + 1024 + lane * 16, sv);
      }
      int o2 = OOB;
      if (lane == 63) o2 = WS_ST + u * 128;
      else if (b < 120) { if (lane < 16) o2 = WS_YP + b * 256 + lane * 16; }
      else if (b >= 128 && b < 128 + NF) { if (lane < 32) o2 = WS_ZP + (b - 128) * 2560 + 2048 + lane * 16; }
      else if (b == 255 && lane < 8) { o2 = WS_TICKET + lane * 128; sv = u32x4{0u, 0u, 0u, 0u}; }
      st_sc1(rs, o2, sv);
      vmwait<0>();
      if (lane == 0) st_sc1_64(rs, WS_FLAGS + b * 8, u32x2{flo, tag});
    }
    {
      float nrm;
      {
        float s = bflo(xb) + bfhi(xb);
#pragma unroll
        for (int i = 0; i < 4; ++i) s += bflo(xa[i]) + bfhi(xa[i]);
        const float mean = wave_sum(s) * (1.f / 640.f);
        float q2 = (bflo(xb) - mean) * (bflo(xb) - mean) + (bfhi(xb) - mean) * (bfhi(xb) - mean);
#pragma unroll
        for (int i = 0; i < 4; ++i) { const float d0 = bflo(xa[i]) - mean, d1 = bfhi(xa[i]) - mean; q2 += d0 * d0 + d1 * d1; }
        const float var = wave_sum(q2) * (1.f / 640.f);
        const float den = rbf(sqrtf(rbf(rbf(var) + 1e-5f)));
        nrm = rbf(rbf(xr - rbf(mean)) / den);
      }
      const int mp = WS_MP + ((lane >> 5) * 640 + hrow) * 8;
      u32x2 g[4];
      wait_until(tstart + T1);
      NOUNROLL for (unsigned s = 0; s < SPIN_G; ++s) {
        MEMBAR();
#pragma unroll
        for (int i = 0; i < 4; ++i) g[i] = ld_sc1_64(rs, mp + i * 1280 * 8);
        if (__all((((g[0][1] ^ tag) | (g[1][1] ^ tag)) | ((g[2][1] ^ tag) | (g[3][1] ^ tag))) == 0u)) break;
        __builtin_amdgcn_s_sleep(16);
      }
      const float mv = rbf((bf(g[0][0]) + bf(g[1][0])) + (bf(g[2][0]) + bf(g[3][0])));
      auto sw = __builtin_amdgcn_permlane32_swap(fb(mv), fb(mv), false, false);
      const float hval = rbf(rbf(nrm * rbf(1.f + bf(sw[1]))) + bf(sw[0]));
      float acc[8] = {0.f, 0.f, 0.f, 0.f, 0.f, 0.f, 0.f, 0.f};
#pragma unroll
      for (int i = 0; i < 5; ++i) {
        const float hk = __shfl(hval, 5 * r4 + i);
#pragma unroll
        for (int d = 0; d < 4; ++d) { acc[2 * d] = fmaf(hk, bflo(wqv[i][d]), acc[2 * d]); acc[2 * d + 1] = fmaf(hk, bfhi(wqv[i][d]), acc[2 * d + 1]); }
      }
      float v[4];
#pragma unroll
      for (int k = 0; k < 4; ++k) v[k] = tr16(acc[k], acc[k + 4]);
      LAS float* QR = (LAS float*)(L + L_QR);
      *(LAS f32x2*)(QR + w * 128 + c16 * 8 + (r4 & 1) * 4 + (r4 >> 1) * 2) = f32x2{tr32(v[0], v[2]), tr32(v[1], v[3])};
      if (lds_arrive(C_QP) == 3u) {
        if (lane < 32) st_sc1(rs, WS_QP + u * 512 + lane * 16, nosent4(sum4(QR + 4 * lane, 128)));
      }
    }
    if (w == 1) {
      wait_until(tstart + T3);
      NOUNROLL for (unsigned s = 0; s < SPIN_G; ++s) {
        MEMBAR();
        const u32x4 g0 = ld_sc1(rs, WS_FLAGS + lane * 16), g1 = ld_sc1(rs, WS_FLAGS + 1024 + lane * 16);
        const unsigned bad = ((g0[0] ^ flo) | (g0[1] ^ tag)) | ((g0[2] ^ flo) | (g0[3] ^ tag)) | ((g1[0] ^ flo) | (g1[1] ^ tag)) | ((g1[2] ^ flo) | (g1[3] ^ tag));
        if (__all(bad == 0u)) break;
        __builtin_amdgcn_s_sleep(16);
      }
      lds_put(L, C_FLAGOK, 1u);
      lds_wait(L, C_PROG, 1u);
      if (lane == 0) {
        const unsigned tk = __hip_atomic_fetch_add((GAS unsigned*)(ws + WS_TICKET + j * 128), 1u, __ATOMIC_RELAXED, __HIP_MEMORY_SCOPE_AGENT);
        lds_put(L, C_ROLE, tk * 8u + (unsigned)j + 1u);
      }
    } else if (w == 0) {
      u32x4 a0, a1, b0, b1;
      const int off = WS_QP + (h * 8 + 2 * r4) * 512 + c16 * 32;
      lds_wait(L, C_FLAGOK, 1u);
      wait_until(tstart + T2);
      NOUNROLL for (unsigned s = 0; s < SPIN_G; ++s) {
        MEMBAR();
        a0 = ld_sc1(rs, off); a1 = ld_sc1(rs, off + 16); b0 = ld_sc1(rs, off + 512); b1 = ld_sc1(rs, off + 528);
        if (__all(max(max(mx4(a0), mx4(a1)), max(mx4(b0), mx4(b1))) != SENT)) break;
        __builtin_amdgcn_s_sleep(16);
      }
      float v[4];
#pragma unroll
      for (int k = 0; k < 4; ++k) v[k] = tr16(bf(a0[k]) + bf(b0[k]), bf(a1[k]) + bf(b1[k]));
      ((LAS unsigned*)(L + L_Q))[c16 * 4 + (r4 & 1) * 2 + (r4 >> 1)] = pack2(tr32(v[0], v[2]), tr32(v[1], v[3]));
      MEMBAR();
      lds_put(L, C_QREADY, 1u);
    }
    lds_wait(L, C_ROLE, 1u);
    const int role = __builtin_amdgcn_readfirstlane(((volatile LAS unsigned*)L)[C_ROLE]) - 1;
    __builtin_amdgcn_s_setprio(0);
    LAS float* YR = (LAS float*)(L + L_YR);
    if (role >= R_M2 && role < R_M2 + N_M2) {
      const int m2 = role - R_M2;
      const int row0 = 160 * w + 20 * r8;
      u32x2 cw[5];
      u32x4 wt[20];
#pragma unroll
      for (int i = 0; i < 5; ++i) cw[i] = __builtin_nontemporal_load((const u32x2*)(cnd + row0 + 4 * i));
      const u16* wb = wmod + (size_t)row0 * (6 * D) + 1280 + m2 * 64 + c8 * 8;
#pragma unroll
      for (int i = 0; i < 20; ++i) wt[i] = ldnt(wb + (size_t)i * (6 * D));
      SB();
      float acc[8] = {0.f, 0.f, 0.f, 0.f, 0.f, 0.f, 0.f, 0.f};
#pragma unroll
      for (int m = 0; m < 10; ++m) mac_pair(acc, wt[2 * m], wt[2 * m + 1], silu2(cw[m >> 1][m & 1]));
      YR[w * 64 + col64] = red8_b345(acc, lane);
      if (lds_arrive(C_T1) == 3u && lane < 16) {
        const f32x4 s = sum4(YR + 4 * lane, 64);
        st_sc1(rs, WS_M2 + (m2 * 64 + 4 * lane) * 4, nosent4(f32x4{rbf(s[0]), rbf(s[1]), rbf(s[2]), rbf(s[3])}));
      }
    } else if (role >= R_WO && role < R_WO + N_WO) {
      const int g = (role - R_WO) & 7, cgp = (role - R_WO) >> 3, hw = 4 * g + w;
      u32x4 wt[16];
      unsigned cpf;
      CODE_PREFETCH(mega_wo, cpf);
      const u16* wb = wo + (size_t)(hw * 128 + 16 * r8) * D + cgp * 64 + c8 * 8;
#pragma unroll
      for (int i = 0; i < WOE; ++i) wt[i] = ldnt(wb + i * D);
      SB();
      lds_wait(L, C_PV, 9u);
#pragma unroll
      for (int i = WOE; i < 16; ++i) wt[i] = ldnt(wb + i * D);
      u32x4 a[4]; u32x2 ml;
      const int off = WS_OP + (hw * 8 + 4 * (lane >> 5)) * 512 + (lane & 31) * 16;
      const int mo = WS_ST + (hw * 8 + c8) * 128;
      ml = ld_sc1_64(rs, mo);
      SB();
#pragma unroll
      for (int m = 0; m < 8; ++m) prepack_now(wt[2 * m], wt[2 * m + 1]);
      float acc[8] = {0.f, 0.f, 0.f, 0.f, 0.f, 0.f, 0.f, 0.f};
      PIN8(acc);
      asm volatile("" :: "v"(cpf));
      float wj[4];
      NOUNROLL for (unsigned s = 0; s < SPIN_G; ++s) {
        MEMBAR();
#pragma unroll
        for (int i = 0; i < 4; ++i) a[i] = ld_sc1(rs, off + i * 512);
        const bool mok = __all(max(ml[0], ml[1]) != SENT);
        {
          float M = bf(ml[0]);
          M = fmaxf(M, dpp<XOR1>(M)); M = fmaxf(M, dpp<XOR2>(M)); M = fmaxf(M, dpp<XOR7>(M));
          const float e = __expf(bf(ml[0]) - M);
          float Ls = bf(ml[1]) * e;
          Ls += dpp<XOR1>(Ls); Ls += dpp<XOR2>(Ls); Ls += dpp<XOR7>(Ls);
          const float wgt = e * __builtin_amdgcn_rcpf(Ls);
#pragma unroll
          for (int i = 0; i < 4; ++i) {
            const float lo = bf(__builtin_amdgcn_readlane(fb(wgt), i)), hi = bf(__builtin_amdgcn_readlane(fb(wgt), 4 + i));
            wj[i] = lane < 32 ? lo : hi;
          }
          asm volatile("" : "+v"(wj[0]), "+v"(wj[1]), "+v"(wj[2]), "+v"(wj[3]));
        }
        if (!mok) ml = ld_sc1_64(rs, mo);
        if (mok && __all(max(max(mx4(a[0]), mx4(a[1])), max(mx4(a[2]), mx4(a[3]))) != SENT)) break;
      }
      float t4[4];
#pragma unroll
      for (int k = 0; k < 4; ++k) t4[k] = fmaf(wj[0], bf(a[0][k]), wj[1] * bf(a[1][k])) + fmaf(wj[2], bf(a[2][k]), wj[3] * bf(a[3][k]));
      LAS unsigned* OB = (LAS unsigned*)(L + L_OB) + w * 64;
      OB[2 * (lane & 31) + (lane >> 5)] = pack2(tr32(t4[0], t4[2]), tr32(t4[1], t4[3]));
      MEMBAR();
      const u32x4 oa = *(volatile LAS u32x4*)(OB + 8 * r8), ob = *(volatile LAS u32x4*)(OB + 8 * r8 + 4);
#pragma unroll
      for (int m = 0; m < 4; ++m) mac_packed(acc, wt[2 * m], wt[2 * m + 1], oa[m]);
#pragma unroll
      for (int m = 0; m < 4; ++m) mac_packed(acc, wt[8 + 2 * m], wt[8 + 2 * m + 1], ob[m]);
      YR[w * 64 + col64] = red8_b345(acc, lane);
      HBAR();
      if (w == 0 && lane < 16) st_sc1(rs, WS_YP + (g * 640 + cgp * 64 + 4 * lane) * 4, nosent4(sum4(YR + 4 * lane, 64)));
      CODE_LABEL(mega_wo_end);
    } else if (role >= R_FFN && role < R_FFN + NF) {
      const int f = role - R_FFN;
      const int r16 = lane >> 2, c4 = lane & 3;
      const int row0 = 160 * w + 10 * r16;
      const bool fin = f < 20 && w == 3;
      const int oc = (f < 20 ? 32 * f : 0) + 4 * c8 + ((lane >> 4) & 1) * 2 + (lane >> 5);
      LAS unsigned* X1 = (LAS unsigned*)(L + L_X1);
      u32x4 wt[18]; unsigned w2t[8];
      unsigned cpf, cpg;
      CODE_PREFETCH(mega_ffn, cpf);
      {
        u64 pc_; int d_, len_;
        asm volatile("s_getpc_b64 %0\n.Lmega_gat_pc:\n\ts_mov_b32 %1, .Lmega_gat_beg-.Lmega_gat_pc\n\ts_mov_b32 %2, .Lmega_gat_end-.Lmega_gat_beg"
                     : "=s"(pc_), "=s"(d_), "=s"(len_));
        cpg = __builtin_amdgcn_raw_buffer_load_b32(mkrs((const void*)(pc_ + (long long)d_), w == 3 ? min(max(len_, 0), 4096) : 0), lane * 64, 0, 0);
      }
      const u16* wb = w1 + (size_t)row0 * DFF + f * 32 + c4 * 8;
      const u16* w2b = w2 + (size_t)(f * 32 + 8 * r4) * D + 160 * w;
#pragma unroll
      for (int i = 0; i < 18; ++i) {
        if (i == FFE) { SB(); lds_wait(L, C_PV, 9u); }
        if (i < 10) wt[i] = ldnt(wb + (size_t)i * DFF);
        else {
          wt[i] = ldnt(w2b + (i - 10) * D + c16 * 8);
          w2t[i - 10] = __builtin_nontemporal_load((const unsigned*)(w2b + (i - 10) * D + 128 + c16 * 2));
        }
      }
      SB();
      lds_arrive_nr(C_WOUT);
      u32x2 sbA, sbB, cbA, cbB; unsigned gbw;
#define M2_LOOK() do { \
        gbw = ld_sc1_32(rs, WS_M2 + 7680 + oc * 4); \
        sbA = ld_sc1_64(rs, WS_M2 + 2560 + (row0 + 2 * c4) * 4); sbB = ld_sc1_64(rs, WS_M2 + 2560 + (row0 + 8) * 4); \
        cbA = ld_sc1_64(rs, WS_M2 + 5120 + (row0 + 2 * c4) * 4); cbB = ld_sc1_64(rs, WS_M2 + 5120 + (row0 + 8) * 4); } while (0)
      M2_LOOK();
      SB();
      asm volatile("" :: "v"(cpf), "v"(cpg));
#pragma unroll
      for (int m = 0; m < 9; ++m) prepack_now(wt[2 * m], wt[2 * m + 1]);
#pragma unroll
      for (int m = 0; m < 4; ++m) {
        const unsigned lo = __builtin_amdgcn_perm(w2t[2 * m + 1], w2t[2 * m], 0x05040100u), hi = __builtin_amdgcn_perm(w2t[2 * m + 1], w2t[2 * m], 0x07060302u);
        w2t[2 * m] = lo; w2t[2 * m + 1] = hi;
      }
      PIN8(w2t);
      NOUNROLL for (unsigned s = 0; s < SPIN_G; ++s) {
        const unsigned mm = max(gbw, max(max(max(sbA[0], sbA[1]), max(sbB[0], sbB[1])), max(max(cbA[0], cbA[1]), max(cbB[0], cbB[1]))));
        if (__all(mm != SENT)) break;
        __builtin_amdgcn_s_sleep(2);
        MEMBAR();
        M2_LOOK();
      }
#undef M2_LOOK
      float c1[4] = {rbf(1.f + bf(cbA[0])), rbf(1.f + bf(cbA[1])), rbf(1.f + bf(cbB[0])), rbf(1.f + bf(cbB[1]))};
      asm volatile("" : "+v"(c1[0]), "+v"(c1[1]), "+v"(c1[2]), "+v"(c1[3]));
      const float s1[4] = {bf(sbA[0]), bf(sbA[1]), bf(sbB[0]), bf(sbB[1])};
      float acc[8] = {0.f, 0.f, 0.f, 0.f, 0.f, 0.f, 0.f, 0.f};
      PIN8(acc);
      float z[8] = {0.f, 0.f, 0.f, 0.f, 0.f, 0.f, 0.f, 0.f};
      float z8 = 0.f, z9 = 0.f;
      PIN8(z);
      asm volatile("" : "+v"(z8), "+v"(z9));
      HBAR();
      unsigned hp5[5];
      {
        const u32x4 ya = *(LAS u32x4*)(X1 + 4 * lane);
        const unsigned yb = X1[256 + lane];
        const unsigned hA = X1[(row0 >> 1) + c4], hB = X1[(row0 >> 1) + 4];
        float sa_ = dot2(yb, 0x3F803F80u, 0.f), sq_ = dot2(yb, yb, 0.f);
#pragma unroll
        for (int k = 0; k < 4; ++k) { sa_ = dot2(ya[k], 0x3F803F80u, sa_); sq_ = dot2(ya[k], ya[k], sq_); }
        float r = tr32(sa_, sq_);
        r = tr16(r, r);
        r = row_sum(r);
        const float mean = bf(__builtin_amdgcn_readlane(fb(r), 0)) * (1.f / 640.f);
        float var = fmaf(-mean, mean, bf(__builtin_amdgcn_readlane(fb(r), 32)) * (1.f / 640.f));
        if (!(var * 1024.f > mean * mean)) {
          float q2 = 0.f;
#pragma unroll
          for (int k = 0; k < 4; ++k) { const float d0 = bflo(ya[k]) - mean, d1 = bfhi(ya[k]) - mean; q2 += d0 * d0 + d1 * d1; }
          const float d0 = bflo(yb) - mean, d1 = bfhi(yb) - mean; q2 += d0 * d0 + d1 * d1;
          var = wave_sum(q2) * (1.f / 640.f);
        }
        const float mr = rbf(mean);
        const float rden = __builtin_amdgcn_rcpf(rbf(__builtin_amdgcn_sqrtf(rbf(rbf(var) + 1e-5f))));
        const float xr2[4] = {bflo(hA), bfhi(hA), bflo(hB), bfhi(hB)};
        float hv[4];
#pragma unroll
        for (int k = 0; k < 4; ++k) hv[k] = rbf(rbf(rbf(xr2[k] - mr) * rden) * c1[k]) + s1[k];
        const unsigned pA = pack2(hv[0], hv[1]);
        hp5[4] = pack2(hv[2], hv[3]);
        hp5[0] = dppu<0x00>(pA); hp5[1] = dppu<0x55>(pA); hp5[2] = dppu<0xAA>(pA); hp5[3] = dppu<0xFF>(pA);
      }
      {
#pragma unroll
        for (int m = 0; m < 5; ++m) mac_packed(acc, wt[2 * m], wt[2 * m + 1], hp5[m]);
        float y = red8_b345(acc, lane);
        y += xor4(y);
        YR[w * 64 + c4 * 8 + ((lane >> 3) & 1) * 4 + ((lane >> 4) & 1) * 2 + (lane >> 5)] = y;
        HBAR();
        const LAS float* yp = YR + 8 * r4 + c8;
        float hd = (yp[0] + yp[64]) + (yp[128] + yp[192]);
        hd = hd < 0.f ? 0.f : hd;
        const unsigned hpk = pack2(hd, dpp<XOR1>(hd));
        const unsigned hp[4] = {dppu<0x150>(hpk), dppu<0x152>(hpk), dppu<0x154>(hpk), dppu<0x156>(hpk)};
#pragma unroll
        for (int m = 0; m < 4; ++m) {
          mac_packed(z, wt[10 + 2 * m], wt[10 + 2 * m + 1], hp[m]);
          z8 = dot2(w2t[2 * m], hp[m], z8);
          z9 = dot2(w2t[2 * m + 1], hp[m], z9);
        }
        float v[4];
#pragma unroll
        for (int k = 0; k < 4; ++k) v[k] = tr16(z[k], z[k + 4]);
        float e = tr16(z8, z9);
        const int zo = WS_ZP + f * 2560 + w * 640;
        st_sc1_64(rs, zo + (c16 * 8 + (r4 & 1) * 4 + (r4 >> 1) * 2) * 4, u32x2{nosent(tr32(v[0], v[2])), nosent(tr32(v[1], v[3]))});
        e = tr32(e, e);
        st_sc1_32(rs, zo + 512 + (c16 * 2 + (r4 & 1)) * 4, nosent(e));
      }
      if (fin) {
        u32x4 zp[7];
        const int zo = WS_ZP + r8 * 2560 + f * 128 + c8 * 16;
        const int z6 = r8 < NF - 48 ? zo + 48 * 2560 : OOB;
        const float xo = bflo((unsigned)((LAS u16*)X1)[oc]);
        NOUNROLL for (unsigned s = 0; s < SPIN_G; ++s) {
          MEMBAR();
#pragma unroll
          for (int i = 0; i < 6; ++i) zp[i] = ld_sc1(rs, zo, i * 8 * 2560);
          zp[6] = ld_sc1(rs, z6);
          unsigned mm = mx4(zp[0]);
#pragma unroll
          for (int i = 1; i < 7; ++i) mm = max(mm, mx4(zp[i]));
          if (__all(mm != SENT)) break;
        }
        float r[4];
#pragma unroll
        for (int k = 0; k < 4; ++k) r[k] = ((bf(zp[0][k]) + bf(zp[1][k])) + (bf(zp[2][k]) + bf(zp[3][k]))) + ((bf(zp[4][k]) + bf(zp[5][k])) + bf(zp[6][k]));
        float v = tr32(tr16(r[0], r[2]), tr16(r[1], r[3]));
        v += dpp<XOR8>(v);
        out[oc] = (u16)f2bf(xo + rbf(bf(gbw) * rbf(v)));
        if (f == 0 && lane == 0) {
          const u64 e1 = (((u64)Ev[1] << 32) | (u64)Ev[0]) + 1ull;
          st_sc1_64(rs, WS_EPOCH, u32x2{(unsigned)e1, (unsigned)(e1 >> 32)});
        }
        CODE_LABEL(mega_ffn_end);
      }
    }
  }
}

extern "C" void kernel_launch(void* const* d_in, const int* in_sizes, int n_in, void* d_out, int out_size, void* d_ws,
                              size_t ws_size, hipStream_t stream) {
  (void)in_sizes; (void)n_in; (void)out_size; (void)ws_size;
  (void)hipFuncSetAttribute((const void*)mega, hipFuncAttributeMaxDynamicSharedMemorySize, LDS_BYTES);
  hipLaunchKernelGGL(mega, dim3(256), dim3(768), LDS_BYTES, stream, (const u16*)d_in[1], (const u16*)d_in[2], (unsigned char*)d_ws,
                     (const u16*)d_in[9], (const u16*)d_in[8], (const u16*)d_in[0], (const u16*)d_in[3], (const u16*)d_in[4],
                     (const u16*)d_in[6], (const u16*)d_in[7], (u16*)d_out);
}
```
